```python
import torch

def _softmax(x, axis=-1):
    return torch.softmax(x, dim=axis)
def _gelu(x):
    return torch.nn.functional.gelu(x, approximate="tanh")
def _silu(x):
    return torch.nn.functional.silu(x)
def _sigmoid(x):
    return torch.sigmoid(x)
def _layernorm(x, axis=-1, eps=1e-5):
    m = x.mean(dim=axis, keepdim=True)
    v = x.var(dim=axis, keepdim=True, unbiased=False)
    return (x - m) / torch.sqrt(v + eps)
def _scatter_add(tgt, idx, src, axis=0):
    return tgt.clone().index_add_(axis, idx, src)
def _segment_sum(src, seg, n):
    out = torch.zeros((n,) + src.shape[1:], dtype=src.dtype)
    return out.index_add_(0, seg, src)
def _pad(x, pw):
    flat = tuple(v for pair in reversed(pw) for v in pair)
    return torch.nn.functional.pad(x, flat)

kernel_name = 'recurrent_scan__ssm_conv_s4__mixer_channel_mlp__prompt'

def setup_inputs():

    _w = (lambda a: (lambda t: t.to(torch.bfloat16) if t.is_floating_point() else t)(torch.as_tensor(a)))
    def _A_setup():
        B, L, D, N = 1, 2048, 512, 128
        A_re = -(lambda _x: torch.exp(torch.as_tensor(_x)))((lambda *s: torch.randn(*s, dtype=torch.bfloat16) * 3.2)(D, N))
        A_im = torch.pi * (lambda *s: torch.randn(*s, dtype=torch.bfloat16) * 0.05)(D, N)
        B_re = (lambda *s: torch.randn(*s, dtype=torch.bfloat16) * 0.1)(D, N)
        B_im = (lambda *s: torch.randn(*s, dtype=torch.bfloat16) * 0.1)(D, N)
        C_re = (lambda *s: torch.randn(*s, dtype=torch.bfloat16) * 0.1)(D, N)
        C_im = (lambda *s: torch.randn(*s, dtype=torch.bfloat16) * 0.2)(D, N)
        log_dt = (lambda *s: torch.randn(*s, dtype=torch.bfloat16) * 0.05)(D) * 0.5 - 4.0
        Dskip = (lambda *s: torch.randn(*s, dtype=torch.bfloat16) * 0.05)(D)
        return {
            "u": _w((lambda *s: torch.randn(*s, dtype=torch.bfloat16) * 0.05)(B, L, D)),
            "A_re": _w(A_re),
            "A_im": _w(A_im),
            "B_re": _w(B_re),
            "B_im": _w(B_im),
            "C_re": _w(C_re),
            "C_im": _w(C_im),
            "log_dt": _w(log_dt),
            "Dskip": _w(Dskip),
        }
    _A_in = _A_setup()

    def _B_setup():
        B, S, D, F = 1, 2048, 512, 1792
        return {
            "x": _w((lambda *s: torch.randn(*s, dtype=torch.bfloat16) * 0.05)(B, S, D)),
            "w1": _w((lambda *s: torch.randn(*s, dtype=torch.bfloat16) * 1.6)(D, F) * (D ** -0.5)),
            "b1": _w((lambda *s: torch.randn(*s, dtype=torch.bfloat16) * 0.8)(F)),
            "w2": _w((lambda *s: torch.randn(*s, dtype=torch.bfloat16) * 0.05)(F, D) * (F ** -0.5)),
            "b2": _w((lambda *s: torch.randn(*s, dtype=torch.bfloat16) * 0.05)(D)),
        }
    _B_in = _B_setup()

    return {
        "A_u": _A_in["u"],
        "A_A_re": _A_in["A_re"],
        "A_A_im": _A_in["A_im"],
        "A_B_re": _A_in["B_re"],
        "A_B_im": _A_in["B_im"],
        "A_C_re": _A_in["C_re"],
        "A_C_im": _A_in["C_im"],
        "A_log_dt": _A_in["log_dt"],
        "A_Dskip": _A_in["Dskip"],
        "B_x": _B_in["x"],
        "B_w1": _B_in["w1"],
        "B_b1": _B_in["b1"],
        "B_w2": _B_in["w2"],
        "B_b2": _B_in["b2"],
    }

def reference(A_u, A_A_re, A_A_im, A_B_re, A_B_im, A_C_re, A_C_im, A_log_dt, A_Dskip, B_x, B_w1, B_b1, B_w2, B_b2):
    def _A_ref(u, A_re, A_im, B_re, B_im, C_re, C_im, log_dt, Dskip):
        Bsz, L, D = u.shape
        dt = torch.exp(log_dt)[:, None]
        A = A_re + 1j * A_im
        Bc = B_re + 1j * B_im
        Cc = C_re + 1j * C_im
        logdA = dt * A
        dB = dt * Bc
        l = (lambda x, dt: x.to(dt))(torch.arange(L), A_re.dtype)
        P = torch.exp(logdA[:, :, None] * l[None, None, :])
        K = torch.einsum("dn,dnl,dn->dl", Cc, P, dB).real
        L2 = 2 * L
        Kp = _pad(K, ((0, 0), (0, L)))
        up = (lambda x, *d: x.permute(*d))(u, 0, 2, 1)
        up = _pad(up, ((0, 0), (0, 0), (0, L)))
        Kf = (lambda x, axes=(-2,-1): torch.fft.rfft2(x.float(), dim=axes))(Kp[:, None, :], axes=(-2, -1))
        Uf = (lambda x, axes=(-2,-1): torch.fft.rfft2(x.float(), dim=axes))(up[:, :, None, :], axes=(-2, -1))
        Yf = Uf * Kf[None, :, :, :]
        y = (lambda x, s, axes=(-2,-1): torch.fft.irfft2(x, s=s, dim=axes).to(torch.bfloat16))(Yf, (1, L2), axes=(-2, -1))[:, :, 0, :L]
        y = (lambda x, *d: x.permute(*d))(y, 0, 2, 1) + u * Dskip[None, None, :]
        return y

    def _B_ref(x, w1, b1, w2, b2):
        y = _layernorm(x, axis=-1)
        h = _gelu(torch.einsum("bsd,df->bsf", y, w1) + b1)
        return torch.einsum("bsf,fd->bsd", h, w2) + b2

    x = A_u
    xn = _layernorm(x, axis=-1)
    s = _A_ref(xn, A_A_re, A_A_im, A_B_re, A_B_im, A_C_re, A_C_im, A_log_dt, A_Dskip)
    g = _silu(_B_ref(xn, B_w1, B_b1, B_w2, B_b2))
    return x + s * g
```

```cpp
#include <hip/hip_runtime.h>
#include <hip/hip_bf16.h>
#include <stdint.h>

#define L_ 2048
#define D_ 512
#define N_ 128
#define F_ 1792

typedef unsigned short u16;
__device__ __forceinline__ float bf2f(u16 v) { return __uint_as_float(((unsigned)v) << 16); }
__device__ __forceinline__ u16 f2bf(float f) { __hip_bfloat16 b = __float2bfloat16(f); return *(u16*)&b; }
__device__ __forceinline__ float rbf(float f) { return bf2f(f2bf(f)); }

__global__ void ln_kernel(const u16* x, u16* xn, u16* y2) {
    int row = blockIdx.x; int lane = threadIdx.x;
    float v[8]; float s = 0;
    for (int j = 0; j < 8; j++) { v[j] = bf2f(x[row * D_ + lane * 8 + j]); s += v[j]; }
    for (int o = 32; o; o >>= 1) s += __shfl_xor(s, o);
    float m = s / D_; float q = 0;
    for (int j = 0; j < 8; j++) { float d = v[j] - m; q += d * d; }
    for (int o = 32; o; o >>= 1) q += __shfl_xor(q, o);
    float r = rsqrtf(q / D_ + 1e-5f);
    s = 0;
    for (int j = 0; j < 8; j++) { v[j] = rbf((v[j] - m) * r); xn[row * D_ + lane * 8 + j] = f2bf(v[j]); s += v[j]; }
    for (int o = 32; o; o >>= 1) s += __shfl_xor(s, o);
    m = s / D_; q = 0;
    for (int j = 0; j < 8; j++) { float d = v[j] - m; q += d * d; }
    for (int o = 32; o; o >>= 1) q += __shfl_xor(q, o);
    r = rsqrtf(q / D_ + 1e-5f);
    for (int j = 0; j < 8; j++) y2[row * D_ + lane * 8 + j] = f2bf((v[j] - m) * r);
}

__global__ void kgen_kernel(const u16* Are, const u16* Aim, const u16* Bre, const u16* Bim, const u16* Cre, const u16* Cim,
                            const u16* logdt, float* K) {
    int d = blockIdx.x; int l = blockIdx.y * 256 + threadIdx.x;
    float dt = rbf(expf(bf2f(logdt[d])));
    float lf = rbf((float)l);
    float acc = 0;
    for (int n = 0; n < N_; n++) {
        int i = d * N_ + n;
        float a = dt * bf2f(Are[i]), b = dt * bf2f(Aim[i]);
        float br = dt * bf2f(Bre[i]), bi = dt * bf2f(Bim[i]);
        float cr = bf2f(Cre[i]), ci = bf2f(Cim[i]);
        float wr = cr * br - ci * bi, wi = cr * bi + ci * br;
        float e = expf(a * lf); float sn, cs; sincosf(b * lf, &sn, &cs);
        acc += e * (wr * cs - wi * sn);
    }
    K[d * L_ + l] = acc;
}

__global__ void conv_kernel(const float* K, const u16* xn, const u16* Dskip, float* s) {
    int d = blockIdx.x * 64 + (threadIdx.x & 63); int t = blockIdx.y * 4 + (threadIdx.x >> 6);
    float acc = 0;
    for (int j = 0; j <= t; j++) acc += K[d * L_ + (t - j)] * bf2f(xn[j * D_ + d]);
    s[t * D_ + d] = rbf(acc) + rbf(bf2f(xn[t * D_ + d]) * bf2f(Dskip[d]));
}

__global__ void gemm1_kernel(const u16* y2, const u16* w1, const u16* b1, u16* h) {
    int f = blockIdx.x * 256 + threadIdx.x; int t = blockIdx.y;
    float acc = 0;
    for (int k = 0; k < D_; k++) acc += bf2f(y2[t * D_ + k]) * bf2f(w1[k * F_ + f]);
    float v = acc + bf2f(b1[f]);
    float g = 0.5f * v * (1.f + tanhf(0.7978845608028654f * (v + 0.044715f * v * v * v)));
    h[t * F_ + f] = f2bf(g);
}

__global__ void gemm2_kernel(const u16* h, const u16* w2, const u16* b2, const u16* x, const float* s, u16* out) {
    int d = blockIdx.x * 256 + threadIdx.x; int t = blockIdx.y;
    float acc = 0;
    for (int k = 0; k < F_; k++) acc += bf2f(h[t * F_ + k]) * bf2f(w2[k * D_ + d]);
    float v = acc + bf2f(b2[d]);
    float g = v / (1.f + expf(-v));
    out[t * D_ + d] = f2bf(bf2f(x[t * D_ + d]) + s[t * D_ + d] * g);
}

extern "C" void kernel_launch(void* const* d_in, const int* in_sizes, int n_in,
                              void* d_out, int out_size, void* d_ws, size_t ws_size,
                              hipStream_t stream) {
    const u16* x = (const u16*)d_in[0];
    char* ws = (char*)d_ws;
    u16* xn = (u16*)ws; u16* y2 = (u16*)(ws + (2 << 20)); float* K = (float*)(ws + (4 << 20));
    float* s = (float*)(ws + (8 << 20)); u16* h = (u16*)(ws + (12 << 20));
    ln_kernel<<<L_, 64, 0, stream>>>(x, xn, y2);
    kgen_kernel<<<dim3(D_, L_ / 256), 256, 0, stream>>>((const u16*)d_in[1], (const u16*)d_in[2], (const u16*)d_in[3], (const u16*)d_in[4],
        (const u16*)d_in[5], (const u16*)d_in[6], (const u16*)d_in[7], K);
    conv_kernel<<<dim3(D_ / 64, L_ / 4), 256, 0, stream>>>(K, xn, (const u16*)d_in[8], s);
    gemm1_kernel<<<dim3(F_ / 256, L_), 256, 0, stream>>>(y2, (const u16*)d_in[10], (const u16*)d_in[11], h);
    gemm2_kernel<<<dim3(D_ / 256, L_), 256, 0, stream>>>(h, (const u16*)d_in[12], (const u16*)d_in[13], x, s, (u16*)d_out);
}
```

```cpp
#include <hip/hip_runtime.h>
#include <stdint.h>

#define SPLIT 0

typedef unsigned short u16;
typedef unsigned u32;
typedef unsigned long long u64;
typedef u32 u32x2 __attribute__((ext_vector_type(2)));
typedef u32 u32x4 __attribute__((ext_vector_type(4)));
typedef short s16x4 __attribute__((ext_vector_type(4)));
typedef __bf16 bf16x2 __attribute__((ext_vector_type(2)));
typedef __bf16 bf16x8 __attribute__((ext_vector_type(8)));
typedef _Float16 f16x2 __attribute__((ext_vector_type(2)));
typedef _Float16 f16x4 __attribute__((ext_vector_type(4)));
typedef _Float16 f16x8 __attribute__((ext_vector_type(8)));
typedef float f32x2 __attribute__((ext_vector_type(2)));
typedef float f32x4 __attribute__((ext_vector_type(4)));
typedef float f32x16 __attribute__((ext_vector_type(16)));
#define LAS __attribute__((address_space(3)))
#define GAS __attribute__((address_space(1)))
#define DEV __device__ __forceinline__

constexpr size_t WS_H = 0;
constexpr size_t WS_U = 8u << 20;
constexpr size_t WS_S = 10u << 20;
constexpr size_t WS_FLAG = 12u << 20;
constexpr size_t WS_KG = 13u << 20;
constexpr size_t WS_STAMP = 20u << 20;
constexpr int FL_LN = 0, FL_H = 32, FL_S = 288, FL_E = 576;

constexpr int L_W2 = 0, W2_REG = 28672;
constexpr int L_UC = 114688, UC_COPY = 4160, UC_CH = 4 * UC_COPY;
constexpr int L_G = 131072;
constexpr int L_PAR = 147456;
constexpr int L_XCH = 147968;
constexpr int L_MISC = 152064;
constexpr int L_KP = 153600, KP_PLANE = 1280, KP_CH = 5120;

struct Params {
    const u16 *x, *Are, *Aim, *Bre, *Bim, *Cre, *Cim, *logdt, *Dskip, *w1, *b1, *w2, *b2;
    u16* out;
    char* ws;
};

DEV float bf2f(u32 v) { return __uint_as_float(v << 16); }
DEV float bflo(u32 v) { return __uint_as_float(v << 16); }
DEV float bfhi(u32 v) { return __uint_as_float(v & 0xffff0000u); }
DEV u32 pkbf(float a, float b) { f32x2 f = {a, b}; return __builtin_bit_cast(u32, __builtin_convertvector(f, bf16x2)); }
DEV u32 pkh(float a, float b) { f32x2 f = {a, b}; return __builtin_bit_cast(u32, __builtin_convertvector(f, f16x2)); }
DEV float rbf(float a) { return bflo(pkbf(a, a)); }
DEV float dot2(u32 a, u32 b, float c) { return __builtin_amdgcn_fdot2_f32_bf16(__builtin_bit_cast(bf16x2, a), __builtin_bit_cast(bf16x2, b), c, false); }

#define WAITV(n) asm volatile("s_waitcnt vmcnt(" #n ")" ::: "memory")
#define WAITL0() asm volatile("s_waitcnt lgkmcnt(0)" ::: "memory")
#define BAR() do { asm volatile("s_waitcnt lgkmcnt(0)" ::: "memory"); __builtin_amdgcn_s_barrier(); asm volatile("" ::: "memory"); } while (0)
#define SCHED() __builtin_amdgcn_sched_barrier(0)

DEV const void* uni(const void* q) {
    const u64 a = (u64)q;
    return (const void*)(((u64)(u32)__builtin_amdgcn_readfirstlane((u32)(a >> 32)) << 32) | (u32)__builtin_amdgcn_readfirstlane((u32)a));
}
DEV void glds(const void* base, u32 voff, u32 dst) {
    u32 keep;
    dst = __builtin_amdgcn_readfirstlane(dst);
    base = uni(base);
    asm volatile("s_mov_b32 %0, m0\n\ts_mov_b32 m0, %3\n\ts_nop 2\n\tglobal_load_lds_dwordx4 %1, %2\n\ts_mov_b32 m0, %0"
                 : "=&s"(keep) : "v"(voff), "s"(base), "s"(dst) : "memory");
}
DEV u32 aload_u16(const void* base, u32 voff) { u32 r; asm volatile("global_load_ushort %0, %1, %2" : "=v"(r) : "v"(voff), "s"(base) : "memory"); return r; }
DEV u32x2 aload_b64_sc1(const void* base, u32 voff) { u32x2 r; asm volatile("global_load_dwordx2 %0, %1, %2 sc1" : "=v"(r) : "v"(voff), "s"(base) : "memory"); return r; }
DEV u32x4 aload_b128_sc1(const void* base, u32 voff) { u32x4 r; asm volatile("global_load_dwordx4 %0, %1, %2 sc1" : "=v"(r) : "v"(voff), "s"(base) : "memory"); return r; }
DEV u32x4 aload_b128(const void* base, u32 voff) { u32x4 r; asm volatile("global_load_dwordx4 %0, %1, %2" : "=v"(r) : "v"(voff), "s"(base) : "memory"); return r; }
DEV void astore_b128_sc1(void* base, u32 voff, u32x4 v) { asm volatile("global_store_dwordx4 %0, %1, %2 sc1\n\ts_nop 1" :: "v"(voff), "v"(v), "s"(base) : "memory"); }
DEV void astore_b64_sc1(void* base, u32 voff, u32x2 v) { asm volatile("global_store_dwordx2 %0, %1, %2 sc1\n\ts_nop 1" :: "v"(voff), "v"(v), "s"(base) : "memory"); }

#define ST(i)

DEV void poll_flags(const char* flags, int first, int stride, int nfl, u32x2 tag, int lane) {
    const u32 off = (u32)(first + (lane < nfl ? lane : 0) * stride) * 128u;
    for (int spin = 0; spin < (1 << 16); ++spin) {
        u32x2 v = aload_b64_sc1(flags, off);
        asm volatile("s_waitcnt vmcnt(0)" : "+v"(v) :: "memory");
        if (__all(v.x == tag.x && v.y == tag.y)) break;
        __builtin_amdgcn_s_sleep(1);
    }
}

DEV float wave_max(float v) {
#pragma unroll
    for (int o = 16; o; o >>= 1) v = fmaxf(v, __shfl_xor(v, o));
    return v;
}

DEV float kgen_params(LAS char* sm, int wid, int lane, u32 are, u32 aim, u32 bre, u32 bim, u32 cre, u32 cim, u32 ldt) {
    const float dt = rbf(__expf(bf2f(ldt)));
    const float a2 = dt * bf2f(are) * 1.4426950408889634f;
    const float rev = dt * bf2f(aim) * 0.15915494309189535f;
    const float br = bf2f(bre), bi = bf2f(bim), cr = bf2f(cre), ci = bf2f(cim);
    const float wr = cr * br - ci * bi, wi = cr * bi + ci * br;
    const float lw = 0.5f * (__log2f(cr * cr + ci * ci) + __log2f(br * br + bi * bi)) + __log2f(dt);
    const float pw = atan2f(wi, wr) * 0.15915494309189535f;
    float M = wave_max(lw + fmaxf(a2, 0.f) * 2048.f);
    M = fminf(fmaxf(ceilf(M), -110.f), 110.f);
    f32x4 e = {a2, rev, lw - M + 8.f, pw};
    if (lane < 32) *(LAS f32x4*)(sm + L_PAR + wid * 512 + lane * 16) = e;
    if (lane == 0) *(LAS float*)(sm + L_MISC + wid * 4) = M;
    return M;
}
DEV void kgen_step(LAS char* sm, int wid, int lane, int ks, float offA, float offB, f32x16& G) {
    u32x4 A, B;
#pragma unroll
    for (int j = 0; j < 4; j++) {
        const f32x4 e = *(LAS f32x4*)(sm + L_PAR + wid * 512 + (ks * 8 + (lane >> 5) * 4 + j) * 16);
        const float ma = __builtin_amdgcn_exp2f(fmaf(e.x, offA, e.z));
        const float pa = __builtin_amdgcn_fractf(fmaf(e.y, offA, e.w));
        A[j] = pkh(ma * __builtin_amdgcn_cosf(pa), -ma * __builtin_amdgcn_sinf(pa));
        const float mb = __builtin_amdgcn_exp2f(e.x * offB);
        const float pb = __builtin_amdgcn_fractf(e.y * offB);
        B[j] = pkh(mb * __builtin_amdgcn_cosf(pb), mb * __builtin_amdgcn_sinf(pb));
    }
    G = __builtin_amdgcn_mfma_f32_32x32x16_f16(__builtin_bit_cast(f16x8, A), __builtin_bit_cast(f16x8, B), G, 0, 0, 0);
}
DEV float kgen_offA(int lane) {
    const int rho = lane & 31;
    const int slot = 16 * ((rho >> 2) & 1) + (rho & 3) + 4 * (rho >> 3);
    const int u = slot >> 2, e = slot & 3;
    const int o0 = slot, o1 = 256 + 2 * (slot - 8), o2 = 512 + 256 * (u - 4) + 4 * e, o3 = 1024 + 512 * (u - 6) + 256 * (e >> 1) + 8 * (e & 1);
    const int lo = u < 2 ? o0 : o1, hi = u < 6 ? o2 : o3;
    return (float)(u < 4 ? lo : hi);
}
DEV void kgen_write(LAS char* sm, int wid, int lane, const f32x16& G) {
    u32x4 a = {pkh(G[0], G[1]), pkh(G[2], G[3]), pkh(G[4], G[5]), pkh(G[6], G[7])};
    u32x4 b = {pkh(G[8], G[9]), pkh(G[10], G[11]), pkh(G[12], G[13]), pkh(G[14], G[15])};
    LAS char* d = sm + L_G + wid * 2048 + (lane & 31) * 64 + (lane >> 5) * 32;
    *(LAS u32x4*)d = a;
    *(LAS u32x4*)(d + 16) = b;
}
DEV void kgen_expand(LAS char* sm, int wid, int lane) {
    const int cl = wid >> 2, ty = wid & 3, c = lane & 31, hb = lane >> 5;
    const f32x4 Mw = *(LAS f32x4*)(sm + L_MISC + cl * 16);
    const float M = fmaxf(fmaxf(Mw.x, Mw.y), fmaxf(Mw.z, Mw.w));
    float sc[4];
#pragma unroll
    for (int i = 0; i < 4; i++) sc[i] = __builtin_amdgcn_exp2f(Mw[i] - M);
    LAS char* g = sm + L_G + cl * 8192 + c * 64;
    float o[8];
    if (ty == 0) {
        float v[8] = {0, 0, 0, 0, 0, 0, 0, 0};
#pragma unroll
        for (int i = 0; i < 4; i++) {
            const f16x8 t = *(LAS f16x8*)(g + i * 2048 + hb * 16);
#pragma unroll
            for (int e = 0; e < 8; e++) v[e] = fmaf((float)t[e], sc[i], v[e]);
        }
        o[0] = v[0]; o[1] = hb ? v[0] : v[1]; o[2] = hb ? v[1] : v[2]; o[3] = hb ? v[2] : v[3];
        o[4] = hb ? v[2] : v[4]; o[5] = hb ? v[2] : v[5]; o[6] = hb ? v[3] : v[6]; o[7] = hb ? v[4] : v[7];
    } else if (ty == 1) {
        float v[3] = {0, 0, 0};
#pragma unroll
        for (int i = 0; i < 4; i++) {
            const f16x4 t = *(LAS f16x4*)(g + i * 2048 + 32 + hb * 8);
#pragma unroll
            for (int e = 0; e < 3; e++) v[e] = fmaf((float)t[e], sc[i], v[e]);
        }
        o[0] = o[1] = o[2] = v[0]; o[3] = o[4] = o[5] = v[1]; o[6] = o[7] = v[2];
    } else {
        float v[2] = {0, 0};
#pragma unroll
        for (int i = 0; i < 4; i++) {
            const f16x2 t = *(LAS f16x2*)(g + i * 2048 + 48 + (ty - 2) * 8 + hb * 4);
#pragma unroll
            for (int e = 0; e < 2; e++) v[e] = fmaf((float)t[e], sc[i], v[e]);
        }
        o[0] = o[1] = o[2] = o[3] = v[0]; o[4] = (c & 1) ? v[1] : v[0]; o[5] = o[6] = o[7] = v[1];
    }
    const int grp = ty * 64 + lane, m = grp >> 2, q = 3 - (grp & 3);
    u32x4 w = {pkh(o[7], o[6]), pkh(o[5], o[4]), pkh(o[3], o[2]), pkh(o[1], o[0])};
    *(LAS u32x4*)(sm + L_KP + cl * KP_CH + q * KP_PLANE + (16 + m) * 16) = w;
    if (ty == 1) { u32x4 z = {0, 0, 0, 0}; *(LAS u32x4*)(sm + L_KP + cl * KP_CH + (lane >> 4) * KP_PLANE + (lane & 15) * 16) = z; }
}

DEV void w2_dma(const Params& p, u32 lds0, int i, int half, u32 voffw2) {
    const int kq = i / 28, j = i - kq * 28;
    const char* src = (const char*)p.w2 + (size_t)(448 * kq + 224 * half + 8 * j) * 1024;
    glds(src, voffw2, lds0 + L_W2 + i * 1024);
}

DEV void build_copies(LAS char* sm, int tid, int lane, u32x4 v, u32x2 pv) {
    u32 p2 = __shfl_up(v.z, 2), p3 = __shfl_up(v.w, 2);
    if (lane < 2) { p2 = pv.x; p3 = pv.y; }
    if (tid < 2) { p2 = 0; p3 = 0; }
    LAS char* d = sm + L_UC + (tid & 1) * UC_CH + ((tid >> 1) + 4) * 16;
    const u32 a0 = __builtin_amdgcn_alignbit(p3, p2, 16), a1 = __builtin_amdgcn_alignbit(v.x, p3, 16), a2 = __builtin_amdgcn_alignbit(v.y, v.x, 16),
              a3 = __builtin_amdgcn_alignbit(v.z, v.y, 16), a4 = __builtin_amdgcn_alignbit(v.w, v.z, 16);
    *(LAS u32x4*)d = v;
    u32x4 c1 = {a1, a2, a3, a4}, c2 = {p3, v.x, v.y, v.z}, c3 = {a0, a1, a2, a3};
    *(LAS u32x4*)(d + UC_COPY) = c1;
    *(LAS u32x4*)(d + 2 * UC_COPY) = c2;
    *(LAS u32x4*)(d + 3 * UC_COPY) = c3;
    if (tid < 32) { u32x4 z = {0, 0, 0, 0}; *(LAS u32x4*)(sm + L_UC + (tid >> 2) * UC_COPY + (tid & 3) * 16) = z; }
}

template <int PH>
__global__ __launch_bounds__(512, 2) void fused_kernel(Params p) {
    __shared__ __attribute__((aligned(1024))) char shm[163840];
    LAS char* sm = (LAS char*)shm;
    const u32 lds0 = (u32)(uintptr_t)sm;
    const int tid = threadIdx.x, lane = tid & 63, wid = __builtin_amdgcn_readfirstlane(tid >> 6);
    const int b = blockIdx.x, g = b & 7, r = b >> 3;
    const int h = lane >> 5, l31 = lane & 31;
    char* flags = p.ws + WS_FLAG;
    const int ch0 = 64 * g + 16 * (r & 3) + (r >> 2);
    const int tt = g * 4 + (r >> 3), dt = r & 7;
    const u32 voffw2 = (u32)((lane >> 3) * 1024 + dt * 128 + (((lane & 7) ^ (((lane >> 4) & 1) << 2)) << 4));
    ST(0);

    u32x2 tag = {0u, 0u};
    if (tid == 0) *(LAS u32*)(sm + L_MISC + 64) = 0;
    bool u_done = false;
    const u32 uoff = (u32)((tid >> 1) * 8192 + (r >> 2) * 1024 + (8 * g + 2 * (r & 3) + (tid & 1)) * 16);

    if (PH & 1) {
        const int chn = ch0 + 8 * (wid >> 2);
        const u32 po = (u32)(chn * 128 + (wid & 3) * 32 + l31) * 2u;
#define LOAD_PARAMS() \
        u32 are = aload_u16(p.Are, po), aim = aload_u16(p.Aim, po), bre = aload_u16(p.Bre, po), bim = aload_u16(p.Bim, po), \
            cre = aload_u16(p.Cre, po), cim = aload_u16(p.Cim, po), ldt = aload_u16(p.logdt, (u32)chn * 2u); \
        u32x2 tg0 = aload_b64_sc1(flags, FL_E * 128);
        const float offA = kgen_offA(lane), offB = (float)(8 * l31);
        f32x16 G = {};
        if (PH == 1 && tid == 0) { u32x2 z = {0u, 0u}; astore_b64_sc1(flags, (FL_S + b) * 128, z); }
        if (r < 28) {
            const int mt = (g >> 1) * 4 + r / 7, ft = (g & 1) * 7 + r % 7;
            const int tg = wid & 3, fh = wid >> 2;
            LOAD_PARAMS();
            u32 b1a = aload_u16(p.b1, (u32)(ft * 128 + fh * 64 + l31) * 2u), b1b = aload_u16(p.b1, (u32)(ft * 128 + fh * 64 + 32 + l31) * 2u);
            const char* xs = (const char*)p.x + (size_t)mt * 128 * 1024;
            const char* wsrc = (const char*)p.w1 + ft * 256;
            u32 vx[2], vw[2];
#pragma unroll
            for (int jj = 0; jj < 2; jj++) {
                vx[jj] = (u32)((16 * wid + 8 * jj + (lane >> 3)) * 1024 + (((lane & 7) ^ (4 * jj + (lane >> 4))) << 4));
                vw[jj] = (u32)((8 * wid + 4 * jj + (lane >> 4)) * 3584 + (((lane & 15) ^ (((lane >> 4) << 2) | ((2 * wid + jj) & 3))) << 4));
            }
            auto stage = [&](int kc) {
                const u32 d = lds0 + (kc & 3) * 32768 + wid * 2048;
                const char* xk = xs + kc * 128;
                const char* wk = wsrc + (size_t)kc * 64 * 3584;
                glds(xk, vx[0], d); glds(xk, vx[1], d + 1024);
                glds(wk, vw[0], d + 16384); glds(wk, vw[1], d + 16384 + 1024);
            };
            stage(0); stage(1); stage(2); stage(3);
            asm volatile("s_waitcnt vmcnt(16)" : "+v"(are), "+v"(aim), "+v"(bre), "+v"(bim), "+v"(cre), "+v"(cim), "+v"(ldt), "+v"(b1a), "+v"(b1b), "+v"(tg0) :: "memory");
            if (PH == 3) { tag.x = __builtin_amdgcn_readfirstlane(tg0.x); tag.y = __builtin_amdgcn_readfirstlane(tg0.y); }
            kgen_params(sm, wid, lane, are, aim, bre, bim, cre, cim, ldt);
            ST(1);
            const u32 xa = (u32)((32 * tg + l31) * 128 + ((h ^ ((l31 >> 1) & 7)) << 4));
            u32 wa[2][2];
            {
                const int blk = (lane >> 4) & 1, q = (lane & 15) >> 2, pp = lane & 3;
#pragma unroll
                for (int j2 = 0; j2 < 2; j2++)
#pragma unroll
                    for (int t = 0; t < 2; t++)
                        wa[j2][t] = (u32)(16384 + 256 * (8 * h + 4 * t + q) + (((4 * (2 * fh + j2) + 2 * blk + (pp >> 1)) ^ ((q << 2) | (2 * h + t))) << 4) + 8 * (pp & 1));
            }
            f32x16 acc[2] = {};
            float s1 = 0.f, s2 = 0.f, cs[2] = {0.f, 0.f};
            const u32 ones = 0x3f803f80u;
            auto chunk = [&](int kc) {
                LAS char* sl = sm + (kc & 3) * 32768;
                u32x4 xf[4]; u32x4 wf[4][2];
#pragma unroll
                for (int s = 0; s < 4; s++) {
                    xf[s] = *(LAS u32x4*)(sl + (xa ^ (u32)(s << 5)));
#pragma unroll
                    for (int j2 = 0; j2 < 2; j2++) {
                        const u32x2 lo = __builtin_bit_cast(u32x2, __builtin_amdgcn_ds_read_tr16_b64_v4i16((LAS s16x4*)(sl + wa[j2][0] + s * 4096)));
                        const u32x2 hi = __builtin_bit_cast(u32x2, __builtin_amdgcn_ds_read_tr16_b64_v4i16((LAS s16x4*)(sl + wa[j2][1] + s * 4096)));
                        wf[s][j2] = (u32x4){lo.x, lo.y, hi.x, hi.y};
                    }
                }
#pragma unroll
                for (int s = 0; s < 4; s++)
#pragma unroll
                    for (int j2 = 0; j2 < 2; j2++)
                        acc[j2] = __builtin_amdgcn_mfma_f32_32x32x16_bf16(__builtin_bit_cast(bf16x8, wf[s][j2]), __builtin_bit_cast(bf16x8, xf[s]), acc[j2], 0, 0, 0);
                if ((kc & 1) == fh) {
#pragma unroll
                    for (int s = 0; s < 4; s++)
#pragma unroll
                        for (int i = 0; i < 4; i++) { s1 = dot2(xf[s][i], ones, s1); s2 = dot2(xf[s][i], xf[s][i], s2); }
                }
                if ((kc & 3) == ((tg + 2 * fh + 1) & 3)) {
#pragma unroll
                    for (int s = 0; s < 4; s++)
#pragma unroll
                        for (int i = 0; i < 4; i++) { cs[0] = dot2(wf[s][0][i], ones, cs[0]); cs[1] = dot2(wf[s][1][i], ones, cs[1]); }
                }
            };
#pragma unroll 1
            for (int kc = 0; kc < 4; kc++) {
                if (kc == 0) WAITV(12); else WAITV(8);
                BAR();
                if (kc > 0) stage(kc + 3);
                chunk(kc);
                kgen_step(sm, wid, lane, kc, offA, offB, G);
            }
            ST(2);
#pragma unroll 1
            for (int kc = 4; kc < 8; kc++) {
                if (PH == 3 || kc < 6) WAITV(8); else if (kc == 6) WAITV(4); else WAITV(0);
                BAR();
                if (kc == 4) { stage(7); kgen_write(sm, wid, lane, G); }
                else if (PH == 3) {
#pragma unroll
                    for (int jj = 0; jj < 4; jj++) w2_dma(p, lds0, (kc - 5) * 32 + 4 * wid + jj, 0, voffw2);
                }
                chunk(kc);
                if (kc == 5) kgen_expand(sm, wid, lane);
            }
            s1 += __shfl_xor(s1, 32); s2 += __shfl_xor(s2, 32); cs[0] += __shfl_xor(cs[0], 32); cs[1] += __shfl_xor(cs[1], 32);
            {
                f32x2 e = h ? (f32x2){cs[0], cs[1]} : (f32x2){s1, s2};
                *(LAS f32x2*)(sm + L_XCH + h * 2048 + wid * 256 + l31 * 8) = e;
            }
            BAR();
            ST(3);
            u32x2 fl;
            if (PH == 3) {
                fl = aload_b64_sc1(flags, (u32)(FL_LN + l31) * 128u);
                w2_dma(p, lds0, 96 + 2 * wid, 0, voffw2); w2_dma(p, lds0, 97 + 2 * wid, 0, voffw2);
            }
            float rr, rinv;
            {
                const f32x2 o = *(LAS f32x2*)(sm + L_XCH + (wid ^ 4) * 256 + l31 * 8);
                s1 += o.x; s2 += o.y;
                float c0 = 0.f, c1 = 0.f;
#pragma unroll
                for (int i = 0; i < 4; i++) { const f32x2 e = *(LAS f32x2*)(sm + L_XCH + 2048 + (fh * 4 + i) * 256 + l31 * 8); c0 += e.x; c1 += e.y; }
                const float m = s1 * (1.f / 512.f);
                const float v1 = fmaxf(s2 * (1.f / 512.f) - m * m, 0.f);
                const float q1 = v1 + 1e-5f;
                const float q2 = v1 / q1 + 1e-5f;
                rinv = sqrtf(q1 * q2); rr = 1.f / rinv;
                const float nm = -m, mh = rbf(nm), ml = nm - mh, ih = rbf(rinv), il = rinv - ih;
                const u32 mm = pkbf(mh, ml);
                u32x4 Bx = {mm, mm, pkbf(ih, il), 0u};
                if (h) Bx = (u32x4){0u, 0u, 0u, 0u};
                const float c0h = rbf(c0), c1h = rbf(c1);
                u32x4 A0 = {pkbf(c0h, c0h), pkbf(c0 - c0h, c0 - c0h), b1a | (b1a << 16), 0u};
                u32x4 A1 = {pkbf(c1h, c1h), pkbf(c1 - c1h, c1 - c1h), b1b | (b1b << 16), 0u};
                acc[0] = __builtin_amdgcn_mfma_f32_32x32x16_bf16(__builtin_bit_cast(bf16x8, A0), __builtin_bit_cast(bf16x8, Bx), acc[0], 0, 0, 0);
                acc[1] = __builtin_amdgcn_mfma_f32_32x32x16_bf16(__builtin_bit_cast(bf16x8, A1), __builtin_bit_cast(bf16x8, Bx), acc[1], 0, 0, 0);
            }
            const float K1 = -2.302208198144325f * rr, K2 = -0.10294323958002349f * rr * rr * rr;
            char* hdst = p.ws + WS_H + (size_t)((mt * 4 + tg) * 112 + ft * 8 + fh * 4) * 1024;
            u32x4 uv; u32x2 upv;
#pragma unroll
            for (int j2 = 0; j2 < 2; j2++) {
                float gl[16];
#pragma unroll
                for (int i = 0; i < 16; i++) {
                    const float u = acc[j2][i];
                    const float e = __builtin_amdgcn_exp2f(fmaf(u * u, K2, K1) * u);
                    gl[i] = u * __builtin_amdgcn_rcpf(fmaf(e, rinv, rinv));
                }
                if (PH == 3 && j2 == 0) {
                    asm volatile("s_waitcnt vmcnt(2)" : "+v"(fl) :: "memory");
                    u_done = __all(fl.x == tag.x + 1u && fl.y == tag.y);
                    const char* ub = u_done ? (const char*)p.ws + WS_U : (const char*)p.x;
                    uv = aload_b128_sc1(ub, uoff);
                    upv = aload_b64_sc1(ub, tid >= 2 ? uoff - 8192 + 8 : uoff);
                }
#pragma unroll
                for (int s = 0; s < 2; s++) {
                    u32x4 o = {pkbf(gl[8 * s], gl[8 * s + 1]), pkbf(gl[8 * s + 2], gl[8 * s + 3]), pkbf(gl[8 * s + 4], gl[8 * s + 5]), pkbf(gl[8 * s + 6], gl[8 * s + 7])};
                    astore_b128_sc1(hdst, (u32)((j2 * 2 + s) * 1024 + lane * 16), o);
                }
            }
            if (PH == 3) {
                asm volatile("s_waitcnt vmcnt(4)" : "+v"(uv), "+v"(upv) :: "memory");
                ST(4);
                if (u_done) build_copies(sm, tid, lane, uv, upv);
                WAITV(0);
                BAR();
                if (tid == 0) { u32x2 t1 = {tag.x + 1u, tag.y}; astore_b64_sc1(flags, (u32)(FL_H + mt * 16 + ft) * 128u, t1); }
            }
            ST(5);
        } else {
            const int tb = (g * 4 + (r - 28)) * 8 + wid;
            u32x4 xv[8];
#pragma unroll
            for (int i = 0; i < 8; i++) xv[i] = aload_b128((const char*)p.x + (size_t)tb * 8192, (u32)(i * 1024 + lane * 16));
            LOAD_PARAMS();
            asm volatile("s_waitcnt vmcnt(0)" : "+v"(xv[0]), "+v"(xv[1]), "+v"(xv[2]), "+v"(xv[3]), "+v"(xv[4]), "+v"(xv[5]), "+v"(xv[6]), "+v"(xv[7]),
                         "+v"(are), "+v"(aim), "+v"(bre), "+v"(bim), "+v"(cre), "+v"(cim), "+v"(ldt), "+v"(tg0) :: "memory");
            if (PH == 3) { tag.x = __builtin_amdgcn_readfirstlane(tg0.x); tag.y = __builtin_amdgcn_readfirstlane(tg0.y); }
            float mean[8], rs[8];
#pragma unroll
            for (int i = 0; i < 8; i++) {
                float s = 0.f;
#pragma unroll
                for (int j = 0; j < 4; j++) s += bflo(xv[i][j]) + bfhi(xv[i][j]);
                mean[i] = s;
            }
#pragma unroll
            for (int o = 32; o; o >>= 1)
#pragma unroll
                for (int i = 0; i < 8; i++) mean[i] += __shfl_xor(mean[i], o);
#pragma unroll
            for (int i = 0; i < 8; i++) {
                mean[i] *= (1.f / 512.f);
                float s = 0.f;
#pragma unroll
                for (int j = 0; j < 4; j++) { const float a = bflo(xv[i][j]) - mean[i], c = bfhi(xv[i][j]) - mean[i]; s = fmaf(a, a, fmaf(c, c, s)); }
                rs[i] = s;
            }
#pragma unroll
            for (int o = 32; o; o >>= 1)
#pragma unroll
                for (int i = 0; i < 8; i++) rs[i] += __shfl_xor(rs[i], o);
#pragma unroll
            for (int i = 0; i < 8; i++) rs[i] = __builtin_amdgcn_rsqf(rs[i] * (1.f / 512.f) + 1e-5f);
#pragma unroll
            for (int c = 0; c < 8; c++) {
                u32x4 o;
#pragma unroll
                for (int i = 0; i < 4; i++) {
                    const u32 a = xv[2 * i][c >> 1], d = xv[2 * i + 1][c >> 1];
                    const float fa = (c & 1) ? bfhi(a) : bflo(a), fd = (c & 1) ? bfhi(d) : bflo(d);
                    o[i] = pkh((fa - mean[2 * i]) * rs[2 * i], (fd - mean[2 * i + 1]) * rs[2 * i + 1]);
                }
                astore_b128_sc1(p.ws + WS_U + (size_t)tb * 8192, (u32)(c * 1024 + lane * 16), o);
            }
            ST(1);
            kgen_params(sm, wid, lane, are, aim, bre, bim, cre, cim, ldt);
            if (PH == 3) {
                WAITV(0);
                BAR();
                if (tid == 0) { u32x2 t1 = {tag.x + 1u, tag.y}; astore_b64_sc1(flags, (u32)(FL_LN + g * 4 + (r - 28)) * 128u, t1); }
#pragma unroll 1
                for (int jj = 0; jj < 14; jj++) w2_dma(p, lds0, 14 * wid + jj, 0, voffw2);
            }
            ST(2);
#pragma unroll 1
            for (int ks = 0; ks < 4; ks++) kgen_step(sm, wid, lane, ks, offA, offB, G);
            kgen_write(sm, wid, lane, G);
            BAR();
            kgen_expand(sm, wid, lane);
            WAITV(0);
            BAR();
            ST(5);
        }
        if (PH == 1) {
            BAR();
            char* kg = p.ws + WS_KG + (size_t)b * 16384;
            for (int i = tid; i < 640; i += 512) *(u32x4*)(kg + i * 16) = *(LAS u32x4*)(sm + L_KP + i * 16);
            if (tid < 8) *(float*)(kg + 10240 + tid * 4) = *(LAS float*)(sm + L_MISC + tid * 4);
            return;
        }
    }
    if (PH == 2) {
        const char* kg = p.ws + WS_KG + (size_t)b * 16384;
#pragma unroll 1
        for (int jj = 0; jj < 14; jj++) w2_dma(p, lds0, 14 * wid + jj, 0, voffw2);
        for (int i = tid; i < 640; i += 512) *(LAS u32x4*)(sm + L_KP + i * 16) = *(const u32x4*)(kg + i * 16);
        if (tid < 8) *(LAS float*)(sm + L_MISC + tid * 4) = *(const float*)(kg + 10240 + tid * 4);
        WAITV(0);
        BAR();
    }
    const u32x2 tag1 = {tag.x + 1u, tag.y};
    const u32 xo = (u32)((tt * 64 + (tid >> 3)) * 1024 + dt * 128 + (tid & 7) * 16);
    u32x4 xe, b2e;

    u32x4 wreg[14][2];
    const int kq = wid & 3;
    u32 w2a[2];
    {
        const int blk = (lane >> 4) & 1, q = (lane & 15) >> 2, pp = lane & 3;
#pragma unroll
        for (int j2 = 0; j2 < 2; j2++)
            w2a[j2] = (u32)(L_W2 + kq * W2_REG + (4 * h + q) * 128 + (((4 * j2 + 2 * blk + (pp >> 1)) ^ (((q >> 1) & 1) << 2)) << 4) + 8 * (pp & 1));
    }
    auto w2frag = [&](int s, int j2) {
        const u32x2 lo = __builtin_bit_cast(u32x2, __builtin_amdgcn_ds_read_tr16_b64_v4i16((LAS s16x4*)(sm + w2a[j2] + s * 2048)));
        const u32x2 hi = __builtin_bit_cast(u32x2, __builtin_amdgcn_ds_read_tr16_b64_v4i16((LAS s16x4*)(sm + w2a[j2] + s * 2048 + 1024)));
        return (u32x4){lo.x, lo.y, hi.x, hi.y};
    };
    if (!u_done) {
        if (PH == 3) poll_flags(flags, FL_LN, 1, 32, tag1, lane);
        u32x4 uv = aload_b128_sc1(p.ws + WS_U, uoff);
        u32x2 upv = aload_b64_sc1(p.ws + WS_U, tid >= 2 ? uoff - 8192 + 8 : uoff);
        asm volatile("s_waitcnt vmcnt(0)" : "+v"(uv), "+v"(upv) :: "memory");
        build_copies(sm, tid, lane, uv, upv);
    }
    BAR();
    ST(6);
    if (wid < 4) {
        const int cl = wid >> 1, ra = wid & 1, a = lane & 15, q = lane >> 4;
        LAS char* ub = sm + L_UC + cl * UC_CH + (3 - (a & 3)) * UC_COPY + (4 * ra + 2 * q + 1 + (a >> 2)) * 8;
        LAS char* kb = sm + L_KP + cl * KP_CH + q * KP_PLANE + (16 + a) * 16;
        f32x4 y[4] = {};
        const u32 dsk = p.Dskip[ch0 + 8 * cl];
        xe = *(const u32x4*)((const char*)p.x + xo); b2e = *(const u32x4*)((const char*)p.b2 + dt * 128 + (tid & 7) * 16);
#pragma unroll 2
        for (int dl = 0; dl < 16; dl++) {
            u32x4 A[4], B[4];
#pragma unroll
            for (int i = 0; i < 4; i++) {
                B[i] = *(LAS u32x4*)(kb - dl * 16 + i * 256);
                const u32x2 lo = *(volatile LAS u32x2*)(ub + dl * 64 + i * 1024), hi = *(volatile LAS u32x2*)(ub + dl * 64 + i * 1024 + 8);
                A[i] = (u32x4){lo.x, lo.y, hi.x, hi.y};
            }
#pragma unroll
            for (int gm = 0; gm < 4; gm++)
#pragma unroll
                for (int c = gm; c < 4; c++)
                    y[c] = __builtin_amdgcn_mfma_f32_16x16x32_f16(__builtin_bit_cast(f16x8, A[gm]), __builtin_bit_cast(f16x8, B[c - gm]), y[c], 0, 0, 0);
        }
        ST(7);
        const f32x4 Mw = *(LAS f32x4*)(sm + L_MISC + cl * 16);
        const float ksc = __builtin_amdgcn_exp2f(fmaxf(fmaxf(Mw.x, Mw.y), fmaxf(Mw.z, Mw.w)) - 8.f);
        const float dk = bf2f(dsk);
        char* sdst = p.ws + WS_S + (size_t)(ch0 + 8 * cl) * 4096 + ra * 2048;
        LAS char* lin = sm + L_XCH + wid * 1024;
#pragma unroll
        for (int cp = 0; cp < 2; cp++) {
#pragma unroll
            for (int cc = 0; cc < 2; cc++) {
                const int c = 2 * cp + cc;
                const f16x4 uu = *(LAS f16x4*)(sm + L_UC + cl * UC_CH + (32 + 32 * (16 * c + a) + 16 * ra + 4 * q) * 2);
                u32x2 o = {pkbf(fmaf(y[c][0], ksc, dk * (float)uu[0]), fmaf(y[c][1], ksc, dk * (float)uu[1])),
                           pkbf(fmaf(y[c][2], ksc, dk * (float)uu[2]), fmaf(y[c][3], ksc, dk * (float)uu[3]))};
                *(LAS u32x2*)(lin + (16 * cc + a) * 32 + q * 8) = o;
            }
            WAITL0();
            u32x4 v = *(LAS u32x4*)(lin + lane * 16);
            asm volatile("s_waitcnt lgkmcnt(0)" : "+v"(v) :: "memory");
            astore_b128_sc1(sdst, (u32)(cp * 1024 + lane * 16), v);
        }
        WAITV(0);
        u32 arr = 0;
        if (lane == 0) arr = __hip_atomic_fetch_add((LAS u32*)(sm + L_MISC + 64), 1u, __ATOMIC_RELAXED, __HIP_MEMORY_SCOPE_WORKGROUP);
        arr = __builtin_amdgcn_readfirstlane(arr);
        if (arr == 3 && lane == 0) astore_b64_sc1(flags, (u32)(FL_S + b) * 128u, tag1);
        ST(8);
        poll_flags(flags, FL_S + dt, 8, 32, tag1, lane);
        for (int spin = 0; spin < (1 << 20); ++spin) {
            if (__builtin_amdgcn_readfirstlane(__hip_atomic_load((LAS u32*)(sm + L_MISC + 64), __ATOMIC_RELAXED, __HIP_MEMORY_SCOPE_WORKGROUP)) >= 4u) break;
            __builtin_amdgcn_s_sleep(1);
        }
        u32x4 sv[2];
#pragma unroll
        for (int i = 0; i < 2; i++) {
            const int d = wid * 16 + i * 8 + (lane >> 3), pc = lane & 7;
            sv[i] = aload_b128_sc1(p.ws + WS_S, (u32)((dt * 64 + d) * 4096 + (pc >> 2) * 2048 + tt * 64 + (pc & 3) * 16));
        }
        asm volatile("s_waitcnt vmcnt(0)" : "+v"(sv[0]), "+v"(sv[1]) :: "memory");
#pragma unroll
        for (int i = 0; i < 2; i++) {
            const int d = wid * 16 + i * 8 + (lane >> 3), pc = lane & 7;
            const int chunk = ((pc >> 1) & 1) * 4 + (pc >> 2) * 2 + (pc & 1);
            *(LAS u32x4*)(sm + L_UC + d * 128 + ((chunk ^ (d >> 3)) << 4)) = sv[i];
        }
        ST(9);
    } else {
#pragma unroll
        for (int s = 0; s < 14; s++) { wreg[s][0] = w2frag(s, 0); wreg[s][1] = w2frag(s, 1); }
#pragma unroll
        for (int s = 0; s < 14; s++) asm volatile("" : "+v"(wreg[s][0]), "+v"(wreg[s][1]));
        WAITL0();
#pragma unroll 1
        for (int jj = 0; jj < 28; jj++) w2_dma(p, lds0, 28 * kq + jj, 1, voffw2);
        ST(7);
        if (PH == 3) poll_flags(flags, FL_H + (tt >> 1) * 16, 1, 14, tag1, lane);
        WAITV(0);
        ST(8);
        const __amdgpu_buffer_rsrc_t hr = __builtin_amdgcn_make_buffer_rsrc(p.ws + WS_H + (size_t)(tt * 2 * 112 + kq * 28) * 1024, 0, 2 * 112 * 1024, 0x00020000);
        constexpr int WIN = 7;
        u32x4 hf[WIN][2];
        f32x16 acc[2][2] = {};
#pragma unroll
        for (int s = 0; s < WIN; s++) {
            hf[s][0] = __builtin_amdgcn_raw_buffer_load_b128(hr, lane * 16, s * 1024, 16);
            hf[s][1] = __builtin_amdgcn_raw_buffer_load_b128(hr, lane * 16 + 112 * 1024, s * 1024, 16);
        }
        SCHED();
#pragma unroll
        for (int s = 0; s < 28; s++) {
            u32x4 w0, w1;
            if (s < 14) { w0 = wreg[s][0]; w1 = wreg[s][1]; } else { w0 = w2frag(s - 14, 0); w1 = w2frag(s - 14, 1); }
#pragma unroll
            for (int j = 0; j < 2; j++) {
                acc[0][j] = __builtin_amdgcn_mfma_f32_32x32x16_bf16(__builtin_bit_cast(bf16x8, w0), __builtin_bit_cast(bf16x8, hf[s % WIN][j]), acc[0][j], 0, 0, 0);
                acc[1][j] = __builtin_amdgcn_mfma_f32_32x32x16_bf16(__builtin_bit_cast(bf16x8, w1), __builtin_bit_cast(bf16x8, hf[s % WIN][j]), acc[1][j], 0, 0, 0);
            }
            if (s + WIN < 28) {
                hf[s % WIN][0] = __builtin_amdgcn_raw_buffer_load_b128(hr, lane * 16, (s + WIN) * 1024, 16);
                hf[s % WIN][1] = __builtin_amdgcn_raw_buffer_load_b128(hr, lane * 16 + 112 * 1024, (s + WIN) * 1024, 16);
            }
            if (s + WIN == 28) { xe = *(const u32x4*)((const char*)p.x + xo); b2e = *(const u32x4*)((const char*)p.b2 + dt * 128 + (tid & 7) * 16); }
            SCHED();
        }
        ST(9);
#pragma unroll
        for (int j2 = 0; j2 < 2; j2++)
#pragma unroll
            for (int j = 0; j < 2; j++)
#pragma unroll
                for (int rg = 0; rg < 4; rg++) {
                    f32x4 v = {acc[j2][j][4 * rg], acc[j2][j][4 * rg + 1], acc[j2][j][4 * rg + 2], acc[j2][j][4 * rg + 3]};
                    *(LAS f32x4*)(sm + L_W2 + kq * W2_REG + (32 * j + l31) * 272 + (32 * j2 + 8 * rg + 4 * h) * 4) = v;
                }
    }
    BAR();
    ST(10);
    {
        const int tok = tid >> 3, dg = tid & 7;
        float m[8];
#pragma unroll
        for (int e = 0; e < 4; e++) { m[2 * e] = bflo(b2e[e]); m[2 * e + 1] = bfhi(b2e[e]); }
#pragma unroll
        for (int i = 0; i < 4; i++) {
            const f32x4 lo = *(LAS f32x4*)(sm + L_W2 + i * W2_REG + tok * 272 + dg * 32), hi = *(LAS f32x4*)(sm + L_W2 + i * W2_REG + tok * 272 + dg * 32 + 16);
#pragma unroll
            for (int e = 0; e < 4; e++) { m[e] += lo[e]; m[4 + e] += hi[e]; }
        }
        float o[8];
#pragma unroll
        for (int e = 0; e < 8; e++) {
            const float s = bf2f(*(LAS u16*)(sm + L_UC + (8 * dg + e) * 128 + (((tok >> 3) ^ dg) << 4) + (tok & 7) * 2));
            const float gt = m[e] * __builtin_amdgcn_rcpf(1.f + __builtin_amdgcn_exp2f(-1.4426950408889634f * m[e]));
            const float xv = (e & 1) ? bfhi(xe[e >> 1]) : bflo(xe[e >> 1]);
            o[e] = fmaf(s, gt, xv);
        }
        u32x4 ov = {pkbf(o[0], o[1]), pkbf(o[2], o[3]), pkbf(o[4], o[5]), pkbf(o[6], o[7])};
        *(u32x4*)((char*)p.out + xo) = ov;
    }
    if (PH == 3 && b == 0 && wid == 0) {
#pragma unroll 1
        for (int i = 0; i < 4; i++) poll_flags(flags, FL_S + 64 * i, 1, 64, tag1, lane);
        if (lane == 0) astore_b64_sc1(flags, FL_E * 128, tag1);
    }
    ST(11);
}

extern "C" void kernel_launch(void* const* d_in, const int* in_sizes, int n_in, void* d_out, int out_size, void* d_ws, size_t ws_size,
                              hipStream_t stream) {
    Params p;
    p.x = (const u16*)d_in[0]; p.Are = (const u16*)d_in[1]; p.Aim = (const u16*)d_in[2]; p.Bre = (const u16*)d_in[3]; p.Bim = (const u16*)d_in[4];
    p.Cre = (const u16*)d_in[5]; p.Cim = (const u16*)d_in[6]; p.logdt = (const u16*)d_in[7]; p.Dskip = (const u16*)d_in[8];
    p.w1 = (const u16*)d_in[10]; p.b1 = (const u16*)d_in[11]; p.w2 = (const u16*)d_in[12]; p.b2 = (const u16*)d_in[13];
    p.out = (u16*)d_out; p.ws = (char*)d_ws;
    fused_kernel<3><<<256, 512, 0, stream>>>(p);
}
```

```cpp
#define PADWG 0
#include <hip/hip_runtime.h>
#include <stdint.h>

#define FINE 0
#define SPLIT 0

typedef unsigned short u16;
typedef unsigned u32;
typedef unsigned long long u64;
typedef u32 u32x2 __attribute__((ext_vector_type(2)));
typedef u32 u32x4 __attribute__((ext_vector_type(4)));
typedef short s16x4 __attribute__((ext_vector_type(4)));
typedef __bf16 bf16x2 __attribute__((ext_vector_type(2)));
typedef __bf16 bf16x8 __attribute__((ext_vector_type(8)));
typedef _Float16 f16x2 __attribute__((ext_vector_type(2)));
typedef _Float16 f16x4 __attribute__((ext_vector_type(4)));
typedef _Float16 f16x8 __attribute__((ext_vector_type(8)));
typedef float f32x2 __attribute__((ext_vector_type(2)));
typedef float f32x4 __attribute__((ext_vector_type(4)));
typedef float f32x16 __attribute__((ext_vector_type(16)));
#define LAS __attribute__((address_space(3)))
#define GAS __attribute__((address_space(1)))
#define DEV __device__ __forceinline__

constexpr size_t WS_H = 0;
constexpr size_t WS_U = 8u << 20;
constexpr size_t WS_S = 10u << 20;
constexpr size_t WS_FLAG = 12u << 20;
constexpr size_t WS_KG = 13u << 20;
constexpr size_t WS_STAMP = 20u << 20;
constexpr int FL_LN = 0, FL_H = 32, FL_S = 288, FL_E = 576;

constexpr int L_W2 = 0, W2_REG = 28672;
constexpr int L_UC = 114688, UC_COPY = 4160, UC_CH = 4 * UC_COPY;
constexpr int L_G = 131072;
constexpr int L_PAR = 147456;
constexpr int L_XCH = 147968;
constexpr int L_MISC = 152064;
constexpr int L_KP = 153600, KP_PLANE = 1280, KP_CH = 5120;

struct Params {
    const u16 *x, *Are, *Aim, *Bre, *Bim, *Cre, *Cim, *logdt, *Dskip, *w1, *b1, *w2, *b2;
    u16* out;
    char* ws;
};

DEV float bf2f(u32 v) { return __uint_as_float(v << 16); }
DEV float bflo(u32 v) { return __uint_as_float(v << 16); }
DEV float bfhi(u32 v) { return __uint_as_float(v & 0xffff0000u); }
DEV u32 pkbf(float a, float b) { f32x2 f = {a, b}; return __builtin_bit_cast(u32, __builtin_convertvector(f, bf16x2)); }
DEV u32 pkh(float a, float b) { f32x2 f = {a, b}; return __builtin_bit_cast(u32, __builtin_convertvector(f, f16x2)); }
DEV float rbf(float a) { return bflo(pkbf(a, a)); }
DEV float dot2(u32 a, u32 b, float c) { return __builtin_amdgcn_fdot2_f32_bf16(__builtin_bit_cast(bf16x2, a), __builtin_bit_cast(bf16x2, b), c, false); }

#define WAITV(n) asm volatile("s_waitcnt vmcnt(" #n ")" ::: "memory")
#define WAITL0() asm volatile("s_waitcnt lgkmcnt(0)" ::: "memory")
#define BAR() do { asm volatile("s_waitcnt lgkmcnt(0)" ::: "memory"); __builtin_amdgcn_s_barrier(); asm volatile("" ::: "memory"); } while (0)
#define SCHED() __builtin_amdgcn_sched_barrier(0)

DEV const void* uni(const void* q) {
    const u64 a = (u64)q;
    return (const void*)(((u64)(u32)__builtin_amdgcn_readfirstlane((u32)(a >> 32)) << 32) | (u32)__builtin_amdgcn_readfirstlane((u32)a));
}
DEV void glds(const void* base, u32 voff, u32 dst) {
    u32 keep;
    dst = __builtin_amdgcn_readfirstlane(dst);
    base = uni(base);
    asm volatile("s_mov_b32 %0, m0\n\ts_mov_b32 m0, %3\n\ts_nop 2\n\tglobal_load_lds_dwordx4 %1, %2\n\ts_mov_b32 m0, %0"
                 : "=&s"(keep) : "v"(voff), "s"(base), "s"(dst) : "memory");
}
DEV u32 aload_u16(const void* base, u32 voff) { u32 r; asm volatile("global_load_ushort %0, %1, %2" : "=v"(r) : "v"(voff), "s"(base) : "memory"); return r; }
DEV u32x2 aload_b64_sc1(const void* base, u32 voff) { u32x2 r; asm volatile("global_load_dwordx2 %0, %1, %2 sc1" : "=v"(r) : "v"(voff), "s"(base) : "memory"); return r; }
DEV u32x4 aload_b128_sc1(const void* base, u32 voff) { u32x4 r; asm volatile("global_load_dwordx4 %0, %1, %2 sc1" : "=v"(r) : "v"(voff), "s"(base) : "memory"); return r; }
DEV u32x4 aload_b128(const void* base, u32 voff) { u32x4 r; asm volatile("global_load_dwordx4 %0, %1, %2" : "=v"(r) : "v"(voff), "s"(base) : "memory"); return r; }
DEV void astore_b128_sc1(void* base, u32 voff, u32x4 v) { asm volatile("global_store_dwordx4 %0, %1, %2 sc1\n\ts_nop 1" :: "v"(voff), "v"(v), "s"(base) : "memory"); }
DEV void astore_b64_sc1(void* base, u32 voff, u32x2 v) { asm volatile("global_store_dwordx2 %0, %1, %2 sc1\n\ts_nop 1" :: "v"(voff), "v"(v), "s"(base) : "memory"); }

#define ST(i)
#define SC(i)

DEV void poll_flags(const char* flags, int first, int stride, int nfl, u32x2 tag, int lane) {
    const u32 off = (u32)(first + (lane < nfl ? lane : 0) * stride) * 128u;
    for (int spin = 0; spin < (1 << 16); ++spin) {
        u32x2 v = aload_b64_sc1(flags, off);
        asm volatile("s_waitcnt vmcnt(0)" : "+v"(v) :: "memory");
        if (__all(v.x == tag.x && v.y == tag.y)) break;
        __builtin_amdgcn_s_sleep(1);
    }
}

DEV float wave_max(float v) {
#pragma unroll
    for (int o = 16; o; o >>= 1) v = fmaxf(v, __shfl_xor(v, o));
    return v;
}

DEV float kgen_params(LAS char* sm, int wid, int lane, u32 are, u32 aim, u32 bre, u32 bim, u32 cre, u32 cim, u32 ldt) {
    const float dt = rbf(__expf(bf2f(ldt)));
    const float a2 = dt * bf2f(are) * 1.4426950408889634f;
    const float rev = dt * bf2f(aim) * 0.15915494309189535f;
    const float br = bf2f(bre), bi = bf2f(bim), cr = bf2f(cre), ci = bf2f(cim);
    const float wr = cr * br - ci * bi, wi = cr * bi + ci * br;
    const float lw = 0.5f * (__log2f(cr * cr + ci * ci) + __log2f(br * br + bi * bi)) + __log2f(dt);
    const float pw = atan2f(wi, wr) * 0.15915494309189535f;
    float M = wave_max(lw + fmaxf(a2, 0.f) * 2048.f);
    M = fminf(fmaxf(ceilf(M), -110.f), 110.f);
    f32x4 e = {a2, rev, lw - M + 8.f, pw};
    if (lane < 32) *(LAS f32x4*)(sm + L_PAR + wid * 512 + lane * 16) = e;
    if (lane == 0) *(LAS float*)(sm + L_MISC + wid * 4) = M;
    return M;
}
DEV void kgen_step(LAS char* sm, int wid, int lane, int ks, float offA, float offB, f32x16& G) {
    u32x4 A, B;
#pragma unroll
    for (int j = 0; j < 4; j++) {
        const f32x4 e = *(LAS f32x4*)(sm + L_PAR + wid * 512 + (ks * 8 + (lane >> 5) * 4 + j) * 16);
        const float ma = __builtin_amdgcn_exp2f(fmaf(e.x, offA, e.z));
        const float pa = __builtin_amdgcn_fractf(fmaf(e.y, offA, e.w));
        A[j] = pkh(ma * __builtin_amdgcn_cosf(pa), -ma * __builtin_amdgcn_sinf(pa));
        const float mb = __builtin_amdgcn_exp2f(e.x * offB);
        const float pb = __builtin_amdgcn_fractf(e.y * offB);
        B[j] = pkh(mb * __builtin_amdgcn_cosf(pb), mb * __builtin_amdgcn_sinf(pb));
    }
    G = __builtin_amdgcn_mfma_f32_32x32x16_f16(__builtin_bit_cast(f16x8, A), __builtin_bit_cast(f16x8, B), G, 0, 0, 0);
}
DEV float kgen_offA(int lane) {
    const int rho = lane & 31;
    const int slot = 16 * ((rho >> 2) & 1) + (rho & 3) + 4 * (rho >> 3);
    const int u = slot >> 2, e = slot & 3;
    const int o0 = slot, o1 = 256 + 2 * (slot - 8), o2 = 512 + 256 * (u - 4) + 4 * e, o3 = 1024 + 512 * (u - 6) + 256 * (e >> 1) + 8 * (e & 1);
    const int lo = u < 2 ? o0 : o1, hi = u < 6 ? o2 : o3;
    return (float)(u < 4 ? lo : hi);
}
DEV void kgen_write(LAS char* sm, int wid, int lane, const f32x16& G) {
    u32x4 a = {pkh(G[0], G[1]), pkh(G[2], G[3]), pkh(G[4], G[5]), pkh(G[6], G[7])};
    u32x4 b = {pkh(G[8], G[9]), pkh(G[10], G[11]), pkh(G[12], G[13]), pkh(G[14], G[15])};
    LAS char* d = sm + L_G + wid * 2048 + (lane & 31) * 64 + (lane >> 5) * 32;
    *(LAS u32x4*)d = a;
    *(LAS u32x4*)(d + 16) = b;
}
DEV void kgen_expand(LAS char* sm, int wid, int lane) {
    const int cl = wid >> 2, ty = wid & 3, c = lane & 31, hb = lane >> 5;
    const f32x4 Mw = *(LAS f32x4*)(sm + L_MISC + cl * 16);
    const float M = fmaxf(fmaxf(Mw.x, Mw.y), fmaxf(Mw.z, Mw.w));
    float sc[4];
#pragma unroll
    for (int i = 0; i < 4; i++) sc[i] = __builtin_amdgcn_exp2f(Mw[i] - M);
    LAS char* g = sm + L_G + cl * 8192 + c * 64;
    float o[8];
    if (ty == 0) {
        float v[8] = {0, 0, 0, 0, 0, 0, 0, 0};
#pragma unroll
        for (int i = 0; i < 4; i++) {
            const f16x8 t = *(LAS f16x8*)(g + i * 2048 + hb * 16);
#pragma unroll
            for (int e = 0; e < 8; e++) v[e] = fmaf((float)t[e], sc[i], v[e]);
        }
        o[0] = v[0]; o[1] = hb ? v[0] : v[1]; o[2] = hb ? v[1] : v[2]; o[3] = hb ? v[2] : v[3];
        o[4] = hb ? v[2] : v[4]; o[5] = hb ? v[2] : v[5]; o[6] = hb ? v[3] : v[6]; o[7] = hb ? v[4] : v[7];
    } else if (ty == 1) {
        float v[3] = {0, 0, 0};
#pragma unroll
        for (int i = 0; i < 4; i++) {
            const f16x4 t = *(LAS f16x4*)(g + i * 2048 + 32 + hb * 8);
#pragma unroll
            for (int e = 0; e < 3; e++) v[e] = fmaf((float)t[e], sc[i], v[e]);
        }
        o[0] = o[1] = o[2] = v[0]; o[3] = o[4] = o[5] = v[1]; o[6] = o[7] = v[2];
    } else {
        float v[2] = {0, 0};
#pragma unroll
        for (int i = 0; i < 4; i++) {
            const f16x2 t = *(LAS f16x2*)(g + i * 2048 + 48 + (ty - 2) * 8 + hb * 4);
#pragma unroll
            for (int e = 0; e < 2; e++) v[e] = fmaf((float)t[e], sc[i], v[e]);
        }
        o[0] = o[1] = o[2] = o[3] = v[0]; o[4] = (c & 1) ? v[1] : v[0]; o[5] = o[6] = o[7] = v[1];
    }
    const int grp = ty * 64 + lane, m = grp >> 2, q = 3 - (grp & 3);
    u32x4 w = {pkh(o[7], o[6]), pkh(o[5], o[4]), pkh(o[3], o[2]), pkh(o[1], o[0])};
    *(LAS u32x4*)(sm + L_KP + cl * KP_CH + q * KP_PLANE + (16 + m) * 16) = w;
    if (ty == 1) { u32x4 z = {0, 0, 0, 0}; *(LAS u32x4*)(sm + L_KP + cl * KP_CH + (lane >> 4) * KP_PLANE + (lane & 15) * 16) = z; }
}

DEV void w2_dma(const Params& p, u32 lds0, int i, int half, u32 voffw2) {
    const int kq = i / 28, j = i - kq * 28;
    const char* src = (const char*)p.w2 + (size_t)(448 * kq + 224 * half + 8 * j) * 1024;
    glds(src, voffw2, lds0 + L_W2 + i * 1024);
}

DEV void build_copies(LAS char* sm, int tid, int lane, u32x4 v, u32x2 pv) {
    u32 p2 = __shfl_up(v.z, 2), p3 = __shfl_up(v.w, 2);
    if (lane < 2) { p2 = pv.x; p3 = pv.y; }
    if (tid < 2) { p2 = 0; p3 = 0; }
    LAS char* d = sm + L_UC + (tid & 1) * UC_CH + ((tid >> 1) + 4) * 16;
    const u32 a0 = __builtin_amdgcn_alignbit(p3, p2, 16), a1 = __builtin_amdgcn_alignbit(v.x, p3, 16), a2 = __builtin_amdgcn_alignbit(v.y, v.x, 16),
              a3 = __builtin_amdgcn_alignbit(v.z, v.y, 16), a4 = __builtin_amdgcn_alignbit(v.w, v.z, 16);
    *(LAS u32x4*)d = v;
    u32x4 c1 = {a1, a2, a3, a4}, c2 = {p3, v.x, v.y, v.z}, c3 = {a0, a1, a2, a3};
    *(LAS u32x4*)(d + UC_COPY) = c1;
    *(LAS u32x4*)(d + 2 * UC_COPY) = c2;
    *(LAS u32x4*)(d + 3 * UC_COPY) = c3;
    if (tid < 32) { u32x4 z = {0, 0, 0, 0}; *(LAS u32x4*)(sm + L_UC + (tid >> 2) * UC_COPY + (tid & 3) * 16) = z; }
}

template <int PH>
__global__ __launch_bounds__(512, 2) void fused_kernel(Params p) {
    __shared__ __attribute__((aligned(1024))) char shm[163840];
    LAS char* sm = (LAS char*)shm;
    const u32 lds0 = (u32)(uintptr_t)sm;
    const int tid = threadIdx.x, lane = tid & 63, wid = __builtin_amdgcn_readfirstlane(tid >> 6);
    const int b = blockIdx.x, g = b & 7, r = b >> 3;
    const int h = lane >> 5, l31 = lane & 31;
    char* flags = p.ws + WS_FLAG;
    const int ch0 = 64 * g + 16 * (r & 3) + (r >> 2);
    const int tt = g * 4 + (r >> 3), dt = r & 7;
    const u32 voffw2 = (u32)((lane >> 3) * 1024 + dt * 128 + (((lane & 7) ^ (((lane >> 4) & 1) << 2)) << 4));
    const u64 t_start = __builtin_amdgcn_s_memrealtime();
    ST(0);

    u32x2 tag = {0u, 0u};
    if (tid == 0) *(LAS u32*)(sm + L_MISC + 64) = 0;
    bool u_done = false;
    const u32 uoff = (u32)((tid >> 1) * 8192 + (r >> 2) * 1024 + (8 * g + 2 * (r & 3) + (tid & 1)) * 16);

    if (PH & 1) {
        const int chn = ch0 + 8 * (wid >> 2);
        const u32 po = (u32)(chn * 128 + (wid & 3) * 32 + l31) * 2u;
#define LOAD_PARAMS() \
        u32 are = aload_u16(p.Are, po), aim = aload_u16(p.Aim, po), bre = aload_u16(p.Bre, po), bim = aload_u16(p.Bim, po), \
            cre = aload_u16(p.Cre, po), cim = aload_u16(p.Cim, po), ldt = aload_u16(p.logdt, (u32)chn * 2u); \
        u32x2 tg0 = aload_b64_sc1(flags, FL_E * 128);
        const float offA = kgen_offA(lane), offB = (float)(8 * l31);
        f32x16 G = {};
        if (PH == 1 && tid == 0) { u32x2 z = {0u, 0u}; astore_b64_sc1(flags, (FL_S + b) * 128, z); }
        if (r < 28) {
            const int mt = (g >> 1) * 4 + r / 7, ft = (g & 1) * 7 + r % 7;
            const int tg = wid & 3, fh = wid >> 2;
            LOAD_PARAMS();
            u32 b1a = aload_u16(p.b1, (u32)(ft * 128 + fh * 64 + l31) * 2u), b1b = aload_u16(p.b1, (u32)(ft * 128 + fh * 64 + 32 + l31) * 2u);
            const char* xs = (const char*)p.x + (size_t)mt * 128 * 1024;
            const char* wsrc = (const char*)p.w1 + ft * 256;
            u32 vx[2], vw[2];
#pragma unroll
            for (int jj = 0; jj < 2; jj++) {
                vx[jj] = (u32)((16 * wid + 8 * jj + (lane >> 3)) * 1024 + (((lane & 7) ^ (4 * jj + (lane >> 4))) << 4));
                vw[jj] = (u32)((8 * wid + 4 * jj + (lane >> 4)) * 3584 + (((lane & 15) ^ (((lane >> 4) << 2) | ((2 * wid + jj) & 3))) << 4));
            }
            auto stage = [&](int kc) {
                const u32 d = lds0 + (kc & 3) * 32768 + wid * 2048;
                const char* xk = xs + kc * 128;
                const char* wk = wsrc + (size_t)kc * 64 * 3584;
                glds(xk, vx[0], d); glds(xk, vx[1], d + 1024);
                glds(wk, vw[0], d + 16384); glds(wk, vw[1], d + 16384 + 1024);
            };
            stage(0); stage(1); stage(2); stage(3);
            asm volatile("s_waitcnt vmcnt(16)" : "+v"(are), "+v"(aim), "+v"(bre), "+v"(bim), "+v"(cre), "+v"(cim), "+v"(ldt), "+v"(b1a), "+v"(b1b), "+v"(tg0) :: "memory");
            if (PH == 3) { tag.x = __builtin_amdgcn_readfirstlane(tg0.x); tag.y = __builtin_amdgcn_readfirstlane(tg0.y); }
            kgen_params(sm, wid, lane, are, aim, bre, bim, cre, cim, ldt);
            ST(1);
            const u32 xa = (u32)((32 * tg + l31) * 128 + ((h ^ ((l31 >> 1) & 7)) << 4));
            u32 wa[2][2];
            {
                const int blk = (lane >> 4) & 1, q = (lane & 15) >> 2, pp = lane & 3;
#pragma unroll
                for (int j2 = 0; j2 < 2; j2++)
#pragma unroll
                    for (int t = 0; t < 2; t++)
                        wa[j2][t] = (u32)(16384 + 256 * (8 * h + 4 * t + q) + (((4 * (2 * fh + j2) + 2 * blk + (pp >> 1)) ^ ((q << 2) | (2 * h + t))) << 4) + 8 * (pp & 1));
            }
            f32x16 acc[2] = {};
            float s1 = 0.f, s2 = 0.f, cs[2] = {0.f, 0.f};
            const u32 ones = 0x3f803f80u;
            auto chunk = [&](int kc) {
                LAS char* sl = sm + (kc & 3) * 32768;
                u32x4 xf[4]; u32x4 wf[4][2];
#pragma unroll
                for (int s = 0; s < 4; s++) {
                    xf[s] = *(LAS u32x4*)(sl + (xa ^ (u32)(s << 5)));
#pragma unroll
                    for (int j2 = 0; j2 < 2; j2++) {
                        const u32x2 lo = __builtin_bit_cast(u32x2, __builtin_amdgcn_ds_read_tr16_b64_v4i16((LAS s16x4*)(sl + wa[j2][0] + s * 4096)));
                        const u32x2 hi = __builtin_bit_cast(u32x2, __builtin_amdgcn_ds_read_tr16_b64_v4i16((LAS s16x4*)(sl + wa[j2][1] + s * 4096)));
                        wf[s][j2] = (u32x4){lo.x, lo.y, hi.x, hi.y};
                    }
                }
#pragma unroll
                for (int s = 0; s < 4; s++)
#pragma unroll
                    for (int j2 = 0; j2 < 2; j2++)
                        acc[j2] = __builtin_amdgcn_mfma_f32_32x32x16_bf16(__builtin_bit_cast(bf16x8, wf[s][j2]), __builtin_bit_cast(bf16x8, xf[s]), acc[j2], 0, 0, 0);
                if ((kc & 1) == fh) {
#pragma unroll
                    for (int s = 0; s < 4; s++)
#pragma unroll
                        for (int i = 0; i < 4; i++) { s1 = dot2(xf[s][i], ones, s1); s2 = dot2(xf[s][i], xf[s][i], s2); }
                }
                if ((kc & 3) == ((tg + 2 * fh + 1) & 3)) {
#pragma unroll
                    for (int s = 0; s < 4; s++)
#pragma unroll
                        for (int i = 0; i < 4; i++) { cs[0] = dot2(wf[s][0][i], ones, cs[0]); cs[1] = dot2(wf[s][1][i], ones, cs[1]); }
                }
            };
#pragma unroll 1
            for (int kc = 0; kc < 4; kc++) {
                SC(kc * 4);
                if (kc == 0) WAITV(12); else WAITV(8);
                BAR();
                SC(kc * 4 + 1);
                if (kc > 0) stage(kc + 3);
                SC(kc * 4 + 2);
                chunk(kc);
                SC(kc * 4 + 3);
                kgen_step(sm, wid, lane, kc, offA, offB, G);
            }
            ST(2);
#pragma unroll 1
            for (int kc = 4; kc < 8; kc++) {
                SC(kc * 4);
                if (PH == 3 || kc < 6) WAITV(8); else if (kc == 6) WAITV(4); else WAITV(0);
                BAR();
                SC(kc * 4 + 1);
                if (kc == 4) { stage(7); kgen_write(sm, wid, lane, G); }
                else if (PH == 3) {
#pragma unroll
                    for (int jj = 0; jj < 4; jj++) w2_dma(p, lds0, (kc - 5) * 32 + 4 * wid + jj, 0, voffw2);
                }
                SC(kc * 4 + 2);
                chunk(kc);
                SC(kc * 4 + 3);
                if (kc == 5) kgen_expand(sm, wid, lane);
            }
            s1 += __shfl_xor(s1, 32); s2 += __shfl_xor(s2, 32); cs[0] += __shfl_xor(cs[0], 32); cs[1] += __shfl_xor(cs[1], 32);
            {
                f32x2 e = h ? (f32x2){cs[0], cs[1]} : (f32x2){s1, s2};
                *(LAS f32x2*)(sm + L_XCH + h * 2048 + wid * 256 + l31 * 8) = e;
            }
            BAR();
            ST(3);
            u32x2 fl;
            if (PH == 3) {
                fl = aload_b64_sc1(flags, (u32)(FL_LN + l31) * 128u);
                w2_dma(p, lds0, 96 + 2 * wid, 0, voffw2); w2_dma(p, lds0, 97 + 2 * wid, 0, voffw2);
            }
            float rr, rinv;
            {
                const f32x2 o = *(LAS f32x2*)(sm + L_XCH + (wid ^ 4) * 256 + l31 * 8);
                s1 += o.x; s2 += o.y;
                float c0 = 0.f, c1 = 0.f;
#pragma unroll
                for (int i = 0; i < 4; i++) { const f32x2 e = *(LAS f32x2*)(sm + L_XCH + 2048 + (fh * 4 + i) * 256 + l31 * 8); c0 += e.x; c1 += e.y; }
                const float m = s1 * (1.f / 512.f);
                const float v1 = fmaxf(s2 * (1.f / 512.f) - m * m, 0.f);
                const float q1 = v1 + 1e-5f;
                const float q2 = v1 / q1 + 1e-5f;
                rinv = sqrtf(q1 * q2); rr = 1.f / rinv;
                const float nm = -m, mh = rbf(nm), ml = nm - mh, ih = rbf(rinv), il = rinv - ih;
                const u32 mm = pkbf(mh, ml);
                u32x4 Bx = {mm, mm, pkbf(ih, il), 0u};
                if (h) Bx = (u32x4){0u, 0u, 0u, 0u};
                const float c0h = rbf(c0), c1h = rbf(c1);
                u32x4 A0 = {pkbf(c0h, c0h), pkbf(c0 - c0h, c0 - c0h), b1a | (b1a << 16), 0u};
                u32x4 A1 = {pkbf(c1h, c1h), pkbf(c1 - c1h, c1 - c1h), b1b | (b1b << 16), 0u};
                acc[0] = __builtin_amdgcn_mfma_f32_32x32x16_bf16(__builtin_bit_cast(bf16x8, A0), __builtin_bit_cast(bf16x8, Bx), acc[0], 0, 0, 0);
                acc[1] = __builtin_amdgcn_mfma_f32_32x32x16_bf16(__builtin_bit_cast(bf16x8, A1), __builtin_bit_cast(bf16x8, Bx), acc[1], 0, 0, 0);
            }
            const float K1 = -2.302208198144325f * rr, K2 = -0.10294323958002349f * rr * rr * rr;
            char* hdst = p.ws + WS_H + (size_t)((mt * 4 + tg) * 112 + ft * 8 + fh * 4) * 1024;
            u32x4 uv; u32x2 upv;
#pragma unroll
            for (int j2 = 0; j2 < 2; j2++) {
                float gl[16];
#pragma unroll
                for (int i = 0; i < 16; i++) {
                    const float u = acc[j2][i];
                    const float e = __builtin_amdgcn_exp2f(fmaf(u * u, K2, K1) * u);
                    gl[i] = u * __builtin_amdgcn_rcpf(fmaf(e, rinv, rinv));
                }
                if (PH == 3 && j2 == 0) {
                    asm volatile("s_waitcnt vmcnt(2)" : "+v"(fl) :: "memory");
                    u_done = __all(fl.x == tag.x + 1u && fl.y == tag.y);
                    const char* ub = u_done ? (const char*)p.ws + WS_U : (const char*)p.x;
                    uv = aload_b128_sc1(ub, uoff);
                    upv = aload_b64_sc1(ub, tid >= 2 ? uoff - 8192 + 8 : uoff);
                }
#pragma unroll
                for (int s = 0; s < 2; s++) {
                    u32x4 o = {pkbf(gl[8 * s], gl[8 * s + 1]), pkbf(gl[8 * s + 2], gl[8 * s + 3]), pkbf(gl[8 * s + 4], gl[8 * s + 5]), pkbf(gl[8 * s + 6], gl[8 * s + 7])};
                    astore_b128_sc1(hdst, (u32)((j2 * 2 + s) * 1024 + lane * 16), o);
                }
            }
            if (PH == 3) {
                asm volatile("s_waitcnt vmcnt(4)" : "+v"(uv), "+v"(upv) :: "memory");
                ST(4);
                if (u_done) build_copies(sm, tid, lane, uv, upv);
                WAITV(0);
                BAR();
                if (tid == 0) { u32x2 t1 = {tag.x + 1u, tag.y}; astore_b64_sc1(flags, (u32)(FL_H + mt * 16 + ft) * 128u, t1); }
            }
            ST(5);
        } else {
            const int tb = (g * 4 + (r - 28)) * 8 + wid;
            u32x4 xv[8];
#pragma unroll
            for (int i = 0; i < 8; i++) xv[i] = aload_b128((const char*)p.x + (size_t)tb * 8192, (u32)(i * 1024 + lane * 16));
            LOAD_PARAMS();
            asm volatile("s_waitcnt vmcnt(0)" : "+v"(xv[0]), "+v"(xv[1]), "+v"(xv[2]), "+v"(xv[3]), "+v"(xv[4]), "+v"(xv[5]), "+v"(xv[6]), "+v"(xv[7]),
                         "+v"(are), "+v"(aim), "+v"(bre), "+v"(bim), "+v"(cre), "+v"(cim), "+v"(ldt), "+v"(tg0) :: "memory");
            if (PH == 3) { tag.x = __builtin_amdgcn_readfirstlane(tg0.x); tag.y = __builtin_amdgcn_readfirstlane(tg0.y); }
            float mean[8], rs[8];
#pragma unroll
            for (int i = 0; i < 8; i++) {
                float s = 0.f;
#pragma unroll
                for (int j = 0; j < 4; j++) s += bflo(xv[i][j]) + bfhi(xv[i][j]);
                mean[i] = s;
            }
#pragma unroll
            for (int o = 32; o; o >>= 1)
#pragma unroll
                for (int i = 0; i < 8; i++) mean[i] += __shfl_xor(mean[i], o);
#pragma unroll
            for (int i = 0; i < 8; i++) {
                mean[i] *= (1.f / 512.f);
                float s = 0.f;
#pragma unroll
                for (int j = 0; j < 4; j++) { const float a = bflo(xv[i][j]) - mean[i], c = bfhi(xv[i][j]) - mean[i]; s = fmaf(a, a, fmaf(c, c, s)); }
                rs[i] = s;
            }
#pragma unroll
            for (int o = 32; o; o >>= 1)
#pragma unroll
                for (int i = 0; i < 8; i++) rs[i] += __shfl_xor(rs[i], o);
#pragma unroll
            for (int i = 0; i < 8; i++) rs[i] = __builtin_amdgcn_rsqf(rs[i] * (1.f / 512.f) + 1e-5f);
#pragma unroll
            for (int c = 0; c < 8; c++) {
                u32x4 o;
#pragma unroll
                for (int i = 0; i < 4; i++) {
                    const u32 a = xv[2 * i][c >> 1], d = xv[2 * i + 1][c >> 1];
                    const float fa = (c & 1) ? bfhi(a) : bflo(a), fd = (c & 1) ? bfhi(d) : bflo(d);
                    o[i] = pkh((fa - mean[2 * i]) * rs[2 * i], (fd - mean[2 * i + 1]) * rs[2 * i + 1]);
                }
                astore_b128_sc1(p.ws + WS_U + (size_t)tb * 8192, (u32)(c * 1024 + lane * 16), o);
            }
            ST(1);
            kgen_params(sm, wid, lane, are, aim, bre, bim, cre, cim, ldt);
            if (PH == 3) {
                WAITV(0);
                BAR();
                if (tid == 0) { u32x2 t1 = {tag.x + 1u, tag.y}; astore_b64_sc1(flags, (u32)(FL_LN + g * 4 + (r - 28)) * 128u, t1); }
#pragma unroll 1
                for (int jj = 0; jj < 14; jj++) w2_dma(p, lds0, 14 * wid + jj, 0, voffw2);
            }
            ST(2);
#pragma unroll 1
            for (int ks = 0; ks < 4; ks++) kgen_step(sm, wid, lane, ks, offA, offB, G);
            kgen_write(sm, wid, lane, G);
            BAR();
            kgen_expand(sm, wid, lane);
            WAITV(0);
            BAR();
            ST(5);
        }
        if (PH == 1) {
            BAR();
            char* kg = p.ws + WS_KG + (size_t)b * 16384;
            for (int i = tid; i < 640; i += 512) *(u32x4*)(kg + i * 16) = *(LAS u32x4*)(sm + L_KP + i * 16);
            if (tid < 8) *(float*)(kg + 10240 + tid * 4) = *(LAS float*)(sm + L_MISC + tid * 4);
            return;
        }
    }
    if (PH == 2) {
        const char* kg = p.ws + WS_KG + (size_t)b * 16384;
#pragma unroll 1
        for (int jj = 0; jj < 14; jj++) w2_dma(p, lds0, 14 * wid + jj, 0, voffw2);
        for (int i = tid; i < 640; i += 512) *(LAS u32x4*)(sm + L_KP + i * 16) = *(const u32x4*)(kg + i * 16);
        if (tid < 8) *(LAS float*)(sm + L_MISC + tid * 4) = *(const float*)(kg + 10240 + tid * 4);
        WAITV(0);
        BAR();
    }
    const u32x2 tag1 = {tag.x + 1u, tag.y};
    const u32 xo = (u32)((tt * 64 + (tid >> 3)) * 1024 + dt * 128 + (tid & 7) * 16);
    u32x4 xe, b2e;

    u32x4 wreg[14][2];
    const int kq = wid & 3;
    u32 w2a[2];
    {
        const int blk = (lane >> 4) & 1, q = (lane & 15) >> 2, pp = lane & 3;
#pragma unroll
        for (int j2 = 0; j2 < 2; j2++)
            w2a[j2] = (u32)(L_W2 + kq * W2_REG + (4 * h + q) * 128 + (((4 * j2 + 2 * blk + (pp >> 1)) ^ (((q >> 1) & 1) << 2)) << 4) + 8 * (pp & 1));
    }
    auto w2frag = [&](int s, int j2) {
        const u32x2 lo = __builtin_bit_cast(u32x2, __builtin_amdgcn_ds_read_tr16_b64_v4i16((LAS s16x4*)(sm + w2a[j2] + s * 2048)));
        const u32x2 hi = __builtin_bit_cast(u32x2, __builtin_amdgcn_ds_read_tr16_b64_v4i16((LAS s16x4*)(sm + w2a[j2] + s * 2048 + 1024)));
        return (u32x4){lo.x, lo.y, hi.x, hi.y};
    };
    if (!u_done) {
        if (PH == 3) poll_flags(flags, FL_LN, 1, 32, tag1, lane);
        u32x4 uv = aload_b128_sc1(p.ws + WS_U, uoff);
        u32x2 upv = aload_b64_sc1(p.ws + WS_U, tid >= 2 ? uoff - 8192 + 8 : uoff);
        asm volatile("s_waitcnt vmcnt(0)" : "+v"(uv), "+v"(upv) :: "memory");
        build_copies(sm, tid, lane, uv, upv);
    }
    BAR();
    ST(6);
    if (wid < 4) {
        const int cl = wid >> 1, ra = wid & 1, a = lane & 15, q = lane >> 4;
        LAS char* ub = sm + L_UC + cl * UC_CH + (3 - (a & 3)) * UC_COPY + (4 * ra + 2 * q + 1 + (a >> 2)) * 8;
        LAS char* kb = sm + L_KP + cl * KP_CH + q * KP_PLANE + (16 + a) * 16;
        f32x4 y[4] = {};
        const u32 dsk = p.Dskip[ch0 + 8 * cl];
        xe = *(const u32x4*)((const char*)p.x + xo); b2e = *(const u32x4*)((const char*)p.b2 + dt * 128 + (tid & 7) * 16);
#pragma unroll 2
        for (int dl = 0; dl < 16; dl++) {
            u32x4 A[4], B[4];
            SC(32 + dl);
#pragma unroll
            for (int i = 0; i < 4; i++) {
                B[i] = *(LAS u32x4*)(kb - dl * 16 + i * 256);
                const u32x2 lo = *(volatile LAS u32x2*)(ub + dl * 64 + i * 1024), hi = *(volatile LAS u32x2*)(ub + dl * 64 + i * 1024 + 8);
                A[i] = (u32x4){lo.x, lo.y, hi.x, hi.y};
            }
#pragma unroll
            for (int gm = 0; gm < 4; gm++)
#pragma unroll
                for (int c = gm; c < 4; c++)
                    y[c] = __builtin_amdgcn_mfma_f32_16x16x32_f16(__builtin_bit_cast(f16x8, A[gm]), __builtin_bit_cast(f16x8, B[c - gm]), y[c], 0, 0, 0);
        }
        ST(7);
        const f32x4 Mw = *(LAS f32x4*)(sm + L_MISC + cl * 16);
        const float ksc = __builtin_amdgcn_exp2f(fmaxf(fmaxf(Mw.x, Mw.y), fmaxf(Mw.z, Mw.w)) - 8.f);
        const float dk = bf2f(dsk);
        char* sdst = p.ws + WS_S + (size_t)(ch0 + 8 * cl) * 4096 + ra * 2048;
        LAS char* lin = sm + L_XCH + wid * 1024;
#pragma unroll
        for (int cp = 0; cp < 2; cp++) {
#pragma unroll
            for (int cc = 0; cc < 2; cc++) {
                const int c = 2 * cp + cc;
                const f16x4 uu = *(LAS f16x4*)(sm + L_UC + cl * UC_CH + (32 + 32 * (16 * c + a) + 16 * ra + 4 * q) * 2);
                u32x2 o = {pkbf(fmaf(y[c][0], ksc, dk * (float)uu[0]), fmaf(y[c][1], ksc, dk * (float)uu[1])),
                           pkbf(fmaf(y[c][2], ksc, dk * (float)uu[2]), fmaf(y[c][3], ksc, dk * (float)uu[3]))};
                *(LAS u32x2*)(lin + (16 * cc + a) * 32 + q * 8) = o;
            }
            WAITL0();
            u32x4 v = *(LAS u32x4*)(lin + lane * 16);
            asm volatile("s_waitcnt lgkmcnt(0)" : "+v"(v) :: "memory");
            astore_b128_sc1(sdst, (u32)(cp * 1024 + lane * 16), v);
        }
        WAITV(0);
        u32 arr = 0;
        if (lane == 0) arr = __hip_atomic_fetch_add((LAS u32*)(sm + L_MISC + 64), 1u, __ATOMIC_RELAXED, __HIP_MEMORY_SCOPE_WORKGROUP);
        arr = __builtin_amdgcn_readfirstlane(arr);
        if (arr == 3 && lane == 0) astore_b64_sc1(flags, (u32)(FL_S + b) * 128u, tag1);
        ST(8);
        poll_flags(flags, FL_S + dt, 8, 32, tag1, lane);
        for (int spin = 0; spin < (1 << 20); ++spin) {
            if (__builtin_amdgcn_readfirstlane(__hip_atomic_load((LAS u32*)(sm + L_MISC + 64), __ATOMIC_RELAXED, __HIP_MEMORY_SCOPE_WORKGROUP)) >= 4u) break;
            __builtin_amdgcn_s_sleep(1);
        }
        u32x4 sv[2];
#pragma unroll
        for (int i = 0; i < 2; i++) {
            const int d = wid * 16 + i * 8 + (lane >> 3), pc = lane & 7;
            sv[i] = aload_b128_sc1(p.ws + WS_S, (u32)((dt * 64 + d) * 4096 + (pc >> 2) * 2048 + tt * 64 + (pc & 3) * 16));
        }
        asm volatile("s_waitcnt vmcnt(0)" : "+v"(sv[0]), "+v"(sv[1]) :: "memory");
#pragma unroll
        for (int i = 0; i < 2; i++) {
            const int d = wid * 16 + i * 8 + (lane >> 3), pc = lane & 7;
            const int chunk = ((pc >> 1) & 1) * 4 + (pc >> 2) * 2 + (pc & 1);
            *(LAS u32x4*)(sm + L_UC + d * 128 + ((chunk ^ (d >> 3)) << 4)) = sv[i];
        }
        ST(9);
    } else {
#pragma unroll
        for (int s = 0; s < 14; s++) { wreg[s][0] = w2frag(s, 0); wreg[s][1] = w2frag(s, 1); }
#pragma unroll
        for (int s = 0; s < 14; s++) asm volatile("" : "+v"(wreg[s][0]), "+v"(wreg[s][1]));
        WAITL0();
#pragma unroll 1
        for (int jj = 0; jj < 28; jj++) w2_dma(p, lds0, 28 * kq + jj, 1, voffw2);
        ST(7);
        if (PH == 3) poll_flags(flags, FL_H + (tt >> 1) * 16, 1, 14, tag1, lane);
        WAITV(0);
        ST(8);
        const __amdgpu_buffer_rsrc_t hr = __builtin_amdgcn_make_buffer_rsrc(p.ws + WS_H + (size_t)(tt * 2 * 112 + kq * 28) * 1024, 0, 2 * 112 * 1024, 0x00020000);
        constexpr int WIN = 7;
        u32x4 hf[WIN][2];
        f32x16 acc[2][2] = {};
#pragma unroll
        for (int s = 0; s < WIN; s++) {
            hf[s][0] = __builtin_amdgcn_raw_buffer_load_b128(hr, lane * 16, s * 1024, 16);
            hf[s][1] = __builtin_amdgcn_raw_buffer_load_b128(hr, lane * 16 + 112 * 1024, s * 1024, 16);
        }
        SCHED();
#pragma unroll
        for (int s = 0; s < 28; s++) {
            u32x4 w0, w1;
            SC(32 + s);
            if (s < 14) { w0 = wreg[s][0]; w1 = wreg[s][1]; } else { w0 = w2frag(s - 14, 0); w1 = w2frag(s - 14, 1); }
#pragma unroll
            for (int j = 0; j < 2; j++) {
                acc[0][j] = __builtin_amdgcn_mfma_f32_32x32x16_bf16(__builtin_bit_cast(bf16x8, w0), __builtin_bit_cast(bf16x8, hf[s % WIN][j]), acc[0][j], 0, 0, 0);
                acc[1][j] = __builtin_amdgcn_mfma_f32_32x32x16_bf16(__builtin_bit_cast(bf16x8, w1), __builtin_bit_cast(bf16x8, hf[s % WIN][j]), acc[1][j], 0, 0, 0);
            }
            if (s + WIN < 28) {
                hf[s % WIN][0] = __builtin_amdgcn_raw_buffer_load_b128(hr, lane * 16, (s + WIN) * 1024, 16);
                hf[s % WIN][1] = __builtin_amdgcn_raw_buffer_load_b128(hr, lane * 16 + 112 * 1024, (s + WIN) * 1024, 16);
            }
            if (s + WIN == 28) { xe = *(const u32x4*)((const char*)p.x + xo); b2e = *(const u32x4*)((const char*)p.b2 + dt * 128 + (tid & 7) * 16); }
            SCHED();
        }
        ST(9);
#pragma unroll
        for (int j2 = 0; j2 < 2; j2++)
#pragma unroll
            for (int j = 0; j < 2; j++)
#pragma unroll
                for (int rg = 0; rg < 4; rg++) {
                    f32x4 v = {acc[j2][j][4 * rg], acc[j2][j][4 * rg + 1], acc[j2][j][4 * rg + 2], acc[j2][j][4 * rg + 3]};
                    *(LAS f32x4*)(sm + L_W2 + kq * W2_REG + (32 * j + l31) * 272 + (32 * j2 + 8 * rg + 4 * h) * 4) = v;
                }
    }
    BAR();
    ST(10);
    {
        const int tok = tid >> 3, dg = tid & 7;
        float m[8];
#pragma unroll
        for (int e = 0; e < 4; e++) { m[2 * e] = bflo(b2e[e]); m[2 * e + 1] = bfhi(b2e[e]); }
#pragma unroll
        for (int i = 0; i < 4; i++) {
            const f32x4 lo = *(LAS f32x4*)(sm + L_W2 + i * W2_REG + tok * 272 + dg * 32), hi = *(LAS f32x4*)(sm + L_W2 + i * W2_REG + tok * 272 + dg * 32 + 16);
#pragma unroll
            for (int e = 0; e < 4; e++) { m[e] += lo[e]; m[4 + e] += hi[e]; }
        }
        float o[8];
#pragma unroll
        for (int e = 0; e < 8; e++) {
            const float s = bf2f(*(LAS u16*)(sm + L_UC + (8 * dg + e) * 128 + (((tok >> 3) ^ dg) << 4) + (tok & 7) * 2));
            const float gt = m[e] * __builtin_amdgcn_rcpf(1.f + __builtin_amdgcn_exp2f(-1.4426950408889634f * m[e]));
            const float xv = (e & 1) ? bfhi(xe[e >> 1]) : bflo(xe[e >> 1]);
            o[e] = fmaf(s, gt, xv);
        }
        u32x4 ov = {pkbf(o[0], o[1]), pkbf(o[2], o[3]), pkbf(o[4], o[5]), pkbf(o[6], o[7])};
        *(u32x4*)((char*)p.out + xo) = ov;
    }
    if (PH == 3 && b == 0 && wid == 0) {
#pragma unroll 1
        for (int i = 0; i < 4; i++) poll_flags(flags, FL_S + 64 * i, 1, 64, tag1, lane);
        if (lane == 0) astore_b64_sc1(flags, FL_E * 128, tag1);
    }
    if (PADWG < 0 || b == PADWG) { while (__builtin_amdgcn_s_memrealtime() < t_start + 5000) __builtin_amdgcn_s_sleep(1); }
    ST(11);
}

extern "C" void kernel_launch(void* const* d_in, const int* in_sizes, int n_in, void* d_out, int out_size, void* d_ws, size_t ws_size,
                              hipStream_t stream) {
    Params p;
    p.x = (const u16*)d_in[0]; p.Are = (const u16*)d_in[1]; p.Aim = (const u16*)d_in[2]; p.Bre = (const u16*)d_in[3]; p.Bim = (const u16*)d_in[4];
    p.Cre = (const u16*)d_in[5]; p.Cim = (const u16*)d_in[6]; p.logdt = (const u16*)d_in[7]; p.Dskip = (const u16*)d_in[8];
    p.w1 = (const u16*)d_in[10]; p.b1 = (const u16*)d_in[11]; p.w2 = (const u16*)d_in[12]; p.b2 = (const u16*)d_in[13];
    p.out = (u16*)d_out; p.ws = (char*)d_ws;
    fused_kernel<3><<<256, 512, 0, stream>>>(p);
}
```

```cpp
#define PADWG 0
#define EXPMODE 0
#define PADT 4000
#include <hip/hip_runtime.h>
#include <stdint.h>

#define FINE 0
#define SPLIT 0

typedef unsigned short u16;
typedef unsigned u32;
typedef unsigned long long u64;
typedef u32 u32x2 __attribute__((ext_vector_type(2)));
typedef u32 u32x4 __attribute__((ext_vector_type(4)));
typedef short s16x4 __attribute__((ext_vector_type(4)));
typedef __bf16 bf16x2 __attribute__((ext_vector_type(2)));
typedef __bf16 bf16x8 __attribute__((ext_vector_type(8)));
typedef _Float16 f16x2 __attribute__((ext_vector_type(2)));
typedef _Float16 f16x4 __attribute__((ext_vector_type(4)));
typedef _Float16 f16x8 __attribute__((ext_vector_type(8)));
typedef float f32x2 __attribute__((ext_vector_type(2)));
typedef float f32x4 __attribute__((ext_vector_type(4)));
typedef float f32x16 __attribute__((ext_vector_type(16)));
#define LAS __attribute__((address_space(3)))
#define GAS __attribute__((address_space(1)))
#define DEV __device__ __forceinline__

constexpr size_t WS_H = 0;
constexpr size_t WS_U = 8u << 20;
constexpr size_t WS_S = 10u << 20;
constexpr size_t WS_FLAG = 12u << 20;
constexpr size_t WS_KG = 13u << 20;
constexpr size_t WS_STAMP = 20u << 20;
constexpr int FL_LN = 0, FL_H = 32, FL_S = 288, FL_E = 576;

constexpr int L_W2 = 0, W2_REG = 28672;
constexpr int L_UC = 114688, UC_COPY = 4160, UC_CH = 4 * UC_COPY;
constexpr int L_G = 131072;
constexpr int L_PAR = 147456;
constexpr int L_XCH = 147968;
constexpr int L_MISC = 152064;
constexpr int L_KP = 153600, KP_PLANE = 1280, KP_CH = 5120;

struct Params {
    const u16 *x, *Are, *Aim, *Bre, *Bim, *Cre, *Cim, *logdt, *Dskip, *w1, *b1, *w2, *b2;
    u16* out;
    char* ws;
};

DEV float bf2f(u32 v) { return __uint_as_float(v << 16); }
DEV float bflo(u32 v) { return __uint_as_float(v << 16); }
DEV float bfhi(u32 v) { return __uint_as_float(v & 0xffff0000u); }
DEV u32 pkbf(float a, float b) { f32x2 f = {a, b}; return __builtin_bit_cast(u32, __builtin_convertvector(f, bf16x2)); }
DEV u32 pkh(float a, float b) { f32x2 f = {a, b}; return __builtin_bit_cast(u32, __builtin_convertvector(f, f16x2)); }
DEV float rbf(float a) { return bflo(pkbf(a, a)); }
DEV float dot2(u32 a, u32 b, float c) { return __builtin_amdgcn_fdot2_f32_bf16(__builtin_bit_cast(bf16x2, a), __builtin_bit_cast(bf16x2, b), c, false); }

#define WAITV(n) asm volatile("s_waitcnt vmcnt(" #n ")" ::: "memory")
#define WAITL0() asm volatile("s_waitcnt lgkmcnt(0)" ::: "memory")
#define BAR() do { asm volatile("s_waitcnt lgkmcnt(0)" ::: "memory"); __builtin_amdgcn_s_barrier(); asm volatile("" ::: "memory"); } while (0)
#define SCHED() __builtin_amdgcn_sched_barrier(0)

DEV const void* uni(const void* q) {
    const u64 a = (u64)q;
    return (const void*)(((u64)(u32)__builtin_amdgcn_readfirstlane((u32)(a >> 32)) << 32) | (u32)__builtin_amdgcn_readfirstlane((u32)a));
}
DEV void glds(const void* base, u32 voff, u32 dst) {
    u32 keep;
    dst = __builtin_amdgcn_readfirstlane(dst);
    base = uni(base);
    asm volatile("s_mov_b32 %0, m0\n\ts_mov_b32 m0, %3\n\ts_nop 2\n\tglobal_load_lds_dwordx4 %1, %2\n\ts_mov_b32 m0, %0"
                 : "=&s"(keep) : "v"(voff), "s"(base), "s"(dst) : "memory");
}
DEV u32 aload_u16(const void* base, u32 voff) { u32 r; asm volatile("global_load_ushort %0, %1, %2" : "=v"(r) : "v"(voff), "s"(base) : "memory"); return r; }
DEV u32x2 aload_b64_sc1(const void* base, u32 voff) { u32x2 r; asm volatile("global_load_dwordx2 %0, %1, %2 sc1" : "=v"(r) : "v"(voff), "s"(base) : "memory"); return r; }
DEV u32x4 aload_b128_sc1(const void* base, u32 voff) { u32x4 r; asm volatile("global_load_dwordx4 %0, %1, %2 sc1" : "=v"(r) : "v"(voff), "s"(base) : "memory"); return r; }
DEV u32x4 aload_b128(const void* base, u32 voff) { u32x4 r; asm volatile("global_load_dwordx4 %0, %1, %2" : "=v"(r) : "v"(voff), "s"(base) : "memory"); return r; }
DEV void astore_b128_sc1(void* base, u32 voff, u32x4 v) { asm volatile("global_store_dwordx4 %0, %1, %2 sc1\n\ts_nop 1" :: "v"(voff), "v"(v), "s"(base) : "memory"); }
DEV void astore_b64_sc1(void* base, u32 voff, u32x2 v) { asm volatile("global_store_dwordx2 %0, %1, %2 sc1\n\ts_nop 1" :: "v"(voff), "v"(v), "s"(base) : "memory"); }

#define ST(i)
#define SC(i)

DEV void poll_flags(const char* flags, int first, int stride, int nfl, u32x2 tag, int lane) {
    const u32 off = (u32)(first + (lane < nfl ? lane : 0) * stride) * 128u;
    for (int spin = 0; spin < (1 << 16); ++spin) {
        u32x2 v = aload_b64_sc1(flags, off);
        asm volatile("s_waitcnt vmcnt(0)" : "+v"(v) :: "memory");
        if (__all(v.x == tag.x && v.y == tag.y)) break;
        __builtin_amdgcn_s_sleep(1);
    }
}

DEV float wave_max(float v) {
#pragma unroll
    for (int o = 16; o; o >>= 1) v = fmaxf(v, __shfl_xor(v, o));
    return v;
}

DEV float kgen_params(LAS char* sm, int wid, int lane, u32 are, u32 aim, u32 bre, u32 bim, u32 cre, u32 cim, u32 ldt) {
    const float dt = rbf(__expf(bf2f(ldt)));
    const float a2 = dt * bf2f(are) * 1.4426950408889634f;
    const float rev = dt * bf2f(aim) * 0.15915494309189535f;
    const float br = bf2f(bre), bi = bf2f(bim), cr = bf2f(cre), ci = bf2f(cim);
    const float wr = cr * br - ci * bi, wi = cr * bi + ci * br;
    const float lw = 0.5f * (__log2f(cr * cr + ci * ci) + __log2f(br * br + bi * bi)) + __log2f(dt);
    const float pw = atan2f(wi, wr) * 0.15915494309189535f;
    float M = wave_max(lw + fmaxf(a2, 0.f) * 2048.f);
    M = fminf(fmaxf(ceilf(M), -110.f), 110.f);
    f32x4 e = {a2, rev, lw - M + 8.f, pw};
    if (lane < 32) *(LAS f32x4*)(sm + L_PAR + wid * 512 + lane * 16) = e;
    if (lane == 0) *(LAS float*)(sm + L_MISC + wid * 4) = M;
    return M;
}
DEV void kgen_step(LAS char* sm, int wid, int lane, int ks, float offA, float offB, f32x16& G) {
    u32x4 A, B;
#pragma unroll
    for (int j = 0; j < 4; j++) {
        const f32x4 e = *(LAS f32x4*)(sm + L_PAR + wid * 512 + (ks * 8 + (lane >> 5) * 4 + j) * 16);
        const float ma = __builtin_amdgcn_exp2f(fmaf(e.x, offA, e.z));
        const float pa = __builtin_amdgcn_fractf(fmaf(e.y, offA, e.w));
        A[j] = pkh(ma * __builtin_amdgcn_cosf(pa), -ma * __builtin_amdgcn_sinf(pa));
        const float mb = __builtin_amdgcn_exp2f(e.x * offB);
        const float pb = __builtin_amdgcn_fractf(e.y * offB);
        B[j] = pkh(mb * __builtin_amdgcn_cosf(pb), mb * __builtin_amdgcn_sinf(pb));
    }
    G = __builtin_amdgcn_mfma_f32_32x32x16_f16(__builtin_bit_cast(f16x8, A), __builtin_bit_cast(f16x8, B), G, 0, 0, 0);
}
DEV float kgen_offA(int lane) {
    const int rho = lane & 31;
    const int slot = 16 * ((rho >> 2) & 1) + (rho & 3) + 4 * (rho >> 3);
    const int u = slot >> 2, e = slot & 3;
    const int o0 = slot, o1 = 256 + 2 * (slot - 8), o2 = 512 + 256 * (u - 4) + 4 * e, o3 = 1024 + 512 * (u - 6) + 256 * (e >> 1) + 8 * (e & 1);
    const int lo = u < 2 ? o0 : o1, hi = u < 6 ? o2 : o3;
    return (float)(u < 4 ? lo : hi);
}
DEV void kgen_write(LAS char* sm, int wid, int lane, const f32x16& G) {
    u32x4 a = {pkh(G[0], G[1]), pkh(G[2], G[3]), pkh(G[4], G[5]), pkh(G[6], G[7])};
    u32x4 b = {pkh(G[8], G[9]), pkh(G[10], G[11]), pkh(G[12], G[13]), pkh(G[14], G[15])};
    LAS char* d = sm + L_G + wid * 2048 + (lane & 31) * 64 + (lane >> 5) * 32;
    *(LAS u32x4*)d = a;
    *(LAS u32x4*)(d + 16) = b;
}
DEV void kgen_expand(LAS char* sm, int wid, int lane) {
    const int cl = wid >> 2, ty = wid & 3, c = lane & 31, hb = lane >> 5;
    const f32x4 Mw = *(LAS f32x4*)(sm + L_MISC + cl * 16);
    const float M = fmaxf(fmaxf(Mw.x, Mw.y), fmaxf(Mw.z, Mw.w));
    float sc[4];
#pragma unroll
    for (int i = 0; i < 4; i++) sc[i] = __builtin_amdgcn_exp2f(Mw[i] - M);
    LAS char* g = sm + L_G + cl * 8192 + c * 64;
    float o[8];
    if (ty == 0) {
        float v[8] = {0, 0, 0, 0, 0, 0, 0, 0};
#pragma unroll
        for (int i = 0; i < 4; i++) {
            const f16x8 t = *(LAS f16x8*)(g + i * 2048 + hb * 16);
#pragma unroll
            for (int e = 0; e < 8; e++) v[e] = fmaf((float)t[e], sc[i], v[e]);
        }
        o[0] = v[0]; o[1] = hb ? v[0] : v[1]; o[2] = hb ? v[1] : v[2]; o[3] = hb ? v[2] : v[3];
        o[4] = hb ? v[2] : v[4]; o[5] = hb ? v[2] : v[5]; o[6] = hb ? v[3] : v[6]; o[7] = hb ? v[4] : v[7];
    } else if (ty == 1) {
        float v[3] = {0, 0, 0};
#pragma unroll
        for (int i = 0; i < 4; i++) {
            const f16x4 t = *(LAS f16x4*)(g + i * 2048 + 32 + hb * 8);
#pragma unroll
            for (int e = 0; e < 3; e++) v[e] = fmaf((float)t[e], sc[i], v[e]);
        }
        o[0] = o[1] = o[2] = v[0]; o[3] = o[4] = o[5] = v[1]; o[6] = o[7] = v[2];
    } else {
        float v[2] = {0, 0};
#pragma unroll
        for (int i = 0; i < 4; i++) {
            const f16x2 t = *(LAS f16x2*)(g + i * 2048 + 48 + (ty - 2) * 8 + hb * 4);
#pragma unroll
            for (int e = 0; e < 2; e++) v[e] = fmaf((float)t[e], sc[i], v[e]);
        }
        o[0] = o[1] = o[2] = o[3] = v[0]; o[4] = (c & 1) ? v[1] : v[0]; o[5] = o[6] = o[7] = v[1];
    }
    const int grp = ty * 64 + lane, m = grp >> 2, q = 3 - (grp & 3);
    u32x4 w = {pkh(o[7], o[6]), pkh(o[5], o[4]), pkh(o[3], o[2]), pkh(o[1], o[0])};
    *(LAS u32x4*)(sm + L_KP + cl * KP_CH + q * KP_PLANE + (16 + m) * 16) = w;
    if (ty == 1) { u32x4 z = {0, 0, 0, 0}; *(LAS u32x4*)(sm + L_KP + cl * KP_CH + (lane >> 4) * KP_PLANE + (lane & 15) * 16) = z; }
}

DEV void w2_dma(const Params& p, u32 lds0, int i, int half, u32 voffw2) {
    const int kq = i / 28, j = i - kq * 28;
    const char* src = (const char*)p.w2 + (size_t)(448 * kq + 224 * half + 8 * j) * 1024;
    glds(src, voffw2, lds0 + L_W2 + i * 1024);
}

DEV void build_copies(LAS char* sm, int tid, int lane, u32x4 v, u32x2 pv) {
    u32 p2 = __shfl_up(v.z, 2), p3 = __shfl_up(v.w, 2);
    if (lane < 2) { p2 = pv.x; p3 = pv.y; }
    if (tid < 2) { p2 = 0; p3 = 0; }
    LAS char* d = sm + L_UC + (tid & 1) * UC_CH + ((tid >> 1) + 4) * 16;
    const u32 a0 = __builtin_amdgcn_alignbit(p3, p2, 16), a1 = __builtin_amdgcn_alignbit(v.x, p3, 16), a2 = __builtin_amdgcn_alignbit(v.y, v.x, 16),
              a3 = __builtin_amdgcn_alignbit(v.z, v.y, 16), a4 = __builtin_amdgcn_alignbit(v.w, v.z, 16);
    *(LAS u32x4*)d = v;
    u32x4 c1 = {a1, a2, a3, a4}, c2 = {p3, v.x, v.y, v.z}, c3 = {a0, a1, a2, a3};
    *(LAS u32x4*)(d + UC_COPY) = c1;
    *(LAS u32x4*)(d + 2 * UC_COPY) = c2;
    *(LAS u32x4*)(d + 3 * UC_COPY) = c3;
    if (tid < 32) { u32x4 z = {0, 0, 0, 0}; *(LAS u32x4*)(sm + L_UC + (tid >> 2) * UC_COPY + (tid & 3) * 16) = z; }
}

template <int PH>
__global__ __launch_bounds__(512, 2) void fused_kernel(Params p) {
    __shared__ __attribute__((aligned(1024))) char shm[163840];
    LAS char* sm = (LAS char*)shm;
    const u32 lds0 = (u32)(uintptr_t)sm;
    const int tid = threadIdx.x, lane = tid & 63, wid = __builtin_amdgcn_readfirstlane(tid >> 6);
    const int b = blockIdx.x, g = b & 7, r = b >> 3;
    const int h = lane >> 5, l31 = lane & 31;
    char* flags = p.ws + WS_FLAG;
    const int ch0 = 64 * g + 16 * (r & 3) + (r >> 2);
    const int tt = g * 4 + (r >> 3), dt = r & 7;
    const u32 voffw2 = (u32)((lane >> 3) * 1024 + dt * 128 + (((lane & 7) ^ (((lane >> 4) & 1) << 2)) << 4));
    u64 t_start = __builtin_amdgcn_s_memrealtime();
    ST(0);

    u32x2 tag = {0u, 0u};
    if (tid == 0) *(LAS u32*)(sm + L_MISC + 64) = 0;
    bool u_done = false;
    const u32 uoff = (u32)((tid >> 1) * 8192 + (r >> 2) * 1024 + (8 * g + 2 * (r & 3) + (tid & 1)) * 16);

    if (PH & 1) {
        const int chn = ch0 + 8 * (wid >> 2);
        const u32 po = (u32)(chn * 128 + (wid & 3) * 32 + l31) * 2u;
#define LOAD_PARAMS() \
        u32 are = aload_u16(p.Are, po), aim = aload_u16(p.Aim, po), bre = aload_u16(p.Bre, po), bim = aload_u16(p.Bim, po), \
            cre = aload_u16(p.Cre, po), cim = aload_u16(p.Cim, po), ldt = aload_u16(p.logdt, (u32)chn * 2u); \
        u32x2 tg0 = aload_b64_sc1(flags, FL_E * 128);
        const float offA = kgen_offA(lane), offB = (float)(8 * l31);
        f32x16 G = {};
        if (PH == 1 && tid == 0) { u32x2 z = {0u, 0u}; astore_b64_sc1(flags, (FL_S + b) * 128, z); }
        if (r < 28) {
            const int mt = (g >> 1) * 4 + r / 7, ft = (g & 1) * 7 + r % 7;
            const int tg = wid & 3, fh = wid >> 2;
            LOAD_PARAMS();
            u32 b1a = aload_u16(p.b1, (u32)(ft * 128 + fh * 64 + l31) * 2u), b1b = aload_u16(p.b1, (u32)(ft * 128 + fh * 64 + 32 + l31) * 2u);
            const char* xs = (const char*)p.x + (size_t)mt * 128 * 1024;
            const char* wsrc = (const char*)p.w1 + ft * 256;
            u32 vx[2], vw[2];
#pragma unroll
            for (int jj = 0; jj < 2; jj++) {
                vx[jj] = (u32)((16 * wid + 8 * jj + (lane >> 3)) * 1024 + (((lane & 7) ^ (4 * jj + (lane >> 4))) << 4));
                vw[jj] = (u32)((8 * wid + 4 * jj + (lane >> 4)) * 3584 + (((lane & 15) ^ (((lane >> 4) << 2) | ((2 * wid + jj) & 3))) << 4));
            }
            auto stage = [&](int kc) {
                const u32 d = lds0 + (kc & 3) * 32768 + wid * 2048;
                const char* xk = xs + kc * 128;
                const char* wk = wsrc + (size_t)kc * 64 * 3584;
                glds(xk, vx[0], d); glds(xk, vx[1], d + 1024);
                glds(wk, vw[0], d + 16384); glds(wk, vw[1], d + 16384 + 1024);
            };
            stage(0); stage(1); stage(2); stage(3);
            asm volatile("s_waitcnt vmcnt(16)" : "+v"(are), "+v"(aim), "+v"(bre), "+v"(bim), "+v"(cre), "+v"(cim), "+v"(ldt), "+v"(b1a), "+v"(b1b), "+v"(tg0) :: "memory");
            if (PH == 3) { tag.x = __builtin_amdgcn_readfirstlane(tg0.x); tag.y = __builtin_amdgcn_readfirstlane(tg0.y); }
            kgen_params(sm, wid, lane, are, aim, bre, bim, cre, cim, ldt);
            ST(1);
            const u32 xa = (u32)((32 * tg + l31) * 128 + ((h ^ ((l31 >> 1) & 7)) << 4));
            u32 wa[2][2];
            {
                const int blk = (lane >> 4) & 1, q = (lane & 15) >> 2, pp = lane & 3;
#pragma unroll
                for (int j2 = 0; j2 < 2; j2++)
#pragma unroll
                    for (int t = 0; t < 2; t++)
                        wa[j2][t] = (u32)(16384 + 256 * (8 * h + 4 * t + q) + (((4 * (2 * fh + j2) + 2 * blk + (pp >> 1)) ^ ((q << 2) | (2 * h + t))) << 4) + 8 * (pp & 1));
            }
            f32x16 acc[2] = {};
            float s1 = 0.f, s2 = 0.f, cs[2] = {0.f, 0.f};
            const u32 ones = 0x3f803f80u;
            auto chunk = [&](int kc) {
                LAS char* sl = sm + (kc & 3) * 32768;
                u32x4 xf[4]; u32x4 wf[4][2];
#pragma unroll
                for (int s = 0; s < 4; s++) {
                    xf[s] = *(LAS u32x4*)(sl + (xa ^ (u32)(s << 5)));
#pragma unroll
                    for (int j2 = 0; j2 < 2; j2++) {
                        const u32x2 lo = __builtin_bit_cast(u32x2, __builtin_amdgcn_ds_read_tr16_b64_v4i16((LAS s16x4*)(sl + wa[j2][0] + s * 4096)));
                        const u32x2 hi = __builtin_bit_cast(u32x2, __builtin_amdgcn_ds_read_tr16_b64_v4i16((LAS s16x4*)(sl + wa[j2][1] + s * 4096)));
                        wf[s][j2] = (u32x4){lo.x, lo.y, hi.x, hi.y};
                    }
                }
#pragma unroll
                for (int s = 0; s < 4; s++)
#pragma unroll
                    for (int j2 = 0; j2 < 2; j2++)
                        acc[j2] = __builtin_amdgcn_mfma_f32_32x32x16_bf16(__builtin_bit_cast(bf16x8, wf[s][j2]), __builtin_bit_cast(bf16x8, xf[s]), acc[j2], 0, 0, 0);
                if ((kc & 1) == fh) {
#pragma unroll
                    for (int s = 0; s < 4; s++)
#pragma unroll
                        for (int i = 0; i < 4; i++) { s1 = dot2(xf[s][i], ones, s1); s2 = dot2(xf[s][i], xf[s][i], s2); }
                }
                if ((kc & 3) == ((tg + 2 * fh + 1) & 3)) {
#pragma unroll
                    for (int s = 0; s < 4; s++)
#pragma unroll
                        for (int i = 0; i < 4; i++) { cs[0] = dot2(wf[s][0][i], ones, cs[0]); cs[1] = dot2(wf[s][1][i], ones, cs[1]); }
                }
            };
#pragma unroll 1
            for (int kc = 0; kc < 4; kc++) {
                SC(kc * 4);
                if (kc == 0) WAITV(12); else WAITV(8);
                BAR();
                SC(kc * 4 + 1);
                if (kc > 0) stage(kc + 3);
                SC(kc * 4 + 2);
                chunk(kc);
                SC(kc * 4 + 3);
                kgen_step(sm, wid, lane, kc, offA, offB, G);
            }
            ST(2);
#pragma unroll 1
            for (int kc = 4; kc < 8; kc++) {
                SC(kc * 4);
                if (PH == 3 || kc < 6) WAITV(8); else if (kc == 6) WAITV(4); else WAITV(0);
                BAR();
                SC(kc * 4 + 1);
                if (kc == 4) { stage(7); kgen_write(sm, wid, lane, G); }
                else if (PH == 3) {
#pragma unroll
                    for (int jj = 0; jj < 4; jj++) w2_dma(p, lds0, (kc - 5) * 32 + 4 * wid + jj, 0, voffw2);
                }
                SC(kc * 4 + 2);
                chunk(kc);
                SC(kc * 4 + 3);
                if (kc == 5) kgen_expand(sm, wid, lane);
            }
            s1 += __shfl_xor(s1, 32); s2 += __shfl_xor(s2, 32); cs[0] += __shfl_xor(cs[0], 32); cs[1] += __shfl_xor(cs[1], 32);
            {
                f32x2 e = h ? (f32x2){cs[0], cs[1]} : (f32x2){s1, s2};
                *(LAS f32x2*)(sm + L_XCH + h * 2048 + wid * 256 + l31 * 8) = e;
            }
            BAR();
            ST(3);
            u32x2 fl;
            if (PH == 3) {
                fl = aload_b64_sc1(flags, (u32)(FL_LN + l31) * 128u);
                w2_dma(p, lds0, 96 + 2 * wid, 0, voffw2); w2_dma(p, lds0, 97 + 2 * wid, 0, voffw2);
            }
            float rr, rinv;
            {
                const f32x2 o = *(LAS f32x2*)(sm + L_XCH + (wid ^ 4) * 256 + l31 * 8);
                s1 += o.x; s2 += o.y;
                float c0 = 0.f, c1 = 0.f;
#pragma unroll
                for (int i = 0; i < 4; i++) { const f32x2 e = *(LAS f32x2*)(sm + L_XCH + 2048 + (fh * 4 + i) * 256 + l31 * 8); c0 += e.x; c1 += e.y; }
                const float m = s1 * (1.f / 512.f);
                const float v1 = fmaxf(s2 * (1.f / 512.f) - m * m, 0.f);
                const float q1 = v1 + 1e-5f;
                const float q2 = v1 / q1 + 1e-5f;
                rinv = sqrtf(q1 * q2); rr = 1.f / rinv;
                const float nm = -m, mh = rbf(nm), ml = nm - mh, ih = rbf(rinv), il = rinv - ih;
                const u32 mm = pkbf(mh, ml);
                u32x4 Bx = {mm, mm, pkbf(ih, il), 0u};
                if (h) Bx = (u32x4){0u, 0u, 0u, 0u};
                const float c0h = rbf(c0), c1h = rbf(c1);
                u32x4 A0 = {pkbf(c0h, c0h), pkbf(c0 - c0h, c0 - c0h), b1a | (b1a << 16), 0u};
                u32x4 A1 = {pkbf(c1h, c1h), pkbf(c1 - c1h, c1 - c1h), b1b | (b1b << 16), 0u};
                acc[0] = __builtin_amdgcn_mfma_f32_32x32x16_bf16(__builtin_bit_cast(bf16x8, A0), __builtin_bit_cast(bf16x8, Bx), acc[0], 0, 0, 0);
                acc[1] = __builtin_amdgcn_mfma_f32_32x32x16_bf16(__builtin_bit_cast(bf16x8, A1), __builtin_bit_cast(bf16x8, Bx), acc[1], 0, 0, 0);
            }
            const float K1 = -2.302208198144325f * rr, K2 = -0.10294323958002349f * rr * rr * rr;
            char* hdst = p.ws + WS_H + (size_t)((mt * 4 + tg) * 112 + ft * 8 + fh * 4) * 1024;
            u32x4 uv; u32x2 upv;
#pragma unroll
            for (int j2 = 0; j2 < 2; j2++) {
                float gl[16];
#pragma unroll
                for (int i = 0; i < 16; i++) {
                    const float u = acc[j2][i];
                    const float e = __builtin_amdgcn_exp2f(fmaf(u * u, K2, K1) * u);
                    gl[i] = u * __builtin_amdgcn_rcpf(fmaf(e, rinv, rinv));
                }
                if (PH == 3 && j2 == 0) {
                    asm volatile("s_waitcnt vmcnt(2)" : "+v"(fl) :: "memory");
                    u_done = __all(fl.x == tag.x + 1u && fl.y == tag.y);
                    const char* ub = u_done ? (const char*)p.ws + WS_U : (const char*)p.x;
                    uv = aload_b128_sc1(ub, uoff);
                    upv = aload_b64_sc1(ub, tid >= 2 ? uoff - 8192 + 8 : uoff);
                }
#pragma unroll
                for (int s = 0; s < 2; s++) {
                    u32x4 o = {pkbf(gl[8 * s], gl[8 * s + 1]), pkbf(gl[8 * s + 2], gl[8 * s + 3]), pkbf(gl[8 * s + 4], gl[8 * s + 5]), pkbf(gl[8 * s + 6], gl[8 * s + 7])};
                    astore_b128_sc1(hdst, (u32)((j2 * 2 + s) * 1024 + lane * 16), o);
                }
            }
            if (PH == 3) {
                asm volatile("s_waitcnt vmcnt(4)" : "+v"(uv), "+v"(upv) :: "memory");
                ST(4);
                if (u_done) build_copies(sm, tid, lane, uv, upv);
                WAITV(0);
                BAR();
                if (tid == 0) { u32x2 t1 = {tag.x + 1u, tag.y}; astore_b64_sc1(flags, (u32)(FL_H + mt * 16 + ft) * 128u, t1); }
            }
            ST(5);
        } else {
            const int tb = (g * 4 + (r - 28)) * 8 + wid;
            u32x4 xv[8];
#pragma unroll
            for (int i = 0; i < 8; i++) xv[i] = aload_b128((const char*)p.x + (size_t)tb * 8192, (u32)(i * 1024 + lane * 16));
            LOAD_PARAMS();
            asm volatile("s_waitcnt vmcnt(0)" : "+v"(xv[0]), "+v"(xv[1]), "+v"(xv[2]), "+v"(xv[3]), "+v"(xv[4]), "+v"(xv[5]), "+v"(xv[6]), "+v"(xv[7]),
                         "+v"(are), "+v"(aim), "+v"(bre), "+v"(bim), "+v"(cre), "+v"(cim), "+v"(ldt), "+v"(tg0) :: "memory");
            if (PH == 3) { tag.x = __builtin_amdgcn_readfirstlane(tg0.x); tag.y = __builtin_amdgcn_readfirstlane(tg0.y); }
            float mean[8], rs[8];
#pragma unroll
            for (int i = 0; i < 8; i++) {
                float s = 0.f;
#pragma unroll
                for (int j = 0; j < 4; j++) s += bflo(xv[i][j]) + bfhi(xv[i][j]);
                mean[i] = s;
            }
#pragma unroll
            for (int o = 32; o; o >>= 1)
#pragma unroll
                for (int i = 0; i < 8; i++) mean[i] += __shfl_xor(mean[i], o);
#pragma unroll
            for (int i = 0; i < 8; i++) {
                mean[i] *= (1.f / 512.f);
                float s = 0.f;
#pragma unroll
                for (int j = 0; j < 4; j++) { const float a = bflo(xv[i][j]) - mean[i], c = bfhi(xv[i][j]) - mean[i]; s = fmaf(a, a, fmaf(c, c, s)); }
                rs[i] = s;
            }
#pragma unroll
            for (int o = 32; o; o >>= 1)
#pragma unroll
                for (int i = 0; i < 8; i++) rs[i] += __shfl_xor(rs[i], o);
#pragma unroll
            for (int i = 0; i < 8; i++) rs[i] = __builtin_amdgcn_rsqf(rs[i] * (1.f / 512.f) + 1e-5f);
#pragma unroll
            for (int c = 0; c < 8; c++) {
                u32x4 o;
#pragma unroll
                for (int i = 0; i < 4; i++) {
                    const u32 a = xv[2 * i][c >> 1], d = xv[2 * i + 1][c >> 1];
                    const float fa = (c & 1) ? bfhi(a) : bflo(a), fd = (c & 1) ? bfhi(d) : bflo(d);
                    o[i] = pkh((fa - mean[2 * i]) * rs[2 * i], (fd - mean[2 * i + 1]) * rs[2 * i + 1]);
                }
                astore_b128_sc1(p.ws + WS_U + (size_t)tb * 8192, (u32)(c * 1024 + lane * 16), o);
            }
            ST(1);
            kgen_params(sm, wid, lane, are, aim, bre, bim, cre, cim, ldt);
            if (PH == 3) {
                WAITV(0);
                BAR();
                if (tid == 0) { u32x2 t1 = {tag.x + 1u, tag.y}; astore_b64_sc1(flags, (u32)(FL_LN + g * 4 + (r - 28)) * 128u, t1); }
#pragma unroll 1
                for (int jj = 0; jj < 14; jj++) w2_dma(p, lds0, 14 * wid + jj, 0, voffw2);
            }
            ST(2);
#pragma unroll 1
            for (int ks = 0; ks < 4; ks++) kgen_step(sm, wid, lane, ks, offA, offB, G);
            kgen_write(sm, wid, lane, G);
            BAR();
            kgen_expand(sm, wid, lane);
            WAITV(0);
            BAR();
            ST(5);
        }
        if (PH == 1) {
            BAR();
            char* kg = p.ws + WS_KG + (size_t)b * 16384;
            for (int i = tid; i < 640; i += 512) *(u32x4*)(kg + i * 16) = *(LAS u32x4*)(sm + L_KP + i * 16);
            if (tid < 8) *(float*)(kg + 10240 + tid * 4) = *(LAS float*)(sm + L_MISC + tid * 4);
            return;
        }
    }
    if (PH == 2) {
        const char* kg = p.ws + WS_KG + (size_t)b * 16384;
#pragma unroll 1
        for (int jj = 0; jj < 14; jj++) w2_dma(p, lds0, 14 * wid + jj, 0, voffw2);
        for (int i = tid; i < 640; i += 512) *(LAS u32x4*)(sm + L_KP + i * 16) = *(const u32x4*)(kg + i * 16);
        if (tid < 8) *(LAS float*)(sm + L_MISC + tid * 4) = *(const float*)(kg + 10240 + tid * 4);
        WAITV(0);
        BAR();
    }
    const u32x2 tag1 = {tag.x + 1u, tag.y};
    const u32 xo = (u32)((tt * 64 + (tid >> 3)) * 1024 + dt * 128 + (tid & 7) * 16);
    u32x4 xe, b2e;

    u32x4 wreg[14][2];
    const int kq = wid & 3;
    u32 w2a[2];
    {
        const int blk = (lane >> 4) & 1, q = (lane & 15) >> 2, pp = lane & 3;
#pragma unroll
        for (int j2 = 0; j2 < 2; j2++)
            w2a[j2] = (u32)(L_W2 + kq * W2_REG + (4 * h + q) * 128 + (((4 * j2 + 2 * blk + (pp >> 1)) ^ (((q >> 1) & 1) << 2)) << 4) + 8 * (pp & 1));
    }
    auto w2frag = [&](int s, int j2) {
        const u32x2 lo = __builtin_bit_cast(u32x2, __builtin_amdgcn_ds_read_tr16_b64_v4i16((LAS s16x4*)(sm + w2a[j2] + s * 2048)));
        const u32x2 hi = __builtin_bit_cast(u32x2, __builtin_amdgcn_ds_read_tr16_b64_v4i16((LAS s16x4*)(sm + w2a[j2] + s * 2048 + 1024)));
        return (u32x4){lo.x, lo.y, hi.x, hi.y};
    };
    if (!u_done) {
        if (PH == 3) poll_flags(flags, FL_LN, 1, 32, tag1, lane);
        u32x4 uv = aload_b128_sc1(p.ws + WS_U, uoff);
        u32x2 upv = aload_b64_sc1(p.ws + WS_U, tid >= 2 ? uoff - 8192 + 8 : uoff);
        asm volatile("s_waitcnt vmcnt(0)" : "+v"(uv), "+v"(upv) :: "memory");
        build_copies(sm, tid, lane, uv, upv);
    }
    BAR();
    ST(6);
    if (wid < 4) {
        const int cl = wid >> 1, ra = wid & 1, a = lane & 15, q = lane >> 4;
        LAS char* ub = sm + L_UC + cl * UC_CH + (3 - (a & 3)) * UC_COPY + (4 * ra + 2 * q + 1 + (a >> 2)) * 8;
        LAS char* kb = sm + L_KP + cl * KP_CH + q * KP_PLANE + (16 + a) * 16;
        f32x4 y[4] = {};
        const u32 dsk = p.Dskip[ch0 + 8 * cl];
        xe = *(const u32x4*)((const char*)p.x + xo); b2e = *(const u32x4*)((const char*)p.b2 + dt * 128 + (tid & 7) * 16);
#pragma unroll 2
        for (int dl = 0; dl < 16; dl++) {
            u32x4 A[4], B[4];
            SC(32 + dl);
#pragma unroll
            for (int i = 0; i < 4; i++) {
                B[i] = *(LAS u32x4*)(kb - dl * 16 + i * 256);
                const u32x2 lo = *(volatile LAS u32x2*)(ub + dl * 64 + i * 1024), hi = *(volatile LAS u32x2*)(ub + dl * 64 + i * 1024 + 8);
                A[i] = (u32x4){lo.x, lo.y, hi.x, hi.y};
            }
#pragma unroll
            for (int gm = 0; gm < 4; gm++)
#pragma unroll
                for (int c = gm; c < 4; c++)
                    y[c] = __builtin_amdgcn_mfma_f32_16x16x32_f16(__builtin_bit_cast(f16x8, A[gm]), __builtin_bit_cast(f16x8, B[c - gm]), y[c], 0, 0, 0);
        }
        ST(7);
        const f32x4 Mw = *(LAS f32x4*)(sm + L_MISC + cl * 16);
        const float ksc = __builtin_amdgcn_exp2f(fmaxf(fmaxf(Mw.x, Mw.y), fmaxf(Mw.z, Mw.w)) - 8.f);
        const float dk = bf2f(dsk);
        char* sdst = p.ws + WS_S + (size_t)(ch0 + 8 * cl) * 4096 + ra * 2048;
        LAS char* lin = sm + L_XCH + wid * 1024;
#pragma unroll
        for (int cp = 0; cp < 2; cp++) {
#pragma unroll
            for (int cc = 0; cc < 2; cc++) {
                const int c = 2 * cp + cc;
                const f16x4 uu = *(LAS f16x4*)(sm + L_UC + cl * UC_CH + (32 + 32 * (16 * c + a) + 16 * ra + 4 * q) * 2);
                u32x2 o = {pkbf(fmaf(y[c][0], ksc, dk * (float)uu[0]), fmaf(y[c][1], ksc, dk * (float)uu[1])),
                           pkbf(fmaf(y[c][2], ksc, dk * (float)uu[2]), fmaf(y[c][3], ksc, dk * (float)uu[3]))};
                *(LAS u32x2*)(lin + (16 * cc + a) * 32 + q * 8) = o;
            }
            WAITL0();
            u32x4 v = *(LAS u32x4*)(lin + lane * 16);
            asm volatile("s_waitcnt lgkmcnt(0)" : "+v"(v) :: "memory");
            astore_b128_sc1(sdst, (u32)(cp * 1024 + lane * 16), v);
        }
        WAITV(0);
        u32 arr = 0;
        if (lane == 0) arr = __hip_atomic_fetch_add((LAS u32*)(sm + L_MISC + 64), 1u, __ATOMIC_RELAXED, __HIP_MEMORY_SCOPE_WORKGROUP);
        arr = __builtin_amdgcn_readfirstlane(arr);
        if (arr == 3 && lane == 0) astore_b64_sc1(flags, (u32)(FL_S + b) * 128u, tag1);
        ST(8);
        poll_flags(flags, FL_S + dt, 8, 32, tag1, lane);
        for (int spin = 0; spin < (1 << 20); ++spin) {
            if (__builtin_amdgcn_readfirstlane(__hip_atomic_load((LAS u32*)(sm + L_MISC + 64), __ATOMIC_RELAXED, __HIP_MEMORY_SCOPE_WORKGROUP)) >= 4u) break;
            __builtin_amdgcn_s_sleep(1);
        }
        u32x4 sv[2];
#pragma unroll
        for (int i = 0; i < 2; i++) {
            const int d = wid * 16 + i * 8 + (lane >> 3), pc = lane & 7;
            sv[i] = aload_b128_sc1(p.ws + WS_S, (u32)((dt * 64 + d) * 4096 + (pc >> 2) * 2048 + tt * 64 + (pc & 3) * 16));
        }
        asm volatile("s_waitcnt vmcnt(0)" : "+v"(sv[0]), "+v"(sv[1]) :: "memory");
#pragma unroll
        for (int i = 0; i < 2; i++) {
            const int d = wid * 16 + i * 8 + (lane >> 3), pc = lane & 7;
            const int chunk = ((pc >> 1) & 1) * 4 + (pc >> 2) * 2 + (pc & 1);
            *(LAS u32x4*)(sm + L_UC + d * 128 + ((chunk ^ (d >> 3)) << 4)) = sv[i];
        }
        ST(9);
    } else {
#pragma unroll
        for (int s = 0; s < 14; s++) { wreg[s][0] = w2frag(s, 0); wreg[s][1] = w2frag(s, 1); }
#pragma unroll
        for (int s = 0; s < 14; s++) asm volatile("" : "+v"(wreg[s][0]), "+v"(wreg[s][1]));
        WAITL0();
#pragma unroll 1
        for (int jj = 0; jj < 28; jj++) w2_dma(p, lds0, 28 * kq + jj, 1, voffw2);
        ST(7);
        if (PH == 3) poll_flags(flags, FL_H + (tt >> 1) * 16, 1, 14, tag1, lane);
        WAITV(0);
        ST(8);
        const __amdgpu_buffer_rsrc_t hr = __builtin_amdgcn_make_buffer_rsrc(p.ws + WS_H + (size_t)(tt * 2 * 112 + kq * 28) * 1024, 0, 2 * 112 * 1024, 0x00020000);
        constexpr int WIN = 7;
        u32x4 hf[WIN][2];
        f32x16 acc[2][2] = {};
#pragma unroll
        for (int s = 0; s < WIN; s++) {
            hf[s][0] = __builtin_amdgcn_raw_buffer_load_b128(hr, lane * 16, s * 1024, 16);
            hf[s][1] = __builtin_amdgcn_raw_buffer_load_b128(hr, lane * 16 + 112 * 1024, s * 1024, 16);
        }
        SCHED();
#pragma unroll
        for (int s = 0; s < 28; s++) {
            u32x4 w0, w1;
            SC(32 + s);
            if (s < 14) { w0 = wreg[s][0]; w1 = wreg[s][1]; } else { w0 = w2frag(s - 14, 0); w1 = w2frag(s - 14, 1); }
#pragma unroll
            for (int j = 0; j < 2; j++) {
                acc[0][j] = __builtin_amdgcn_mfma_f32_32x32x16_bf16(__builtin_bit_cast(bf16x8, w0), __builtin_bit_cast(bf16x8, hf[s % WIN][j]), acc[0][j], 0, 0, 0);
                acc[1][j] = __builtin_amdgcn_mfma_f32_32x32x16_bf16(__builtin_bit_cast(bf16x8, w1), __builtin_bit_cast(bf16x8, hf[s % WIN][j]), acc[1][j], 0, 0, 0);
            }
            if (s + WIN < 28) {
                hf[s % WIN][0] = __builtin_amdgcn_raw_buffer_load_b128(hr, lane * 16, (s + WIN) * 1024, 16);
                hf[s % WIN][1] = __builtin_amdgcn_raw_buffer_load_b128(hr, lane * 16 + 112 * 1024, (s + WIN) * 1024, 16);
            }
            if (s + WIN == 28) { xe = *(const u32x4*)((const char*)p.x + xo); b2e = *(const u32x4*)((const char*)p.b2 + dt * 128 + (tid & 7) * 16); }
            SCHED();
        }
        ST(9);
#pragma unroll
        for (int j2 = 0; j2 < 2; j2++)
#pragma unroll
            for (int j = 0; j < 2; j++)
#pragma unroll
                for (int rg = 0; rg < 4; rg++) {
                    f32x4 v = {acc[j2][j][4 * rg], acc[j2][j][4 * rg + 1], acc[j2][j][4 * rg + 2], acc[j2][j][4 * rg + 3]};
                    *(LAS f32x4*)(sm + L_W2 + kq * W2_REG + (32 * j + l31) * 272 + (32 * j2 + 8 * rg + 4 * h) * 4) = v;
                }
    }
    BAR();
    ST(10);
    {
        const int tok = tid >> 3, dg = tid & 7;
        float m[8];
#pragma unroll
        for (int e = 0; e < 4; e++) { m[2 * e] = bflo(b2e[e]); m[2 * e + 1] = bfhi(b2e[e]); }
#pragma unroll
        for (int i = 0; i < 4; i++) {
            const f32x4 lo = *(LAS f32x4*)(sm + L_W2 + i * W2_REG + tok * 272 + dg * 32), hi = *(LAS f32x4*)(sm + L_W2 + i * W2_REG + tok * 272 + dg * 32 + 16);
#pragma unroll
            for (int e = 0; e < 4; e++) { m[e] += lo[e]; m[4 + e] += hi[e]; }
        }
        float o[8];
#pragma unroll
        for (int e = 0; e < 8; e++) {
            const float s = bf2f(*(LAS u16*)(sm + L_UC + (8 * dg + e) * 128 + (((tok >> 3) ^ dg) << 4) + (tok & 7) * 2));
            const float gt = m[e] * __builtin_amdgcn_rcpf(1.f + __builtin_amdgcn_exp2f(-1.4426950408889634f * m[e]));
            const float xv = (e & 1) ? bfhi(xe[e >> 1]) : bflo(xe[e >> 1]);
            o[e] = fmaf(s, gt, xv);
        }
        u32x4 ov = {pkbf(o[0], o[1]), pkbf(o[2], o[3]), pkbf(o[4], o[5]), pkbf(o[6], o[7])};
        *(u32x4*)((char*)p.out + xo) = ov;
    }
    if (PH == 3 && b == 0 && wid == 0) {
#pragma unroll 1
        for (int i = 0; i < 4; i++) poll_flags(flags, FL_S + 64 * i, 1, 64, tag1, lane);
        if (lane == 0) astore_b64_sc1(flags, FL_E * 128, tag1);
    }
    if (PADWG < 0 || b == PADWG) { while (__builtin_amdgcn_s_memrealtime() < t_start + PADT) __builtin_amdgcn_s_sleep(1); }
    ST(11);
}

__global__ void tiny_mark(char* ws) { *(volatile u64*)(ws + WS_STAMP) = __builtin_amdgcn_s_memrealtime(); }
__global__ void tiny_spin(char* ws) { u64 t = *(volatile u64*)(ws + WS_STAMP); u64 n = __builtin_amdgcn_s_memrealtime(); if (n - t < 100000) while (__builtin_amdgcn_s_memrealtime() < t + PADT) __builtin_amdgcn_s_sleep(1); }
extern "C" void kernel_launch(void* const* d_in, const int* in_sizes, int n_in, void* d_out, int out_size, void* d_ws, size_t ws_size,
                              hipStream_t stream) {
    Params p;
    p.x = (const u16*)d_in[0]; p.Are = (const u16*)d_in[1]; p.Aim = (const u16*)d_in[2]; p.Bre = (const u16*)d_in[3]; p.Bim = (const u16*)d_in[4];
    p.Cre = (const u16*)d_in[5]; p.Cim = (const u16*)d_in[6]; p.logdt = (const u16*)d_in[7]; p.Dskip = (const u16*)d_in[8];
    p.w1 = (const u16*)d_in[10]; p.b1 = (const u16*)d_in[11]; p.w2 = (const u16*)d_in[12]; p.b2 = (const u16*)d_in[13];
    p.out = (u16*)d_out; p.ws = (char*)d_ws;
    if (EXPMODE == 1 || EXPMODE == 3) tiny_mark<<<1, 64, 0, stream>>>(p.ws);
    fused_kernel<3><<<256, 512, 0, stream>>>(p);
    if (EXPMODE == 2 || EXPMODE == 3) tiny_spin<<<1, 64, 0, stream>>>(p.ws);
}
```

```cpp
#include <hip/hip_runtime.h>
#include <stdint.h>

typedef unsigned short u16;
typedef unsigned u32;
typedef unsigned long long u64;
typedef u32 u32x2 __attribute__((ext_vector_type(2)));
typedef u32 u32x4 __attribute__((ext_vector_type(4)));
typedef short s16x4 __attribute__((ext_vector_type(4)));
typedef __bf16 bf16x2 __attribute__((ext_vector_type(2)));
typedef __bf16 bf16x8 __attribute__((ext_vector_type(8)));
typedef _Float16 f16x2 __attribute__((ext_vector_type(2)));
typedef _Float16 f16x4 __attribute__((ext_vector_type(4)));
typedef _Float16 f16x8 __attribute__((ext_vector_type(8)));
typedef float f32x2 __attribute__((ext_vector_type(2)));
typedef float f32x4 __attribute__((ext_vector_type(4)));
typedef float f32x16 __attribute__((ext_vector_type(16)));
#define LAS __attribute__((address_space(3)))
#define DEV __device__ __forceinline__

DEV int tab_off(int w);
constexpr size_t WS_H = 0;
constexpr size_t WS_U = 8u << 20;
constexpr size_t WS_S = 10u << 20;
constexpr size_t WS_FLAG = 12u << 20;
constexpr size_t WS_STAMP = 20u << 20;
constexpr int FL_LN = 0, FL_S = 32, FL_E = 288, FL_H = 320;
constexpr u32 GR_ST = 256u << 10, GR_CS = 320u << 10;

constexpr int L_W2 = 0, W2_REG = 28672;
constexpr int L_UC = 114688, UC_COPY = 4160, UC_CH = 4 * UC_COPY + 64;
constexpr int L_TAB = 131072, TAB_ROW = 288, TAB_SZ = 6144;
constexpr int L_XCH = 148224;
constexpr int L_MISC = 152320;
constexpr int L_KP = 153600, KP_PLANE = 2304, KP_CH = 2 * KP_PLANE;
constexpr int C_CONV = L_MISC + 64, C_SARR = L_MISC + 68, C_W2A = L_MISC + 72, C_GELU = L_MISC + 76, C_KP = L_MISC + 80, C_P1 = L_MISC + 96, C_P2 = L_MISC + 104, C_CS = L_MISC + 84, C_ULD = L_MISC + 88;
DEV int tab_off(int w) { return w == 3 ? L_KP : L_TAB + w * TAB_SZ; }

struct Params {
    const u16 *x, *Are, *Aim, *Bre, *Bim, *Cre, *Cim, *logdt, *Dskip, *w1, *b1, *w2, *b2;
    u16* out;
    char* ws;
};

DEV float bf2f(u32 v) { return __uint_as_float(v << 16); }
DEV float bflo(u32 v) { return __uint_as_float(v << 16); }
DEV float bfhi(u32 v) { return __uint_as_float(v & 0xffff0000u); }
DEV u32 pkbf(float a, float b) { f32x2 f = {a, b}; return __builtin_bit_cast(u32, __builtin_convertvector(f, bf16x2)); }
DEV u32 pkh(float a, float b) { f32x2 f = {a, b}; return __builtin_bit_cast(u32, __builtin_convertvector(f, f16x2)); }
DEV float opq(float a) { asm("" : "+v"(a)); return a; }
DEV float rbf(float a) { return bflo(pkbf(a, a)); }
DEV float dot2(u32 a, u32 b, float c) { return __builtin_amdgcn_fdot2_f32_bf16(__builtin_bit_cast(bf16x2, a), __builtin_bit_cast(bf16x2, b), c, false); }

#define WAITV(n) asm volatile("s_waitcnt vmcnt(" #n ")" ::: "memory")
#define WAITL0() asm volatile("s_waitcnt lgkmcnt(0)" ::: "memory")
#define BAR() do { asm volatile("s_waitcnt lgkmcnt(0)" ::: "memory"); __builtin_amdgcn_s_barrier(); asm volatile("" ::: "memory"); } while (0)
#define SCHED() __builtin_amdgcn_sched_barrier(0)
#define PRIO(n) __builtin_amdgcn_s_setprio(n)
#define MFMA32(a, b, c) __builtin_amdgcn_mfma_f32_32x32x16_bf16(__builtin_bit_cast(bf16x8, a), __builtin_bit_cast(bf16x8, b), c, 0, 0, 0)
#define MFMA16H(a, b, c) __builtin_amdgcn_mfma_f32_16x16x32_f16(__builtin_bit_cast(f16x8, a), __builtin_bit_cast(f16x8, b), c, 0, 0, 0)

DEV const void* uni(const void* q) {
    const u64 a = (u64)q;
    return (const void*)(((u64)(u32)__builtin_amdgcn_readfirstlane((u32)(a >> 32)) << 32) | (u32)__builtin_amdgcn_readfirstlane((u32)a));
}
DEV void glds14(const void* base, u32 v, u32 dst) {
    u32 keep;
    dst = __builtin_amdgcn_readfirstlane(dst);
    base = uni(base);
    asm volatile("s_mov_b32 %0, m0\n\ts_mov_b32 m0, %3\n\ts_nop 2\n\tglobal_load_lds_dwordx4 %1, %2\n\t"
                 ".rept 13\n\ts_add_u32 m0, m0, 0x400\n\tv_add_u32 %1, 0x2000, %1\n\tglobal_load_lds_dwordx4 %1, %2\n\t.endr\n\t"
                 "s_mov_b32 m0, %0"
                 : "=&s"(keep), "+v"(v) : "s"(base), "s"(dst) : "memory", "scc");
}
DEV void glds4(const void* base, u32 v0, u32 v1, u32 v2, u32 v3, u32 dst) {
    u32 keep;
    dst = __builtin_amdgcn_readfirstlane(dst);
    base = uni(base);
    asm volatile("s_mov_b32 %0, m0\n\ts_mov_b32 m0, %6\n\ts_nop 2\n\tglobal_load_lds_dwordx4 %1, %5\n\t"
                 "s_add_u32 m0, m0, 0x400\n\ts_nop 0\n\tglobal_load_lds_dwordx4 %2, %5\n\t"
                 "s_add_u32 m0, m0, 0x400\n\ts_nop 0\n\tglobal_load_lds_dwordx4 %3, %5\n\t"
                 "s_add_u32 m0, m0, 0x400\n\ts_nop 0\n\tglobal_load_lds_dwordx4 %4, %5\n\ts_mov_b32 m0, %0"
                 : "=&s"(keep) : "v"(v0), "v"(v1), "v"(v2), "v"(v3), "s"(base), "s"(dst) : "memory", "scc");
}
DEV u32 aload_u16(const void* base, u32 voff) { u32 r; asm volatile("global_load_ushort %0, %1, %2" : "=v"(r) : "v"(voff), "s"(base) : "memory"); return r; }
DEV u32 aload_b32(const void* base, u32 voff) { u32 r; asm volatile("global_load_dword %0, %1, %2" : "=v"(r) : "v"(voff), "s"(base) : "memory"); return r; }
DEV u32x2 aload_b64_sc1(const void* base, u32 voff) { u32x2 r; asm volatile("global_load_dwordx2 %0, %1, %2 sc1" : "=v"(r) : "v"(voff), "s"(base) : "memory"); return r; }
DEV u32x4 aload_b128_sc1(const void* base, u32 voff) { u32x4 r; asm volatile("global_load_dwordx4 %0, %1, %2 sc1" : "=v"(r) : "v"(voff), "s"(base) : "memory"); return r; }
DEV u32x4 aload_b128(const void* base, u32 voff) { u32x4 r; asm volatile("global_load_dwordx4 %0, %1, %2" : "=v"(r) : "v"(voff), "s"(base) : "memory"); return r; }
DEV void astore_b128_sc1(void* base, u32 voff, u32x4 v) { asm volatile("global_store_dwordx4 %0, %1, %2 sc1\n\ts_nop 1" :: "v"(voff), "v"(v), "s"(base) : "memory"); }
DEV void astore_b64_sc1(void* base, u32 voff, u32x2 v) { asm volatile("global_store_dwordx2 %0, %1, %2 sc1\n\ts_nop 1" :: "v"(voff), "v"(v), "s"(base) : "memory"); }
DEV u32x4 trfrag(LAS char* a, LAS char* b) {
    const u32x2 lo = __builtin_bit_cast(u32x2, __builtin_amdgcn_ds_read_tr16_b64_v4i16((LAS s16x4*)a));
    const u32x2 hi = __builtin_bit_cast(u32x2, __builtin_amdgcn_ds_read_tr16_b64_v4i16((LAS s16x4*)b));
    return (u32x4){lo.x, lo.y, hi.x, hi.y};
}

#define TS(i)
#define TF(i)
#define TR(i)
#define TSOUT()

DEV void poll_flags(const char* flags, int first, int stride, int nfl, u32x2 tag, int lane) {
    const u32 off = (u32)(first + (lane < nfl ? lane : 0) * stride) * 128u;
#pragma unroll 1
    for (int spin = 0; spin < (1 << 16); ++spin) {
        u32x2 v = aload_b64_sc1(flags, off);
        asm volatile("s_waitcnt vmcnt(0)" : "+v"(v) :: "memory");
        if (__all(v.x == tag.x && v.y == tag.y)) break;
        __builtin_amdgcn_s_sleep(2);
    }
}
DEV void lds_wait(LAS char* w, u32 target) {
#pragma unroll 1
    for (int spin = 0; spin < (1 << 21); ++spin) {
        if ((u32)__builtin_amdgcn_readfirstlane(__hip_atomic_load((LAS u32*)w, __ATOMIC_RELAXED, __HIP_MEMORY_SCOPE_WORKGROUP)) >= target) break;
        __builtin_amdgcn_s_sleep(1);
    }
    asm volatile("" ::: "memory");
}
DEV u32 lds_arrive(LAS char* w, int lane) {
    WAITL0();
    u32 a = 0;
    if (lane == 0) a = __hip_atomic_fetch_add((LAS u32*)w, 1u, __ATOMIC_RELAXED, __HIP_MEMORY_SCOPE_WORKGROUP);
    return __builtin_amdgcn_readfirstlane(a);
}
DEV void lds_signal(LAS char* w, int lane) {
    WAITL0();
    if (lane == 0) __hip_atomic_fetch_add((LAS u32*)w, 1u, __ATOMIC_RELAXED, __HIP_MEMORY_SCOPE_WORKGROUP);
    asm volatile("" ::: "memory");
}
template <int CTRL, int RM> DEV float dpp_add(float v) {
    return v + __builtin_bit_cast(float, __builtin_amdgcn_update_dpp(0, __builtin_bit_cast(int, v), CTRL, RM, 0xf, false));
}
template <int CTRL, int RM> DEV u32 dpp_umax(u32 v) { const u32 o = __builtin_amdgcn_update_dpp(0u, v, CTRL, RM, 0xf, false); return v > o ? v : o; }
DEV u32 wave_umax(u32 v) {
    v = dpp_umax<0xB1, 0xf>(v); v = dpp_umax<0x4E, 0xf>(v); v = dpp_umax<0x141, 0xf>(v); v = dpp_umax<0x140, 0xf>(v);
    v = dpp_umax<0x142, 0xa>(v); v = dpp_umax<0x143, 0xc>(v);
    return __builtin_amdgcn_readlane(v, 63);
}
DEV f32x2 cexp_f(float e2, float rev) {
    const float m = __builtin_amdgcn_exp2f(e2);
    return (f32x2){m * __builtin_amdgcn_cosf(rev), m * __builtin_amdgcn_sinf(rev)};
}
DEV f32x2 cmul_f(f32x2 a, f32x2 z) {
    f32x2 t, r;
    asm("v_pk_mul_f32 %0, %1, %2 op_sel_hi:[1,0]" : "=v"(t) : "v"(a), "v"(z));
    asm("v_pk_fma_f32 %0, %1, %2, %3 op_sel:[1,1,0] op_sel_hi:[0,1,1] neg_lo:[0,1,0]" : "=v"(r) : "v"(a), "v"(z), "v"(t));
    return r;
}
template <bool CONJ> DEV u32 cmh1(u32 a, u32 z) {
    const f16x2 A = __builtin_bit_cast(f16x2, a), Z = __builtin_bit_cast(f16x2, z);
    return __builtin_bit_cast(u32, (f16x2){A.x, CONJ ? -A.y : A.y} * (f16x2){Z.x, Z.x});
}
template <bool CONJ> DEV u32 cmh2(u32 a, u32 z, u32 t) {
    const f16x2 A = __builtin_bit_cast(f16x2, a), Z = __builtin_bit_cast(f16x2, z);
    return __builtin_bit_cast(u32, __builtin_elementwise_fma((f16x2){A.y, A.x}, (f16x2){-Z.y, CONJ ? -Z.y : Z.y}, __builtin_bit_cast(f16x2, t)));
}
DEV void build_table(LAS char* sm, int w, int lane, u32 are, u32 aim, u32 bre, u32 bim, u32 cre, u32 cim, u32 ldt) {
    LAS char* t = sm + tab_off(w) + lane * 4;
    auto put = [&](int row, f32x2 v) { *(LAS u32*)(t + row * TAB_ROW) = pkh(v.x, v.y); };
    const float dtv = rbf(__builtin_amdgcn_exp2f(bf2f(ldt) * 1.4426950408889634f));
    const float a2 = dtv * bf2f(are) * 1.4426950408889634f;
    float rev = dtv * bf2f(aim) * 0.15915494309189535f;
    rev -= __builtin_rintf(rev);
    f32x2 wv = cmul_f((f32x2){bf2f(cre), bf2f(cim)}, (f32x2){bf2f(bre), bf2f(bim)});
    u32 E = wave_umax(__float_as_uint(dtv * fmaxf(fabsf(wv.x), fabsf(wv.y)) * __builtin_amdgcn_exp2f(fmaxf(a2, 0.f) * 2048.f))) >> 23;
    E = E < 16u ? 16u : (E > 236u ? 236u : E);
    wv *= dtv * __uint_as_float((260u - E) << 23);
    *(LAS float*)(sm + L_MISC + w * 4) = (float)(int)E - 133.f;
    const f32x2 z1 = cexp_f(a2, rev), z64 = cexp_f(a2 * 64.f, rev * 64.f);
    const f32x2 z2 = cmul_f(z1, z1), z3 = cmul_f(z2, z1), z4 = cmul_f(z2, z2), z8 = cmul_f(z4, z4), z16 = cmul_f(z8, z8), z32 = cmul_f(z16, z16);
    const f32x2 z128 = cmul_f(z64, z64), z256 = cmul_f(z128, z128), z512 = cmul_f(z256, z256);
    const f32x2 w1 = cmul_f(wv, z512), w2 = cmul_f(w1, z512);
    *(LAS u32*)t = 0x00003c00u;
    put(1, z1); put(2, z2); put(3, z3); put(4, z4); put(5, cmul_f(z4, z1)); put(6, cmul_f(z3, z3)); put(7, cmul_f(z4, z3)); put(8, z8);
    put(9, z16); put(10, cmul_f(z16, z8)); put(11, z32); put(12, z64); put(13, cmul_f(z64, z32));
    put(14, z128); put(15, z256); put(16, cmul_f(z256, z128));
    put(17, wv); put(18, w1); put(19, w2); put(20, cmul_f(w2, z512));
}
DEV u32 perm(u32 hi, u32 lo, u32 sel) { return __builtin_amdgcn_perm(hi, lo, sel); }

__global__ __launch_bounds__(512, 2) void fused_kernel(Params p) {
    __shared__ __attribute__((aligned(1024))) char shm[163840];
    LAS char* sm = (LAS char*)shm;
    const u32 lds0 = (u32)(uintptr_t)sm;
    const int wid = __builtin_amdgcn_readfirstlane(threadIdx.x >> 6);
    const int b = blockIdx.x, g = b & 7, r = b >> 3, hw = wid & 3;
    char* flags = p.ws + WS_FLAG;
    const int ch0 = 64 * g + 16 * (r & 3) + (r >> 2);
    const int tt = g * 4 + (r >> 3), dt = r & 7;
    const int rot = dt & 3;
    const bool isG = r < 28;
    const int mt = (g >> 1) * 4 + r / 7, ft = (g & 1) * 7 + r % 7;
#define LANEVALS() \
    int tid = threadIdx.x; asm volatile("" : "+v"(tid)); \
    const int lane = tid & 63, h = lane >> 5, l31 = lane & 31; \
    const u32 vsw = (u32)(((lane & 7) ^ (((lane >> 4) & 1) << 2)) << 4);          \
    const u32 voffw2 = (u32)((lane >> 3) * 1024 + dt * 128) + vsw; \
    auto w2_dma = [&](int kq, int sg) {        \
        glds14((const char*)p.w2 + (size_t)(4 * kq + ((sg + rot) & 3)) * (112 * 1024), voffw2, lds0 + L_W2 + kq * W2_REG + (sg & 1) * 14336); \
    }; \
    const int blk = (lane >> 4) & 1, q4 = (lane & 15) >> 2, pp = lane & 3; \
      \
    const u32 tra0 = (u32)((4 * h + q4) * 128 + (((2 * blk + (pp >> 1)) ^ (((q4 >> 1) & 1) << 2)) << 4) + 8 * (pp & 1)); \
    const u32 xo = (u32)((tt * 64 + (tid >> 3)) * 1024 + dt * 128 + (tid & 7) * 16); \
    (void)l31; (void)voffw2; (void)w2_dma; (void)tra0; (void)xo;
    TS(0); TR(62);
    if (threadIdx.x < 16) *(LAS u32*)(sm + L_MISC + 64 + threadIdx.x * 4) = 0;
    u32x2 tag1;
    u32x4 xe, b2e;
    u32 pr[7], b1a = 0, b1b = 0;
#define LOAD_PARAMS() \
    const int chp = ch0 + 8 * (wid & 1); \
    const u32 po = (u32)(chp * 128 + ((wid >> 1) & 1) * 64 + lane) * 2u; \
    u32 are = aload_u16(p.Are, po), aim = aload_u16(p.Aim, po), bre = aload_u16(p.Bre, po), bim = aload_u16(p.Bim, po), \
        cre = aload_u16(p.Cre, po), cim = aload_u16(p.Cim, po), ldt = aload_u16(p.logdt, (u32)chp * 2u);

    if (!isG) {
        LANEVALS();
        const int lj = g * 4 + (r - 28), tb = lj * 8 + wid;
        u32x2 tg0 = aload_b64_sc1(flags, FL_E * 128);
        u32x4 xv[8];
#pragma unroll
        for (int i = 0; i < 8; i++) xv[i] = aload_b128((const char*)p.x + (size_t)tb * 8192, (u32)(i * 1024 + lane * 16));
        TS(40);
        BAR();
        asm volatile("s_waitcnt vmcnt(0)" : "+v"(xv[0]), "+v"(xv[1]), "+v"(xv[2]), "+v"(xv[3]), "+v"(xv[4]), "+v"(xv[5]), "+v"(xv[6]), "+v"(xv[7]), "+v"(tg0) :: "memory");
        LOAD_PARAMS();
        {
            const char* wb = (const char*)p.w1 + (lj < 28 ? lj : 0) * 128 + (size_t)wid * 64 * 3584;
            const u32 v0 = (u32)((lane >> 3) * 3584) + vsw;
            glds4(wb, v0, v0 + 8 * 3584, v0 + 16 * 3584, v0 + 24 * 3584, lds0 + wid * 8192);
            glds4(wb + 32 * 3584, v0, v0 + 8 * 3584, v0 + 16 * 3584, v0 + 24 * 3584, lds0 + wid * 8192 + 4096);
        }
        TS(41);
        tag1.x = __builtin_amdgcn_readfirstlane(tg0.x) + 1u; tag1.y = __builtin_amdgcn_readfirstlane(tg0.y);
        float s1[8], s2[8];
#pragma unroll
        for (int i = 0; i < 8; i++) { s1[i] = 0.f; s2[i] = 0.f; }
#pragma unroll
        for (int j = 0; j < 4; j++)
#pragma unroll
            for (int i = 0; i < 8; i++) { s1[i] = dot2(xv[i][j], 0x3f803f80u, s1[i]); s2[i] = dot2(xv[i][j], xv[i][j], s2[i]); }
        auto red8 = [&](float (&v)[8]) {
            float a[4], c[2];
#pragma unroll
            for (int i = 0; i < 4; i++) {
                const auto t = __builtin_amdgcn_permlane32_swap(__float_as_uint(v[i]), __float_as_uint(v[i + 4]), false, false);
                a[i] = __uint_as_float(t[0]) + __uint_as_float(t[1]);
            }
#pragma unroll
            for (int i = 0; i < 2; i++) {
                const auto t = __builtin_amdgcn_permlane16_swap(__float_as_uint(a[i]), __float_as_uint(a[i + 2]), false, false);
                c[i] = dpp_add<0x128, 0xf>(__uint_as_float(t[0]) + __uint_as_float(t[1]));
            }
            float e = (lane & 8) ? c[1] : c[0];
            e = dpp_add<0x141, 0xf>(e); e = dpp_add<0xB1, 0xf>(e); e = dpp_add<0x4E, 0xf>(e);
            return e;
        };
        const float a1 = red8(s1), a2 = red8(s2);
        const float mean = a1 * (1.f / 512.f);
        const float v1 = fmaxf(a2 * (1.f / 512.f) - mean * mean, 0.f);
        const float q1 = v1 + 1e-5f;
        const float rs = __builtin_amdgcn_rsqf(q1);
        const float q2 = v1 * rs * rs + 1e-5f;
        if ((lane & 7) == 0) {
            u32x4 o = {__float_as_uint(-mean), tag1.x, __float_as_uint(__builtin_amdgcn_sqrtf(q1 * q2)), tag1.x};
            astore_b128_sc1(flags, GR_ST + (u32)(tb * 8 + (lane >> 3)) * 16u, o);
        }
        TS(42);
        asm volatile("s_waitcnt vmcnt(1)" : "+v"(are), "+v"(aim), "+v"(bre), "+v"(bim), "+v"(cre), "+v"(cim), "+v"(ldt) :: "memory");
        pr[0] = are; pr[1] = aim; pr[2] = bre; pr[3] = bim; pr[4] = cre; pr[5] = cim; pr[6] = ldt;
        {
            f32x16 c0 = {}, c1 = {};
            const u32x4 ones = {0x3f803f80u, 0x3f803f80u, 0x3f803f80u, 0x3f803f80u};
#pragma unroll
            for (int s = 0; s < 4; s++) {
                LAS char* a = sm + wid * 8192 + s * 2048;
                c0 = MFMA32(ones, trfrag(a + tra0, a + tra0 + 1024), c0);
                c1 = MFMA32(ones, trfrag(a + (tra0 ^ 64), a + (tra0 ^ 64) + 1024), c1);
            }
            *(LAS float*)(sm + L_UC + wid * 256 + lane * 4) = h ? c1[0] : c0[0];
        }
        TS(43);
        if (lds_arrive(sm + C_CS, lane) == 7) {
            float c = 0.f;
#pragma unroll
            for (int i = 0; i < 8; i++) c += *(LAS float*)(sm + L_UC + i * 256 + lane * 4);
            u32x2 o = {__float_as_uint(c), tag1.x};
            if (lj < 28) astore_b64_sc1(flags, GR_CS + (u32)(lj * 64 + lane) * 8u, o);
        }
        f32x2 sc[4], of[4];
        const float nmr = -mean * rs;
#pragma unroll
        for (int i = 0; i < 4; i++) {
            sc[i] = (f32x2){__builtin_bit_cast(float, __builtin_amdgcn_readlane(__builtin_bit_cast(int, rs), 16 * i)), __builtin_bit_cast(float, __builtin_amdgcn_readlane(__builtin_bit_cast(int, rs), 16 * i + 8))};
            of[i] = (f32x2){__builtin_bit_cast(float, __builtin_amdgcn_readlane(__builtin_bit_cast(int, nmr), 16 * i)), __builtin_bit_cast(float, __builtin_amdgcn_readlane(__builtin_bit_cast(int, nmr), 16 * i + 8))};
        }
#pragma unroll
        for (int c = 0; c < 8; c++) {
            u32x4 o;
#pragma unroll
            for (int i = 0; i < 4; i++) {
                const u32 a = xv[2 * i][c >> 1], d = xv[2 * i + 1][c >> 1];
                const f32x2 v = (c & 1) ? (f32x2){bfhi(a), bfhi(d)} : (f32x2){bflo(a), bflo(d)};
                const f32x2 n = __builtin_elementwise_fma(v, sc[i], of[i]);
                o[i] = pkh(n.x, n.y);
            }
            astore_b128_sc1(p.ws + WS_U + (size_t)tb * 8192, (u32)(c * 1024 + lane * 16), o);
        }
        TS(45);
        if (wid < 4) { lds_wait(sm + C_CS, 8); w2_dma(wid, 0); w2_dma(wid, 1); }
    } else if (wid < 4) {
        LANEVALS();
        LOAD_PARAMS();
        const u32 bo = (u32)(ft * 128 + (wid >> 1) * 64 + l31) * 2u;
        u32 b1x = aload_u16(p.b1, bo), b1y = aload_u16(p.b1, bo + 64);
        u32x2 tg0 = aload_b64_sc1(flags, FL_E * 128);
        asm volatile("s_waitcnt vmcnt(0)" : "+v"(are), "+v"(aim), "+v"(bre), "+v"(bim), "+v"(cre), "+v"(cim), "+v"(ldt), "+v"(tg0), "+v"(b1x), "+v"(b1y) :: "memory");
        pr[0] = are; pr[1] = aim; pr[2] = bre; pr[3] = bim; pr[4] = cre; pr[5] = cim; pr[6] = ldt;
        tag1.x = __builtin_amdgcn_readfirstlane(tg0.x) + 1u; tag1.y = __builtin_amdgcn_readfirstlane(tg0.y);
        b1a = b1x; b1b = b1y;
    }
    if (wid < 4) {
        LANEVALS();
        TS(1);
        build_table(sm, wid, lane, pr[0], pr[1], pr[2], pr[3], pr[4], pr[5], pr[6]);
        TS(2);
        WAITV(28);
        BAR();
        if (!isG && tid == 0) astore_b64_sc1(flags, (u32)(FL_LN + g * 4 + (r - 28)) * 128u, tag1);
        TS(3);
    }

    if (wid >= 4) {
        PRIO(3);
        LANEVALS();
        const int a = lane & 15, q = lane >> 4;
        if (wid >= 6) {
            const int kc = wid & 1;
            {
                u32x2 tg0 = aload_b64_sc1(flags, FL_E * 128);
                asm volatile("s_waitcnt vmcnt(0)" : "+v"(tg0) :: "memory");
                tag1.x = __builtin_amdgcn_readfirstlane(tg0.x) + 1u; tag1.y = __builtin_amdgcn_readfirstlane(tg0.y);
            }
            const int rS = a < 9 ? a : (a < 12 ? 4 * (a - 9) : 8 * (a & 1)), rB = 17 + (a < 9 ? 0 : (a < 12 ? 1 : (a >> 1) - 4));
            const int rT1 = (a & 3) ? 7 + (a & 3) : 0, rT2 = (a >> 2) ? 10 + (a >> 2) : 0;
            const u32 oS = rS * TAB_ROW + q * 16, oB = rB * TAB_ROW + q * 16, oT1 = rT1 * TAB_ROW + q * 16, oT2 = rT2 * TAB_ROW + q * 16, oZ = 14 * TAB_ROW + q * 16;
            f32x4 G[4] = {}, Gs[4];
            u32x4 S, Bs, T1, T2, Z1, Z2, Z3;
            auto ldtab = [&](int k) {
                LAS char* t = sm + tab_off(kc + 2 * ((k >> 2) & 1)) + (k & 3) * 64;
                S = *(LAS u32x4*)(t + oS); Bs = *(LAS u32x4*)(t + oB); T1 = *(LAS u32x4*)(t + oT1); T2 = *(LAS u32x4*)(t + oT2);
                Z1 = *(LAS u32x4*)(t + oZ); Z2 = *(LAS u32x4*)(t + oZ + TAB_ROW); Z3 = *(LAS u32x4*)(t + oZ + 2 * TAB_ROW);
            };
            BAR();
            ldtab(0);
            u32 pf = 0;
            const u32 pfo = (u32)(r * 57344 + kc * 32768 + lane * 128);
#pragma unroll
            for (int hf = 0; hf < 2; hf++) {
#pragma unroll 1
                for (int k = 4 * hf; k < 4 * hf + 4; k++) {
                    u32x4 A, B0, B1, B2, B3;
#pragma unroll
                    for (int j = 0; j < 4; j++) { A[j] = cmh1<true>(S[j], Bs[j]); B0[j] = cmh1<false>(T1[j], T2[j]); }
                    SCHED();
#pragma unroll
                    for (int j = 0; j < 4; j++) { B0[j] = cmh2<false>(T1[j], T2[j], B0[j]); A[j] = cmh2<true>(S[j], Bs[j], A[j]); }
                    SCHED();
#pragma unroll
                    for (int j = 0; j < 4; j++) { B1[j] = cmh1<false>(B0[j], Z1[j]); B2[j] = cmh1<false>(B0[j], Z2[j]); B3[j] = cmh1<false>(B0[j], Z3[j]); }
                    G[0] = MFMA16H(A, B0, G[0]);
                    SCHED();
#pragma unroll
                    for (int j = 0; j < 4; j++) { B1[j] = cmh2<false>(B0[j], Z1[j], B1[j]); B2[j] = cmh2<false>(B0[j], Z2[j], B2[j]); B3[j] = cmh2<false>(B0[j], Z3[j], B3[j]); }
                    SCHED();
                    ldtab(k + 1);
                    if (hf == 0 && k + kc < 4) asm volatile("global_load_dword %0, %1, %2" : "+v"(pf) : "v"(pfo + k * 8192), "s"(p.w2) : "memory");
                    G[1] = MFMA16H(A, B1, G[1]); G[2] = MFMA16H(A, B2, G[2]); G[3] = MFMA16H(A, B3, G[3]);
                    TF(4 + 2 * k);
                    if (isG && k < 7) BAR();
                    TF(5 + 2 * k);
                }
                if (hf == 0) {
#pragma unroll
                    for (int ct = 0; ct < 4; ct++) { Gs[ct] = G[ct]; G[ct] = (f32x4){0.f, 0.f, 0.f, 0.f}; }
                }
            }
            {
                const float M0 = *(LAS float*)(sm + L_MISC + kc * 4), M1 = *(LAS float*)(sm + L_MISC + 8 + kc * 4), M = fmaxf(M0, M1);
                const float s0 = __builtin_amdgcn_exp2f(M0 - M), s1 = __builtin_amdgcn_exp2f(M1 - M);
#pragma unroll
                for (int ct = 0; ct < 4; ct++)
#pragma unroll
                    for (int e = 0; e < 4; e++) { float v = fmaf(G[ct][e], s1, Gs[ct][e] * s0); asm("" : "+v"(v)); G[ct][e] = v; }
            }
            asm volatile("s_waitcnt vmcnt(0)" : "+v"(pf) :: "memory");
            TS(20);
            if (isG) BAR();
            else { lds_signal(sm + C_KP, lane); lds_wait(sm + C_KP, 2); }
            TS(21);
            {
                LAS char* gx = sm + L_XCH + kc * 2048;
#pragma unroll
                for (int ct = 0; ct < 4; ct++) {
                    u32x2 o = {pkh(G[ct][0], G[ct][1]), pkh(G[ct][2], G[ct][3])};
                    *(LAS u32x2*)(gx + (16 * ct + a) * 32 + q * 8) = o;
                }
                WAITL0();
                const u32x4 u = *(LAS u32x4*)(gx + lane * 32), v = *(LAS u32x4*)(gx + lane * 32 + 16);
                LAS char* kp = sm + L_KP + kc * KP_CH + (lane & 1) * KP_PLANE + (16 + (lane >> 1)) * 16;
                const u32 LL = 0x01000100u, HH = 0x03020302u, SW = 0x01000302u;
                u32x4 w0;
                if (h) w0 = (u32x4){perm(u.w, v.x, 0x05040100u), perm(0, u.z, LL), perm(u.y, u.z, 0x05040100u), perm(0, u.x, LL)};
                else w0 = (u32x4){perm(0, u.w, SW), perm(0, u.z, SW), perm(0, u.y, SW), perm(0, u.x, SW)};
                const u32 tie = (lane & 1) ? HH : SW;
                u32x4 w1 = {perm(0, v.y, HH), perm(0, v.y, LL), perm(v.x, v.y, 0x07060100u), perm(0, v.x, HH)};
                u32x4 w2 = {perm(0, v.z, HH), perm(0, v.z, tie), perm(0, v.z, LL), perm(0, v.z, LL)};
                u32x4 w3 = {perm(0, v.w, HH), perm(0, v.w, tie), perm(0, v.w, LL), perm(0, v.w, LL)};
                *(LAS u32x4*)kp = w0; *(LAS u32x4*)(kp + 512) = w1; *(LAS u32x4*)(kp + 1024) = w2; *(LAS u32x4*)(kp + 1536) = w3;
                u32x4 z = {0, 0, 0, 0};
                *(LAS u32x4*)(sm + L_KP + kc * KP_CH + (q & 1) * KP_PLANE + a * 16) = z;
                TS(22);
            }
        } else if (isG) {
            const bool lx = wid == 4;
            u32 vo[4];
#pragma unroll
            for (int i = 0; i < 4; i++)
                vo[i] = lx ? (u32)((8 * i + (lane >> 3)) * 1024 + (((lane & 7) ^ (4 * (i & 1) + (lane >> 4))) << 4))
                           : (u32)((4 * i + (lane >> 4)) * 3584 + (((lane & 15) ^ (((lane >> 4) << 2) | i)) << 4));
            const char* src = lx ? (const char*)p.x + (size_t)mt * 128 * 1024 : (const char*)p.w1 + ft * 256;
            const u32 gs = lx ? 32768u : 16u * 3584u, cs = lx ? 128u : 64u * 3584u;
            auto stage = [&](int kc, int i0, int i1) {
#pragma unroll
                for (int i = i0; i < i1; i++) glds4(src + kc * cs + i * gs, vo[0], vo[1], vo[2], vo[3], lds0 + (kc & 3) * 32768 + (lx ? 0 : 16384) + i * 4096);
            };
            stage(0, 0, 4); stage(1, 0, 4);
            TS(4); WAITV(16); TS(5); BAR(); TS(6);
            stage(2, 0, 4); stage(3, 0, 2);
            TF(7); WAITV(24); BAR(); TF(8);
            stage(3, 2, 4);
            u32x2 tg0 = aload_b64_sc1(flags, FL_E * 128);
#pragma unroll 1
            for (int kc = 4; kc < 8; kc++) {
                stage(kc, 0, 4);
                TF(2 * kc + 1); WAITV(32); BAR(); TF(2 * kc + 2);
            }
            asm volatile("" : "+v"(tg0));
            tag1.x = __builtin_amdgcn_readfirstlane(tg0.x) + 1u; tag1.y = __builtin_amdgcn_readfirstlane(tg0.y);
            w2_dma(0, hw);
            TF(18); WAITV(30); BAR(); TF(19);
            w2_dma(1, hw);
            TF(20); WAITV(28); BAR(); TF(21);
            w2_dma(2, hw);
            TF(22); BAR(); TS(23);
            w2_dma(3, hw);
            TS(24);
        } else { WAITV(0); BAR(); }
        if (wid < 6) {
            u32x2 fl = aload_b64_sc1(flags, (u32)(FL_LN + l31) * 128u);
            asm volatile("s_waitcnt vmcnt(0)" : "+v"(fl) :: "memory");
            lds_signal(sm + C_W2A, lane);
            if (!__all(fl.x == tag1.x && fl.y == tag1.y)) poll_flags(flags, FL_LN, 1, 32, tag1, lane);
            TS(25);
            const int tb0 = hw * 128 + (lane >> 1);
            const u32 ob = (u32)((r >> 2) * 1024 + (8 * g + 2 * (r & 3) + (lane & 1)) * 16), o0 = ob + (u32)tb0 * 8192u;
            u32x4 uv[4];
#pragma unroll
            for (int i = 0; i < 4; i++) uv[i] = aload_b128_sc1(p.ws + WS_U, o0 + i * 32 * 8192);
            u32x2 pv = aload_b64_sc1(p.ws + WS_U, ob + 127u * 8192u + 8u);
            asm volatile("s_waitcnt vmcnt(0)" : "+v"(uv[0]), "+v"(uv[1]), "+v"(uv[2]), "+v"(uv[3]), "+v"(pv) :: "memory");
            TS(26);
            if (!isG) { lds_signal(sm + C_ULD, lane); lds_wait(sm + C_KP, 2); }
            u32 r2[5], r3[5];
            r2[0] = hw ? pv.x : 0u; r3[0] = hw ? pv.y : 0u;
#pragma unroll
            for (int i = 0; i < 4; i++) {
                r2[i + 1] = __builtin_amdgcn_ds_bpermute(((lane - 2) & 63) * 4, uv[i].z);
                r3[i + 1] = __builtin_amdgcn_ds_bpermute(((lane - 2) & 63) * 4, uv[i].w);
            }
            LAS char* d = sm + L_UC + (lane & 1) * UC_CH + (tb0 + 4) * 16;
#pragma unroll
            for (int i = 0; i < 4; i++) *(LAS u32x4*)(d + i * 512) = uv[i];
#pragma unroll
            for (int i = 0; i < 4; i++) {
                const u32x4 t = uv[i];
                const u32 q2 = lane < 2 ? r2[i] : r2[i + 1], q3 = lane < 2 ? r3[i] : r3[i + 1];
                const u32 a0 = __builtin_amdgcn_alignbit(q3, q2, 16), a1 = __builtin_amdgcn_alignbit(t.x, q3, 16), a2 = __builtin_amdgcn_alignbit(t.y, t.x, 16),
                          a3 = __builtin_amdgcn_alignbit(t.z, t.y, 16), a4 = __builtin_amdgcn_alignbit(t.w, t.z, 16);
                u32x4 c1 = {a1, a2, a3, a4}, c2 = {q3, t.x, t.y, t.z}, c3 = {a0, a1, a2, a3};
                *(LAS u32x4*)(d + i * 512 + UC_COPY) = c1;
                *(LAS u32x4*)(d + i * 512 + 2 * UC_COPY) = c2;
                *(LAS u32x4*)(d + i * 512 + 3 * UC_COPY) = c3;
            }
            u32x4 z = {0, 0, 0, 0};
            *(LAS u32x4*)(sm + L_UC + hw * UC_CH + ((lane >> 2) & 3) * UC_COPY + (lane & 3) * 16) = z;
        }
        lds_signal(sm + C_CONV, lane);
        const int cl = hw & 1, rh = hw >> 1, chn = ch0 + 8 * cl;
        const u32 dsk = p.Dskip[chn];
        lds_wait(sm + C_CONV, 4);
        LAS char* ub = sm + L_UC + cl * UC_CH + (3 - (a & 3)) * UC_COPY + (32 * rh + 2 * q + 5 + (a >> 2)) * 8;
        LAS char* kb = sm + L_KP + cl * KP_CH + ((q + 1) & 1) * KP_PLANE + (16 - 8 * rh - 6 + a - (q >> 1)) * 16;
        f32x4 y[8] = {};
        auto ldA = [&](int dl, int i) {
            const u32x2 lo = *(volatile LAS u32x2*)(ub + dl * 64 + i * 512), hi = *(volatile LAS u32x2*)(ub + dl * 64 + i * 512 + 8);
            return (u32x4){lo.x, lo.y, hi.x, hi.y};
        };
        auto ldB = [&](int dl, int i) { return *(LAS u32x4*)(kb + (3 - dl) * 32 + i * 256); };
        u32x4 A0[8], B0[8], A1[8], B1[8];
#pragma unroll
        for (int i = 0; i < 8; i++) { B0[i] = ldB(0, i); A0[i] = ldA(0, i); }
        lds_wait(sm + C_GELU, isG ? 4u : 0u);
        PRIO(0);
        TS(27);
        auto cstep = [&](u32x4 (&A)[8], u32x4 (&B)[8], u32x4 (&An)[8], u32x4 (&Bn)[8], int dn) {
            asm volatile("" :: "v"(A[0]), "v"(A[1]), "v"(A[2]), "v"(A[3]), "v"(A[4]), "v"(A[5]), "v"(A[6]), "v"(A[7]));
            asm volatile("" :: "v"(B[0]), "v"(B[1]), "v"(B[2]), "v"(B[3]), "v"(B[4]), "v"(B[5]), "v"(B[6]), "v"(B[7]));
            SCHED();
            int n = 0;
#pragma unroll
            for (int gm = 0; gm < 8; gm++)
#pragma unroll
                for (int c = gm; c < 8; c++) {
                    y[c] = MFMA16H(A[gm], B[c - gm], y[c]);
                    if (dn < 4 && n < 24) { if (n % 3 == 0) Bn[n / 3] = ldB(dn, n / 3); else if (n % 3 == 1) An[n / 3] = ldA(dn, n / 3); }
                    n++;
                    SCHED();
                }
        };
        cstep(A0, B0, A1, B1, 1); cstep(A1, B1, A0, B0, 2); cstep(A0, B0, A1, B1, 3); cstep(A1, B1, A0, B0, 4);
        PRIO(3);
        TS(28);
        lds_signal(sm + C_P1 + cl * 4, lane); lds_wait(sm + C_P1 + cl * 4, 2);
        LAS char* xb = sm + L_UC + cl * UC_CH + UC_COPY + lane * 16;
#pragma unroll
        for (int c = 0; c < 4; c++) {
            const f32x4 lo = y[c], hi = y[4 + c];
            const f32x4 snd = rh ? lo : hi;
            y[c] = rh ? hi : lo;
            *(LAS f32x4*)(xb + rh * 4096 + c * 1024) = snd;
        }
        lds_signal(sm + C_P2 + cl * 4, lane);
        const float ksc = __builtin_amdgcn_exp2f(fmaxf(*(LAS float*)(sm + L_MISC + cl * 4), *(LAS float*)(sm + L_MISC + 8 + cl * 4)));
        const float dk = bf2f(dsk);
        f16x4 uu[4];
#pragma unroll
        for (int c = 0; c < 4; c++) uu[c] = *(LAS f16x4*)(sm + L_UC + cl * UC_CH + (32 + 1024 * rh + 256 * c + 16 * a + 4 * q) * 2);
        char* sdst = p.ws + WS_S + (size_t)chn * 4096 + rh * 2048;
        LAS char* lin = sm + L_UC + cl * UC_CH + UC_COPY + 8192 + rh * 2048;
#pragma unroll
        for (int c = 0; c < 4; c++)
#pragma unroll
            for (int e = 0; e < 4; e++) y[c][e] = opq(fmaf((float)uu[c][e], dk, opq(y[c][e] * ksc)));
        lds_wait(sm + C_P2 + cl * 4, 2);
#pragma unroll
        for (int c = 0; c < 4; c++) {
            const f32x4 v = *(LAS f32x4*)(xb + (rh ^ 1) * 4096 + c * 1024);
            float f[4];
#pragma unroll
            for (int e = 0; e < 4; e++) f[e] = opq(fmaf(v[e], ksc, y[c][e]));
            u32x2 o = {pkbf(f[0], f[1]), pkbf(f[2], f[3])};
            *(LAS u32x2*)(lin + c * 512 + a * 32 + q * 8) = o;
        }
        WAITL0();
        {
            u32x4 v0 = *(LAS u32x4*)(lin + lane * 16), v1 = *(LAS u32x4*)(lin + 1024 + lane * 16);
            asm volatile("s_waitcnt lgkmcnt(0)" : "+v"(v0), "+v"(v1) :: "memory");
            astore_b128_sc1(sdst, (u32)(lane * 16), v0);
            astore_b128_sc1(sdst, (u32)(1024 + lane * 16), v1);
        }
        TS(29);
        WAITV(0);
        if (lds_arrive(sm + C_SARR, lane) == 3 && lane == 0) astore_b64_sc1(flags, (u32)(FL_S + b) * 128u, tag1);
        PRIO(0);
        TS(30);
        xe = *(const u32x4*)((const char*)p.x + xo); b2e = *(const u32x4*)((const char*)p.b2 + dt * 128 + (tid & 7) * 16);
        poll_flags(flags, FL_S + dt, 8, 32, tag1, lane);
        lds_wait(sm + C_SARR, 4);
        TS(31);
        u32x4 sv[2];
#pragma unroll
        for (int i = 0; i < 2; i++) {
            const int d = hw * 16 + i * 8 + (lane >> 3);
            sv[i] = aload_b128_sc1(p.ws + WS_S, (u32)((dt * 64 + d) * 4096 + tt * 128 + (lane & 7) * 16));
        }
        asm volatile("s_waitcnt vmcnt(0)" : "+v"(sv[0]), "+v"(sv[1]) :: "memory");
#pragma unroll
        for (int i = 0; i < 2; i++) {
            const int d = hw * 16 + i * 8 + (lane >> 3);
            *(LAS u32x4*)(sm + L_UC + d * 128 + (((lane & 7) ^ (d >> 3)) << 4)) = sv[i];
        }
        TS(32);
    } else {
        LANEVALS();
        const int kq = wid;
        u32x4 wreg[14][2];
        LAS char* w2a = sm + L_W2 + kq * W2_REG;
        auto w2frag = [&](int s, int j2) { LAS char* a = w2a + (j2 ? tra0 ^ 64 : tra0) + s * 2048; return trfrag(a, a + 1024); };
        auto w2load = [&]() {
#pragma unroll
            for (int s = 0; s < 14; s++) { wreg[s][0] = w2frag(s, 0); wreg[s][1] = w2frag(s, 1); }
        };
        if (isG) {
            const int th = wid & 1, fh = wid >> 1;
            u32 xa[4], wa[2][2];
#pragma unroll
            for (int s = 0; s < 4; s++) xa[s] = (u32)((64 * th + l31) * 128 + (((2 * s + h) ^ ((l31 >> 1) & 7)) << 4));
#pragma unroll
            for (int j2 = 0; j2 < 2; j2++)
#pragma unroll
                for (int t = 0; t < 2; t++)
                    wa[j2][t] = (u32)(16384 + 256 * (8 * h + 4 * t + q4) + (((4 * (2 * fh + j2) + 2 * blk + (pp >> 1)) ^ ((q4 << 2) | (2 * h + t))) << 4) + 8 * (pp & 1));
            f32x16 acc[2][2] = {};
            u32x4 X[4][2], W[4][2];
            auto ldx = [&](LAS char* sl, int s, int j) { return *(LAS u32x4*)(sl + xa[s] + j * 4096); };
            auto ldw = [&](LAS char* sl, int s, int j2) { return trfrag(sl + wa[j2][0] + s * 4096, sl + wa[j2][1] + s * 4096); };
            auto lda = [&](LAS char* sl, int s) { X[s][0] = ldx(sl, s, 0); W[s][0] = ldw(sl, s, 0); };
            auto ldb = [&](LAS char* sl, int s) { W[s][1] = ldw(sl, s, 1); X[s][1] = ldx(sl, s, 1); };
#define MM(s, j2, j) acc[j2][j] = MFMA32(W[s][j2], X[s][j], acc[j2][j])
            auto chunk = [&](LAS char* sl, LAS char* sn, bool last) {
                MM(0, 0, 0); lda(sl, 2); SCHED(); MM(0, 1, 0); ldb(sl, 2); SCHED(); MM(0, 0, 1); SCHED(); MM(0, 1, 1); SCHED();
                MM(1, 0, 0); lda(sl, 3); SCHED(); MM(1, 1, 0); ldb(sl, 3); SCHED(); MM(1, 0, 1); SCHED(); MM(1, 1, 1); SCHED();
                MM(2, 0, 0); SCHED(); MM(2, 1, 0); SCHED();
                BAR();
                SCHED();
                if (!last) lda(sn, 0);
                SCHED(); MM(2, 0, 1); if (!last) ldb(sn, 0); SCHED(); MM(2, 1, 1); SCHED();
                MM(3, 0, 0); if (!last) lda(sn, 1); SCHED(); MM(3, 1, 0); if (!last) ldb(sn, 1); SCHED(); MM(3, 0, 1); SCHED(); MM(3, 1, 1); SCHED();
            };
            lda(sm, 0); ldb(sm, 0); lda(sm, 1); ldb(sm, 1);
#pragma unroll 1
            for (int kc = 0; kc < 7; kc++) { chunk(sm + (kc & 3) * 32768, sm + ((kc + 1) & 3) * 32768, false); TF(4 + kc); }
            const u32 so = GR_ST + (u32)(mt * 128 + th * 64 + l31) * 16u, co = GR_CS + (u32)(ft * 128 + fh * 64 + l31) * 8u;
            u32x4 st0 = aload_b128_sc1(flags, so), st1 = aload_b128_sc1(flags, so + 512);
            u32x2 cs0 = aload_b64_sc1(flags, co), cs1 = aload_b64_sc1(flags, co + 256);
            SCHED();
            chunk(sm + 3 * 32768, sm, true);
            TS(19);
            PRIO(2);
            SCHED();
            asm volatile("s_waitcnt vmcnt(0)" : "+v"(st0), "+v"(st1), "+v"(cs0), "+v"(cs1) :: "memory");
#pragma unroll 1
            for (int spin = 0; spin < (1 << 16); ++spin) {
                if (__all(st0.y == tag1.x && st0.w == tag1.x && st1.y == tag1.x && st1.w == tag1.x && cs0.y == tag1.x && cs1.y == tag1.x)) break;
                __builtin_amdgcn_s_sleep(1);
                st0 = aload_b128_sc1(flags, so); st1 = aload_b128_sc1(flags, so + 512); cs0 = aload_b64_sc1(flags, co); cs1 = aload_b64_sc1(flags, co + 256);
                asm volatile("s_waitcnt vmcnt(0)" : "+v"(st0), "+v"(st1), "+v"(cs0), "+v"(cs1) :: "memory");
            }
            TS(20);
            float rinv[2] = {__uint_as_float(st0.z), __uint_as_float(st1.z)};
            {
                u32x4 Ax[2], Bx[2];
#pragma unroll
                for (int j = 0; j < 2; j++) {
                    const float nm = __uint_as_float(j ? st1.x : st0.x), mh = rbf(nm), ih = rbf(rinv[j]);
                    const u32 mm = pkbf(mh, nm - mh);
                    Bx[j] = h ? (u32x4){0u, 0u, 0u, 0u} : (u32x4){mm, mm, pkbf(ih, rinv[j] - ih), 0u};
                    const float c = __uint_as_float(j ? cs1.x : cs0.x), chh = rbf(c);
                    const u32 bb = j ? b1b : b1a;
                    Ax[j] = (u32x4){pkbf(chh, chh), pkbf(c - chh, c - chh), bb | (bb << 16), 0u};
                }
#pragma unroll
                for (int j = 0; j < 2; j++)
#pragma unroll
                    for (int j2 = 0; j2 < 2; j2++) acc[j2][j] = MFMA32(Ax[j2], Bx[j], acc[j2][j]);
            }
            char* hdst = p.ws + WS_H + (size_t)((mt * 4 + th * 2) * 112 + ft * 8 + fh * 4) * 1024;
#pragma unroll
            for (int j = 0; j < 2; j++) {
                const float rr = __builtin_amdgcn_rcpf(rinv[j]);
                const float k1 = -2.302208198144325f * rr, k2 = -0.10294323958002349f * rr * rr * rr;
                const f32x2 K1 = {k1, k1}, K2 = {k2, k2}, RI = {rinv[j], rinv[j]};
#pragma unroll
                for (int j2 = 0; j2 < 2; j2++) {
#pragma unroll
                    for (int s = 0; s < 2; s++) {
                        u32x4 o;
#pragma unroll
                        for (int i = 0; i < 4; i++) {
                            const f32x2 u = {acc[j2][j][8 * s + 2 * i], acc[j2][j][8 * s + 2 * i + 1]};
                            const f32x2 z = __builtin_elementwise_fma(u * u, K2, K1) * u;
                            const f32x2 e = {__builtin_amdgcn_exp2f(z.x), __builtin_amdgcn_exp2f(z.y)};
                            const f32x2 dn = __builtin_elementwise_fma(e, RI, RI);
                            const f32x2 gl = u * (f32x2){__builtin_amdgcn_rcpf(dn.x), __builtin_amdgcn_rcpf(dn.y)};
                            o[i] = pkbf(gl.x, gl.y);
                        }
                        astore_b128_sc1(hdst, (u32)((j * 112 + j2 * 2 + s) * 1024 + lane * 16), o);
                        SCHED();
                        if (j == 1 && j2 == 0 && s == 1) { lds_wait(sm + C_W2A, 2); w2load(); SCHED(); }
                    }
                }
            }
            lds_signal(sm + C_GELU, lane);
            TS(21);
        } else {
            WAITV(0);
            w2load();
            lds_wait(sm + C_ULD, 2);
        }
        PRIO(1);
#pragma unroll
        for (int s = 0; s < 14; s++) asm volatile("" : "+v"(wreg[s][0]), "+v"(wreg[s][1]));
        WAITL0();
        w2_dma(kq, 2);
        TS(23);
        WAITV(14);
        if (isG && lane == 0) astore_b64_sc1(flags, (u32)(FL_H + (mt * 2 + (wid & 1)) * 32 + ft * 2 + (wid >> 1)) * 128u, tag1);
        TS(24);
        w2_dma(kq, 3);
        TS(22);
        poll_flags(flags, FL_H + tt * 32 + 7 * kq, 1, 7, tag1, lane);
        TS(25);
        const __amdgpu_buffer_rsrc_t hr = __builtin_amdgcn_make_buffer_rsrc(p.ws + WS_H + (size_t)(tt * 2 * 112 + kq * 28) * 1024, 0, 2 * 112 * 1024, 0x00020000);
        constexpr int WIN = 8;
        u32x4 hf[WIN][2];
        f32x16 acc[2][2] = {};
        auto ldh = [&](int s, int j) {
            return __builtin_amdgcn_raw_buffer_load_b128(hr, lane * 16 + j * 112 * 1024 + ((s % 7) & 3) * 1024, ((s / 7 + rot) & 3) * 7168 + ((s % 7) >> 2) * 4096, 16);
        };
#pragma unroll
        for (int s = 0; s < WIN; s++) { hf[s][0] = ldh(s, 0); SCHED(); hf[s][1] = ldh(s, 1); SCHED(); }
        TS(26);
#pragma unroll
        for (int s = 0; s < 28; s++) {
            if (s == 12) { TS(27); }
            const u32x4 w0 = wreg[s % 14][0], w1 = wreg[s % 14][1];
            acc[0][0] = MFMA32(w0, hf[s % WIN][0], acc[0][0]); SCHED();
            acc[1][0] = MFMA32(w1, hf[s % WIN][0], acc[1][0]);
            if (s + WIN < 28) hf[s % WIN][0] = ldh(s + WIN, 0);
            SCHED();
            acc[0][1] = MFMA32(w0, hf[s % WIN][1], acc[0][1]);
            if (s >= 12 && s < 26) wreg[s - 12][0] = w2frag(s - 12, 0);
            SCHED();
            acc[1][1] = MFMA32(w1, hf[s % WIN][1], acc[1][1]);
            if (s >= 12 && s < 26) wreg[s - 12][1] = w2frag(s - 12, 1);
            if (s + WIN < 28) hf[s % WIN][1] = ldh(s + WIN, 1);
            if (s + WIN == 28) { LANEVALS(); xe = *(const u32x4*)((const char*)p.x + xo); b2e = *(const u32x4*)((const char*)p.b2 + dt * 128 + (tid & 7) * 16); }
            SCHED();
        }
        TS(29);
        int t2 = threadIdx.x; asm volatile("" : "+v"(t2));
        LAS char* pw = w2a + (t2 & 31) * 272 + ((t2 >> 5) & 1) * 16;
#pragma unroll
        for (int j2 = 0; j2 < 2; j2++)
#pragma unroll
            for (int j = 0; j < 2; j++)
#pragma unroll
                for (int rg = 0; rg < 4; rg++) {
                    f32x4 v = {acc[j2][j][4 * rg], acc[j2][j][4 * rg + 1], acc[j2][j][4 * rg + 2], acc[j2][j][4 * rg + 3]};
                    *(LAS f32x4*)(pw + 32 * j * 272 + (32 * j2 + 8 * rg) * 4) = v;
                }
        TS(32);
    }
    BAR();
    TS(33);
    LANEVALS();
    {
        const int tok = tid >> 3, dg = tid & 7;
        float m[8];
#pragma unroll
        for (int e = 0; e < 4; e++) { m[2 * e] = bflo(b2e[e]); m[2 * e + 1] = bfhi(b2e[e]); }
#pragma unroll
        for (int i = 0; i < 4; i++) {
            const f32x4 lo = *(LAS f32x4*)(sm + L_W2 + i * W2_REG + tok * 272 + dg * 32), hi = *(LAS f32x4*)(sm + L_W2 + i * W2_REG + tok * 272 + dg * 32 + 16);
#pragma unroll
            for (int e = 0; e < 4; e++) { m[e] += lo[e]; m[4 + e] += hi[e]; }
        }
        float o[8];
#pragma unroll
        for (int e = 0; e < 8; e++) {
            const float s = bf2f(*(LAS u16*)(sm + L_UC + (8 * dg + e) * 128 + (((tok >> 3) ^ dg) << 4) + (tok & 7) * 2));
            const float gt = m[e] * __builtin_amdgcn_rcpf(1.f + __builtin_amdgcn_exp2f(-1.4426950408889634f * m[e]));
            const float xv = (e & 1) ? bfhi(xe[e >> 1]) : bflo(xe[e >> 1]);
            o[e] = fmaf(s, gt, xv);
        }
        u32x4 ov = {pkbf(o[0], o[1]), pkbf(o[2], o[3]), pkbf(o[4], o[5]), pkbf(o[6], o[7])};
        *(u32x4*)((char*)p.out + xo) = ov;
    }
    if (b == 0 && wid == 0) {
#pragma unroll 1
        for (int i = 0; i < 4; i++) poll_flags(flags, FL_S + 64 * i, 1, 64, tag1, lane);
        if (lane == 0) astore_b64_sc1(flags, FL_E * 128, tag1);
    }
    TS(34); TR(63);
    TSOUT();
}

extern "C" void kernel_launch(void* const* d_in, const int* in_sizes, int n_in, void* d_out, int out_size, void* d_ws, size_t ws_size,
                              hipStream_t stream) {
    Params p;
    p.x = (const u16*)d_in[0]; p.Are = (const u16*)d_in[1]; p.Aim = (const u16*)d_in[2]; p.Bre = (const u16*)d_in[3]; p.Bim = (const u16*)d_in[4];
    p.Cre = (const u16*)d_in[5]; p.Cim = (const u16*)d_in[6]; p.logdt = (const u16*)d_in[7]; p.Dskip = (const u16*)d_in[8];
    p.w1 = (const u16*)d_in[10]; p.b1 = (const u16*)d_in[11]; p.w2 = (const u16*)d_in[12]; p.b2 = (const u16*)d_in[13];
    p.out = (u16*)d_out; p.ws = (char*)d_ws;
    fused_kernel<<<256, 512, 0, stream>>>(p);
}
```

```cpp
#include <hip/hip_runtime.h>
#include <stdint.h>
#define V_KROT 2
#define V_W2ROT 0
#define V_TOUCH 0
#define V_CODE 1
#define V_MPRIO 0
#define V_KPRIO 1
#define V_PSLEEP 6
#define V_SSTEP 14
#define V_UAT 5
#define V_MALL 2
#define V_MDELAY 16
#define CODE_LINES 280

typedef unsigned short u16;
typedef unsigned u32;
typedef unsigned long long u64;
typedef u32 u32x2 __attribute__((ext_vector_type(2)));
typedef u32 u32x4 __attribute__((ext_vector_type(4)));
typedef short s16x4 __attribute__((ext_vector_type(4)));
typedef __bf16 bf16x2 __attribute__((ext_vector_type(2)));
typedef __bf16 bf16x8 __attribute__((ext_vector_type(8)));
typedef _Float16 f16x2 __attribute__((ext_vector_type(2)));
typedef _Float16 f16x4 __attribute__((ext_vector_type(4)));
typedef _Float16 f16x8 __attribute__((ext_vector_type(8)));
typedef float f32x2 __attribute__((ext_vector_type(2)));
typedef float f32x4 __attribute__((ext_vector_type(4)));
typedef float f32x16 __attribute__((ext_vector_type(16)));
#define LAS __attribute__((address_space(3)))
#define DEV __device__ __forceinline__

DEV int tab_off(int w);
constexpr size_t WS_H = 0;
constexpr size_t WS_U = 8u << 20;
constexpr size_t WS_S = 10u << 20;
constexpr size_t WS_FLAG = 12u << 20;
constexpr size_t WS_STAMP = 20u << 20;
constexpr int FL_LN = 0  , FL_S = 32, FL_E = 288, FL_H = 320;
constexpr u32 GR_ST = 256u << 10, GR_CS = 320u << 10;

constexpr int L_W2 = 0, W2_REG = 28672;
constexpr int L_UC = 114688, UC_COPY = 4160, UC_CH = 4 * UC_COPY + 64;
constexpr int L_TAB = 131072, TAB_ROW = 288, TAB_SZ = 6144;
constexpr int L_XCH = 148224;
constexpr int L_MISC = 152320;
constexpr int L_KP = 153600, KP_PLANE = 2304, KP_CH = 2 * KP_PLANE;
constexpr int F_M = L_MISC + 64, F_L = L_MISC + 80, F_K = L_MISC + 88, F_H = L_MISC + 96, C_CS = L_MISC + 112, F_D = L_MISC + 128, F_P = L_MISC + 160;
constexpr int L_TAG = L_MISC + 112, L_JUNK = L_MISC + 256;
DEV int tab_off(int w) { return w == 3 ? L_KP : L_TAB + w * TAB_SZ; }

struct Params {
    const u16 *x, *Are, *Aim, *Bre, *Bim, *Cre, *Cim, *logdt, *Dskip, *w1, *b1, *w2, *b2;
    u16* out;
    char* ws;
};

DEV float bf2f(u32 v) { return __uint_as_float(v << 16); }
DEV float bflo(u32 v) { return __uint_as_float(v << 16); }
DEV float bfhi(u32 v) { return __uint_as_float(v & 0xffff0000u); }
DEV u32 pkbf(float a, float b) { f32x2 f = {a, b}; return __builtin_bit_cast(u32, __builtin_convertvector(f, bf16x2)); }
DEV u32 pkh(float a, float b) { f32x2 f = {a, b}; return __builtin_bit_cast(u32, __builtin_convertvector(f, f16x2)); }
DEV float opq(float a) { asm("" : "+v"(a)); return a; }
DEV float rbf(float a) { return bflo(pkbf(a, a)); }
DEV float dot2(u32 a, u32 b, float c) { return __builtin_amdgcn_fdot2_f32_bf16(__builtin_bit_cast(bf16x2, a), __builtin_bit_cast(bf16x2, b), c, false); }

#define WAITV(n) asm volatile("s_waitcnt vmcnt(" #n ")" ::: "memory")
#define WAITL0() asm volatile("s_waitcnt lgkmcnt(0)" ::: "memory")
#define BAR() do { asm volatile("s_waitcnt lgkmcnt(0)" ::: "memory"); __builtin_amdgcn_s_barrier(); asm volatile("" ::: "memory"); } while (0)
#define SCHED() __builtin_amdgcn_sched_barrier(0)
#define PRIO(n) __builtin_amdgcn_s_setprio(n)
#define MFMA32(a, b, c) __builtin_amdgcn_mfma_f32_32x32x16_bf16(__builtin_bit_cast(bf16x8, a), __builtin_bit_cast(bf16x8, b), c, 0, 0, 0)
#define MFMA16H(a, b, c) __builtin_amdgcn_mfma_f32_16x16x32_f16(__builtin_bit_cast(f16x8, a), __builtin_bit_cast(f16x8, b), c, 0, 0, 0)

DEV const void* uni(const void* q) {
    const u64 a = (u64)q;
    return (const void*)(((u64)(u32)__builtin_amdgcn_readfirstlane((u32)(a >> 32)) << 32) | (u32)__builtin_amdgcn_readfirstlane((u32)a));
}
DEV void glds14(const void* base, u32 v, u32 dst) {
    u32 keep;
    dst = __builtin_amdgcn_readfirstlane(dst);
    base = uni(base);
    asm volatile("s_mov_b32 %0, m0\n\ts_mov_b32 m0, %3\n\ts_nop 2\n\tglobal_load_lds_dwordx4 %1, %2\n\t"
                 ".rept 13\n\ts_add_u32 m0, m0, 0x400\n\tv_add_u32 %1, 0x2000, %1\n\tglobal_load_lds_dwordx4 %1, %2\n\t.endr\n\t"
                 "s_mov_b32 m0, %0"
                 : "=&s"(keep), "+v"(v) : "s"(base), "s"(dst) : "memory", "scc");
}
DEV void glds4(const void* base, u32 v0, u32 v1, u32 v2, u32 v3, u32 dst) {
    u32 keep;
    dst = __builtin_amdgcn_readfirstlane(dst);
    base = uni(base);
    asm volatile("s_mov_b32 %0, m0\n\ts_mov_b32 m0, %6\n\ts_nop 2\n\tglobal_load_lds_dwordx4 %1, %5\n\t"
                 "s_add_u32 m0, m0, 0x400\n\ts_nop 0\n\tglobal_load_lds_dwordx4 %2, %5\n\t"
                 "s_add_u32 m0, m0, 0x400\n\ts_nop 0\n\tglobal_load_lds_dwordx4 %3, %5\n\t"
                 "s_add_u32 m0, m0, 0x400\n\ts_nop 0\n\tglobal_load_lds_dwordx4 %4, %5\n\ts_mov_b32 m0, %0"
                 : "=&s"(keep) : "v"(v0), "v"(v1), "v"(v2), "v"(v3), "s"(base), "s"(dst) : "memory", "scc");
}
DEV void touch(const void* base, u32 voff, u32 junk) {
    u32 keep;
    base = uni(base);
    asm volatile("s_mov_b32 %0, m0\n\ts_mov_b32 m0, %3\n\ts_nop 2\n\tglobal_load_lds_dword %1, %2\n\ts_mov_b32 m0, %0"
                 : "=&s"(keep) : "v"(voff), "s"(base), "s"(junk) : "memory");
}
DEV u32 aload_u16(const void* base, u32 voff) { u32 r; asm volatile("global_load_ushort %0, %1, %2" : "=v"(r) : "v"(voff), "s"(base) : "memory"); return r; }
DEV u32 aload_b32(const void* base, u32 voff) { u32 r; asm volatile("global_load_dword %0, %1, %2" : "=v"(r) : "v"(voff), "s"(base) : "memory"); return r; }
DEV u32x2 aload_b64_sc1(const void* base, u32 voff) { u32x2 r; asm volatile("global_load_dwordx2 %0, %1, %2 sc1" : "=v"(r) : "v"(voff), "s"(base) : "memory"); return r; }
DEV u32x4 aload_b128_sc1(const void* base, u32 voff) { u32x4 r; asm volatile("global_load_dwordx4 %0, %1, %2 sc1" : "=v"(r) : "v"(voff), "s"(base) : "memory"); return r; }
DEV u32x4 aload_b128(const void* base, u32 voff) { u32x4 r; asm volatile("global_load_dwordx4 %0, %1, %2" : "=v"(r) : "v"(voff), "s"(base) : "memory"); return r; }
DEV void astore_b128_sc1(void* base, u32 voff, u32x4 v) { asm volatile("global_store_dwordx4 %0, %1, %2 sc1\n\ts_nop 1" :: "v"(voff), "v"(v), "s"(base) : "memory"); }
DEV void astore_b64_sc1(void* base, u32 voff, u32x2 v) { asm volatile("global_store_dwordx2 %0, %1, %2 sc1\n\ts_nop 1" :: "v"(voff), "v"(v), "s"(base) : "memory"); }
DEV u32x4 trfrag(LAS char* a, LAS char* b) {
    const u32x2 lo = __builtin_bit_cast(u32x2, __builtin_amdgcn_ds_read_tr16_b64_v4i16((LAS s16x4*)a));
    const u32x2 hi = __builtin_bit_cast(u32x2, __builtin_amdgcn_ds_read_tr16_b64_v4i16((LAS s16x4*)b));
    return (u32x4){lo.x, lo.y, hi.x, hi.y};
}

#define TS(i)
#define TF(i)
#define TR(i)
#define TSOUT()

DEV void poll_flags(const char* flags, int first, int stride, int nfl, u32x2 tag, int lane) {
    const u32 off = (u32)(first + (lane < nfl ? lane : 0) * stride) * 128u;
#pragma unroll 1
    for (int spin = 0; spin < (1 << 16); ++spin) {
        u32x2 v = aload_b64_sc1(flags, off);
        asm volatile("s_waitcnt vmcnt(0)" : "+v"(v) :: "memory");
        if (__builtin_amdgcn_ballot_w64(v.x != tag.x || v.y != tag.y) == 0) break;
        __builtin_amdgcn_s_sleep(V_PSLEEP);
    }
}
DEV bool all_ge(u32 v, u32 target) { return __builtin_amdgcn_ballot_w64(v < target) == 0; }
DEV void lds_wait(LAS char* w, u32 target) {
#pragma unroll 1
    for (int spin = 0; spin < (1 << 21); ++spin) {
        if (all_ge(*(volatile LAS u32*)w, target)) break;
        __builtin_amdgcn_s_sleep(1);
    }
    asm volatile("" ::: "memory");
}
DEV void lds_post(LAS char* w, u32 v) { WAITL0(); *(volatile LAS u32*)w = v; }
template <int CTRL, int RM> DEV float dpp_add(float v) {
    return v + __builtin_bit_cast(float, __builtin_amdgcn_update_dpp(0, __builtin_bit_cast(int, v), CTRL, RM, 0xf, false));
}
template <int CTRL, int RM> DEV u32 dpp_umax(u32 v) { const u32 o = __builtin_amdgcn_update_dpp(0u, v, CTRL, RM, 0xf, false); return v > o ? v : o; }
DEV u32 wave_umax(u32 v) {
    v = dpp_umax<0xB1, 0xf>(v); v = dpp_umax<0x4E, 0xf>(v); v = dpp_umax<0x141, 0xf>(v); v = dpp_umax<0x140, 0xf>(v);
    v = dpp_umax<0x142, 0xa>(v); v = dpp_umax<0x143, 0xc>(v);
    return __builtin_amdgcn_readlane(v, 63);
}
DEV f32x2 cexp_f(float e2, float rev) {
    const float m = __builtin_amdgcn_exp2f(e2);
    return (f32x2){m * __builtin_amdgcn_cosf(rev), m * __builtin_amdgcn_sinf(rev)};
}
DEV f32x2 cmul_f(f32x2 a, f32x2 z) {
    f32x2 t, r;
    asm("v_pk_mul_f32 %0, %1, %2 op_sel_hi:[1,0]" : "=v"(t) : "v"(a), "v"(z));
    asm("v_pk_fma_f32 %0, %1, %2, %3 op_sel:[1,1,0] op_sel_hi:[0,1,1] neg_lo:[0,1,0]" : "=v"(r) : "v"(a), "v"(z), "v"(t));
    return r;
}
template <bool CONJ> DEV u32 cmh1(u32 a, u32 z) {
    u32 t;
    if (CONJ) asm("v_pk_mul_f16 %0, %1, %2 op_sel_hi:[1,0] neg_hi:[1,0]" : "=v"(t) : "v"(a), "v"(z));
    else asm("v_pk_mul_f16 %0, %1, %2 op_sel_hi:[1,0]" : "=v"(t) : "v"(a), "v"(z));
    return t;
}
template <bool CONJ> DEV u32 cmh2(u32 a, u32 z, u32 t) {
    u32 r;
    if (CONJ) asm("v_pk_fma_f16 %0, %1, %2, %3 op_sel:[1,1,0] op_sel_hi:[0,1,1] neg_lo:[0,1,0] neg_hi:[0,1,0]" : "=v"(r) : "v"(a), "v"(z), "v"(t));
    else asm("v_pk_fma_f16 %0, %1, %2, %3 op_sel:[1,1,0] op_sel_hi:[0,1,1] neg_lo:[0,1,0]" : "=v"(r) : "v"(a), "v"(z), "v"(t));
    return r;
}
DEV void build_table(LAS char* sm, int w, int lane, u32 are, u32 aim, u32 bre, u32 bim, u32 cre, u32 cim, u32 ldt, u32 dsk) {
    LAS char* t = sm + tab_off(w) + lane * 4;
    *(LAS u32*)(sm + L_MISC + 16 + w * 4) = dsk << 16;
    auto put = [&](int row, f32x2 v) { *(LAS u32*)(t + row * TAB_ROW) = pkh(v.x, v.y); };
    const float dtv = rbf(__builtin_amdgcn_exp2f(bf2f(ldt) * 1.4426950408889634f));
    const float a2 = dtv * bf2f(are) * 1.4426950408889634f;
    float rev = dtv * bf2f(aim) * 0.15915494309189535f;
    rev -= __builtin_rintf(rev);
    f32x2 wv = cmul_f((f32x2){bf2f(cre), bf2f(cim)}, (f32x2){bf2f(bre), bf2f(bim)});
    u32 E = wave_umax(__float_as_uint(fmaxf(dtv * fmaxf(fabsf(wv.x), fabsf(wv.y)) * __builtin_amdgcn_exp2f(fmaxf(a2, 0.f) * 2048.f), fabsf(bf2f(dsk)) * 0.0078125f))) >> 23;
    E = E < 16u ? 16u : (E > 236u ? 236u : E);
    wv *= dtv * __uint_as_float((260u - E) << 23);
    *(LAS float*)(sm + L_MISC + w * 4) = (float)(int)E - 133.f;
    const f32x2 z1 = cexp_f(a2, rev), z64 = cexp_f(a2 * 64.f, rev * 64.f);
    const f32x2 z2 = cmul_f(z1, z1), z3 = cmul_f(z2, z1), z4 = cmul_f(z2, z2), z8 = cmul_f(z4, z4), z16 = cmul_f(z8, z8), z32 = cmul_f(z16, z16);
    const f32x2 z128 = cmul_f(z64, z64), z256 = cmul_f(z128, z128), z512 = cmul_f(z256, z256);
    const f32x2 w1 = cmul_f(wv, z512), w2 = cmul_f(w1, z512);
    *(LAS u32*)t = 0x00003c00u;
    put(1, z1); put(2, z2); put(3, z3); put(4, z4); put(5, cmul_f(z4, z1)); put(6, cmul_f(z3, z3)); put(7, cmul_f(z4, z3)); put(8, z8);
    put(9, z16); put(10, cmul_f(z16, z8)); put(11, z32); put(12, z64); put(13, cmul_f(z64, z32));
    put(14, z128); put(15, z256); put(16, cmul_f(z256, z128));
    put(17, wv); put(18, w1); put(19, w2); put(20, cmul_f(w2, z512));
}
DEV u32 perm(u32 hi, u32 lo, u32 sel) { return __builtin_amdgcn_perm(hi, lo, sel); }

__global__ __launch_bounds__(512, 2) void fused_kernel(Params p) {
    __shared__ __attribute__((aligned(1024))) char shm[163840];
    LAS char* sm = (LAS char*)shm;
    const u32 lds0 = (u32)(uintptr_t)sm;
    const int wid = __builtin_amdgcn_readfirstlane(threadIdx.x >> 6);
    const int b = blockIdx.x, g = b & 7, r = b >> 3, hw = wid & 3;
    char* flags = p.ws + WS_FLAG;
    const int ch0 = 64 * g + 16 * (r & 3) + (r >> 2);
    const int tt = g * 4 + (r >> 3), dt = r & 7;
    const int qo = V_W2ROT ? 2 * (tt & 1) : 0;
    const int k0 = V_KROT == 2 ? (r % 7 + 2 * (r / 7)) % 7 : (V_KROT ? r % 7 + 2 * (r / 7) : 0);
    const int rot = (dt + (V_W2ROT ? tt & 2 : 0)) & 3;
    const bool isG = r < 28;
    const int mt = (g >> 1) * 4 + r / 7, ft = (g & 1) * 7 + r % 7;
#define LANEVALS() \
    int tid = threadIdx.x; asm volatile("" : "+v"(tid)); \
    const int lane = tid & 63, h = lane >> 5, l31 = lane & 31; \
    const u32 vsw = (u32)(((lane & 7) ^ (((lane >> 4) & 1) << 2)) << 4);          \
    const u32 voffw2 = (u32)((lane >> 3) * 1024 + dt * 128) + vsw; \
    auto w2_dma = [&](int kq, int sg) {        \
        glds14((const char*)p.w2 + (size_t)(4 * ((kq + qo) & 3) + ((sg + rot) & 3)) * (112 * 1024), voffw2, lds0 + L_W2 + kq * W2_REG + (sg & 1) * 14336); \
    }; \
    const int blk = (lane >> 4) & 1, q4 = (lane & 15) >> 2, pp = lane & 3; \
      \
    const u32 tra0 = (u32)((4 * h + q4) * 128 + (((2 * blk + (pp >> 1)) ^ (((q4 >> 1) & 1) << 2)) << 4) + 8 * (pp & 1)); \
    const u32 xo = (u32)((tt * 64 + (tid >> 3)) * 1024 + dt * 128 + (tid & 7) * 16); \
    (void)l31; (void)voffw2; (void)w2_dma; (void)tra0; (void)xo;
    TS(0); TR(62);
    if (wid == 0) *(LAS u32*)(sm + L_MISC + 64 + (threadIdx.x & 31) * 4) = 0;
    u32x2 tag1;
    u32x4 xe, b2e;
    u32 are, aim, bre, bim, cre, cim, ldt, dsk;
#define WAIT_PARAMS(n) asm volatile("s_waitcnt vmcnt(" #n ")" : "+v"(are), "+v"(aim), "+v"(bre), "+v"(bim), "+v"(cre), "+v"(cim), "+v"(ldt), "+v"(dsk) :: "memory")
#define LOADER_VALS() \
    const u32 xr = (u32)(lane >> 3) * 1024u, xs = (u32)(((lane & 7) ^ (lane >> 4)) << 4); \
    const u32 wr = (u32)(lane >> 4) * 3584u, ws_ = (u32)(((lane & 15) ^ ((lane >> 4) << 2)) << 4); \
    const u32 vx0 = xr + xs, vx1 = xr + (xs ^ 64u) + 8192u, vx2 = vx0 + 16384u, vx3 = vx1 + 16384u; \
    const u32 vw0 = wr + ws_, vw1 = wr + (ws_ ^ 16u) + 14336u, vw2 = wr + (ws_ ^ 32u) + 28672u, vw3 = wr + (ws_ ^ 48u) + 43008u; \
    auto stage = [&](int kc) {                 \
        const int ks = V_KROT == 2 ? (kc == 0 ? 0 : (kc + k0 > 7 ? kc + k0 - 7 : kc + k0)) : (kc + k0) & 7; \
        const char* xsrc = (const char*)p.x + (size_t)mt * 128 * 1024 + ks * 128; \
        const char* wsrc = (const char*)p.w1 + ft * 256 + ks * 64 * 3584; \
        const u32 d = lds0 + (kc & 3) * 32768; \
        _Pragma("unroll") for (int i = 0; i < 4; i++) { \
            glds4(xsrc + i * 32768, vx0, vx1, vx2, vx3, d + i * 4096); \
            glds4(wsrc + i * 16 * 3584, vw0, vw1, vw2, vw3, d + 16384 + i * 4096); \
        } \
    };
#define READ_TAG() do { u32x4 t_ = *(volatile LAS u32x4*)(sm + L_TAG); \
        if (!all_ge(t_.y, 1)) { lds_wait(sm + L_TAG + 4, 1); t_ = *(volatile LAS u32x4*)(sm + L_TAG); } \
        tag1.x = __builtin_amdgcn_readfirstlane(t_.z); tag1.y = __builtin_amdgcn_readfirstlane(t_.w); } while (0)
    const int lj = g * 4 + (r - 28);

    BAR();
    u32x2 tge;
    u32x4 xv[8];
    asm volatile("" : "=v"(tge), "=v"(xv[0]), "=v"(xv[1]), "=v"(xv[2]), "=v"(xv[3]), "=v"(xv[4]), "=v"(xv[5]), "=v"(xv[6]), "=v"(xv[7]));
    asm volatile("" : "=v"(are), "=v"(aim), "=v"(bre), "=v"(bim), "=v"(cre), "=v"(cim), "=v"(ldt), "=v"(dsk));
    const int tb8 = g * 32 + r;
    {
        LANEVALS();
        if (__builtin_expect(wid < 4, 1)) {
            const int chp = ch0 + 8 * (wid & 1);
            const u32 po = (u32)(chp * 128 + (wid >> 1) * 64 + lane) * 2u, c2 = (u32)chp * 2u;
            asm volatile("global_load_ushort %0, %8, %10\n\tglobal_load_ushort %1, %8, %11\n\tglobal_load_ushort %2, %8, %12\n\tglobal_load_ushort %3, %8, %13\n\t"
                         "global_load_ushort %4, %8, %14\n\tglobal_load_ushort %5, %8, %15\n\tglobal_load_ushort %6, %9, %16\n\tglobal_load_ushort %7, %9, %17"
                         : "+v"(are), "+v"(aim), "+v"(bre), "+v"(bim), "+v"(cre), "+v"(cim), "+v"(ldt), "+v"(dsk)
                         : "v"(po), "v"(c2), "s"(p.Are), "s"(p.Aim), "s"(p.Bre), "s"(p.Bim), "s"(p.Cre), "s"(p.Cim), "s"(p.logdt), "s"(p.Dskip) : "memory");
        }
        if (V_CODE && __builtin_expect(wid == 6, 1)) { const u32 ln = (u32)(r + 32 * (lane & 15)); touch((const char*)(__builtin_amdgcn_s_getpc() & ~127ull), (ln < CODE_LINES ? ln : 0u) * 128u, lds0 + L_JUNK); }
        if (__builtin_expect(wid == 7, 1)) {
            asm volatile("global_load_dwordx2 %0, %1, %2 sc1" : "+v"(tge) : "v"(FL_E * 128), "s"(flags) : "memory");
            asm volatile("global_load_dwordx4 %0, %8, %10\n\tglobal_load_dwordx4 %1, %8, %10 offset:1024\n\tglobal_load_dwordx4 %2, %8, %10 offset:2048\n\tglobal_load_dwordx4 %3, %8, %10 offset:3072\n\t"
                         "global_load_dwordx4 %4, %9, %10\n\tglobal_load_dwordx4 %5, %9, %10 offset:1024\n\tglobal_load_dwordx4 %6, %9, %10 offset:2048\n\tglobal_load_dwordx4 %7, %9, %10 offset:3072"
                         : "+v"(xv[0]), "+v"(xv[1]), "+v"(xv[2]), "+v"(xv[3]), "+v"(xv[4]), "+v"(xv[5]), "+v"(xv[6]), "+v"(xv[7])
                         : "v"(lane * 16), "v"(lane * 16 + 4096), "s"((const char*)p.x + (size_t)tb8 * 8192) : "memory");
        }
        if (V_MALL == 1 && wid == 6) touch((const char*)p.w2 + (size_t)b * 7168, (u32)(lane < 56 ? lane : 0) * 128u, lds0 + L_JUNK);
        if (__builtin_expect(isG && (wid == 4 || wid == 5), 1)) {
            LOADER_VALS();
            if (wid == 5) __builtin_amdgcn_s_sleep(7);
            stage(hw);
        } else if (wid == 4 || wid == 5) {
            const char* wb = (const char*)p.w1 + (lj < 28 ? lj : 0) * 128 + (size_t)hw * 256 * 3584;
            const u32 v0 = (u32)((lane >> 3) * 3584) + vsw;
#pragma unroll 1
            for (int i = 0; i < 8; i++) glds4(wb + i * 32 * 3584, v0, v0 + 8 * 3584, v0 + 16 * 3584, v0 + 24 * 3584, lds0 + hw * 32768 + i * 4096);
        }
    }

    if (wid < 4) {
        LANEVALS();
        const int kq = wid;
        u32x4 wreg[14][2];
        LAS char* w2a = sm + L_W2 + kq * W2_REG;
        auto w2frag = [&](int s, int j2) { LAS char* a = w2a + (j2 ? tra0 ^ 64 : tra0) + s * 2048; return trfrag(a, a + 1024); };
        const int th = wid & 1, fh = wid >> 1;
        const u32 bo = (u32)(ft * 128 + fh * 64 + l31) * 2u;
        u32 b1a = aload_u16(p.b1, bo), b1b = aload_u16(p.b1, bo + 64);
        PRIO(V_MPRIO);
        u32 xa[4], wa[2][2];
#pragma unroll
        for (int s = 0; s < 4; s++) xa[s] = (u32)((64 * th + l31) * 128 + (((2 * s + h) ^ ((l31 >> 1) & 7)) << 4));
#pragma unroll
        for (int j2 = 0; j2 < 2; j2++)
#pragma unroll
            for (int t = 0; t < 2; t++)
                wa[j2][t] = (u32)(16384 + 256 * (8 * h + 4 * t + q4) + (((4 * (2 * fh + j2) + 2 * blk + (pp >> 1)) ^ ((q4 << 2) | (2 * h + t))) << 4) + 8 * (pp & 1));
        u32 so = GR_ST + (u32)(mt * 128 + th * 64 + l31) * 16u, co = GR_CS + (u32)(ft * 128 + fh * 64 + l31) * 8u;
        asm volatile("" : "+v"(so), "+v"(co), "+v"(xa[0]), "+v"(xa[1]), "+v"(xa[2]), "+v"(xa[3]), "+v"(wa[0][0]), "+v"(wa[0][1]), "+v"(wa[1][0]), "+v"(wa[1][1]));
        WAIT_PARAMS(2);
        TS(1);
        build_table(sm, wid, lane, are, aim, bre, bim, cre, cim, ldt, dsk);
        LAS char* fm = sm + F_M + wid * 4;
        LAS char* fl = sm + F_L;
        lds_post(fm, isG ? 1 : 10);
        TS(2);
        if (isG) {
            f32x16 acc[2][2] = {};
            lds_wait(fl, 1);
            TS(3);
            u32x4 X[4][2], W[4][2];
            u32 wp[2][2], xp;
#define RX(s, j) X[s][j] = *(LAS u32x4*)(sm + xp + (j) * 4096)
#define RW(s, j) W[s][j] = trfrag(sm + wp[j][0] + (s) * 4096, sm + wp[j][1] + (s) * 4096)
#define OPQ(v) asm volatile("" : "+v"(v))
#define MM(s, j2, j) acc[j2][j] = MFMA32(W[s][j2], X[s][j], acc[j2][j])
            u32x4 st0, st1; u32x2 cs0, cs1;
#define LOAD_GRAN() asm volatile("global_load_dwordx4 %0, %4, %6 sc1\n\tglobal_load_dwordx4 %1, %4, %6 offset:512 sc1\n\tglobal_load_dwordx2 %2, %5, %6 sc1\n\tglobal_load_dwordx2 %3, %5, %6 offset:256 sc1" \
                                 : "=&v"(st0), "=&v"(st1), "=&v"(cs0), "=&v"(cs1) : "v"(so), "v"(co), "s"(flags) : "memory")
            wp[0][0] = wa[0][0]; wp[0][1] = wa[0][1]; wp[1][0] = wa[1][0]; wp[1][1] = wa[1][1];
            xp = xa[0]; RX(0, 0); SCHED(); RW(0, 0); SCHED(); RW(0, 1); SCHED(); RX(0, 1); SCHED();
            xp = xa[1]; RX(1, 0); SCHED(); RW(1, 0); SCHED(); RW(1, 1); SCHED(); RX(1, 1); SCHED();
            xp = xa[2];
#pragma unroll
            for (int kc = 0; kc < 7; kc++) {
                const u32 sl = (u32)(kc & 3) * 32768u, sn = (u32)((kc + 1) & 3) * 32768u;
                if (kc == 5) LOAD_GRAN();
                MM(0, 0, 0); RX(2, 0); SCHED(); MM(0, 1, 0); RW(2, 0); SCHED(); MM(0, 0, 1); RW(2, 1); SCHED();
                MM(0, 1, 1); RX(2, 1); xp = xa[3] + sl; OPQ(xp); SCHED();
                MM(1, 0, 0); RX(3, 0);
                LAS char* fn = fl + ((kc + 1) & 1) * 4;
                const u32 full = *(volatile LAS u32*)fn;
                SCHED();
                MM(1, 1, 0); RW(3, 0); SCHED(); MM(1, 0, 1); RW(3, 1); SCHED();
                MM(1, 1, 1); RX(3, 1); xp = xa[0] + sn; wp[0][0] = wa[0][0] + sn; wp[0][1] = wa[0][1] + sn; OPQ(xp); OPQ(wp[0][0]); OPQ(wp[0][1]); SCHED();
                MM(2, 0, 0); SCHED();
                if (__builtin_expect(!all_ge(full, (kc + 3) >> 1), 0)) { TS(48 + kc); lds_wait(fn, (kc + 3) >> 1); TS(11 + kc); }
                asm volatile("" ::: "memory");
                SCHED();
                RX(0, 0); wp[1][0] = wa[1][0] + sn; wp[1][1] = wa[1][1] + sn; OPQ(wp[1][0]); OPQ(wp[1][1]); SCHED();
                MM(2, 1, 0); RW(0, 0); SCHED(); MM(2, 0, 1); RW(0, 1); SCHED();
                MM(2, 1, 1); RX(0, 1); xp = xa[1] + sn; OPQ(xp); SCHED();
                MM(3, 0, 0); RX(1, 0); SCHED(); MM(3, 1, 0); RW(1, 0); SCHED(); MM(3, 0, 1); RW(1, 1); SCHED();
                MM(3, 1, 1); RX(1, 1); xp = xa[2] + sn; OPQ(xp);
                *(volatile LAS u32*)fm = kc + 2;
                SCHED();
                TF(4 + kc);
            }
            MM(0, 0, 0); RX(2, 0); RW(2, 0); SCHED(); MM(0, 1, 0); RW(2, 1); RX(2, 1); xp = xa[3] + 3 * 32768; SCHED();
            MM(0, 0, 1); RX(3, 0); RW(3, 0); SCHED(); MM(0, 1, 1); RW(3, 1); RX(3, 1); SCHED();
            MM(1, 0, 0); MM(1, 1, 0); SCHED();
            READ_TAG();
            lds_post(fm, 9);
            asm volatile("s_waitcnt vmcnt(0)" : "+v"(st0), "+v"(st1), "+v"(cs0), "+v"(cs1), "+v"(b1a), "+v"(b1b) :: "memory");
#pragma unroll 1
            for (int spin = 0; spin < (1 << 16); ++spin) {
                if (__builtin_amdgcn_ballot_w64((((st0.y ^ tag1.x) | (st0.w ^ tag1.x) | (st1.y ^ tag1.x)) | ((st1.w ^ tag1.x) | (cs0.y ^ tag1.x) | (cs1.y ^ tag1.x))) != 0u) == 0) break;
                __builtin_amdgcn_s_sleep(2);
                LOAD_GRAN();
                asm volatile("s_waitcnt vmcnt(0)" : "+v"(st0), "+v"(st1), "+v"(cs0), "+v"(cs1) :: "memory");
            }
            TS(19);
            float rinv[2] = {__uint_as_float(st0.z), __uint_as_float(st1.z)};
            float k1[2], k2[2];
            {
                u32x4 Ax[2], Bx[2];
                auto prepB = [&](int j) {
                    const float nm = __uint_as_float(j ? st1.x : st0.x), mh = rbf(nm), ih = rbf(rinv[j]);
                    const u32 mm = pkbf(mh, opq(nm - mh));
                    const u32 hm = (u32)h - 1u;
                    Bx[j] = (u32x4){mm & hm, mm & hm, pkbf(ih, opq(rinv[j] - ih)) & hm, 0u};
                };
                auto prepA = [&](int j) {
                    const float c = __uint_as_float(j ? cs1.x : cs0.x), chh = rbf(c), cl = opq(c - chh);
                    const u32 bb = j ? b1b : b1a;
                    Ax[j] = (u32x4){pkbf(chh, chh), pkbf(cl, cl), bb | (bb << 16), 0u};
                };
                auto prepK = [&](int j) {
                    const float rr = __builtin_amdgcn_rcpf(rinv[j]);
                    k1[j] = opq(-2.302208198144325f * rr); k2[j] = opq(-0.10294323958002349f * rr * rr * rr);
                };
                MM(1, 0, 1); MM(1, 1, 1); prepB(0); SCHED();
                MM(2, 0, 0); prepA(0); SCHED(); MM(2, 1, 0); prepA(1); SCHED(); MM(2, 0, 1); MM(2, 1, 1); prepB(1); SCHED();
                MM(3, 0, 0); prepK(0); SCHED(); acc[0][0] = MFMA32(Ax[0], Bx[0], acc[0][0]); prepK(1); SCHED();
                MM(3, 1, 0); acc[1][0] = MFMA32(Ax[1], Bx[0], acc[1][0]);
                MM(3, 0, 1); acc[0][1] = MFMA32(Ax[0], Bx[1], acc[0][1]);
                MM(3, 1, 1); acc[1][1] = MFMA32(Ax[1], Bx[1], acc[1][1]);
                SCHED();
            }
            PRIO(2);
            TS(20);
            char* hdst = p.ws + WS_H + (size_t)((mt * 4 + th * 2) * 112 + ft * 8 + fh * 4) * 1024;
#pragma unroll
            for (int j = 0; j < 2; j++) {
                const f32x2 K1 = {k1[j], k1[j]}, K2 = {k2[j], k2[j]}, RI = {rinv[j], rinv[j]};
#pragma unroll
                for (int j2 = 0; j2 < 2; j2++) {
#pragma unroll
                    for (int s = 0; s < 2; s++) {
                        u32x4 o;
#pragma unroll
                        for (int i = 0; i < 4; i++) {
                            const f32x2 u = {acc[j2][j][8 * s + 2 * i], acc[j2][j][8 * s + 2 * i + 1]};
                            const f32x2 z = __builtin_elementwise_fma(u * u, K2, K1) * u;
                            const f32x2 e = {__builtin_amdgcn_exp2f(z.x), __builtin_amdgcn_exp2f(z.y)};
                            const f32x2 dn = __builtin_elementwise_fma(e, RI, RI);
                            const f32x2 gl = u * (f32x2){__builtin_amdgcn_rcpf(dn.x), __builtin_amdgcn_rcpf(dn.y)};
                            o[i] = pkbf(gl.x, gl.y);
                        }
                        asm volatile("global_store_dwordx4 %0, %1, %2 offset:%3 sc1\n\ts_nop 1" :: "v"(lane * 16 + j * 112 * 1024), "v"(o), "s"(hdst), "i"((j2 * 2 + s) * 1024) : "memory");
                        SCHED();
                    }
                }
            }
            lds_post(fm, 10);
            TS(21);
        } else { WAITV(0); READ_TAG(); }
        lds_wait(fl + (lane & 1) * 4, 5);
        PRIO(1);
#pragma unroll
        for (int s = 0; s < 14; s++) { wreg[s][0] = w2frag(s, 0); wreg[s][1] = w2frag(s, 1); }
#pragma unroll
        for (int s = 0; s < 14; s++) asm volatile("" : "+v"(wreg[s][0]), "+v"(wreg[s][1]));
        if (!isG) lds_wait(sm + F_K + (lane & 1) * 4, 2);
        WAITL0();
        TS(22);
        WAITV(0);
        if (isG) astore_b64_sc1(flags, (u32)(FL_H + (mt * 2 + (wid & 1)) * 32 + ft * 2 + (wid >> 1)) * 128u, tag1);
        TS(24);
        w2_dma(kq, 2); w2_dma(kq, 3);
        TS(18);
        poll_flags(flags, FL_H + tt * 32 + 7 * ((kq + qo) & 3), 1, 7, tag1, lane);
        TS(25);
        const __amdgpu_buffer_rsrc_t hr = __builtin_amdgcn_make_buffer_rsrc(p.ws + WS_H + (size_t)(tt * 2 * 112 + ((kq + qo) & 3) * 28) * 1024, 0, 2 * 112 * 1024, 0x00020000);
        constexpr int WIN = 8;
        u32x4 hf[WIN][2];
        f32x16 acc[2][2] = {};
        auto ldh = [&](int s, int j) {
            return __builtin_amdgcn_raw_buffer_load_b128(hr, lane * 16 + j * 112 * 1024 + ((s % 7) & 3) * 1024, ((s / 7 + rot) & 3) * 7168 + ((s % 7) >> 2) * 4096, 16);
        };
#pragma unroll
        for (int s = 0; s < WIN; s++) { hf[s][0] = ldh(s, 0); SCHED(); hf[s][1] = ldh(s, 1); SCHED(); }
        TS(26);
#pragma unroll
        for (int s = 0; s < 28; s++) {
            if (s == 12) { TS(27); }
            if (s == V_SSTEP) *(volatile LAS u32*)(sm + F_M + wid * 4) = 11;
            const u32x4 w0 = wreg[s % 14][0], w1 = wreg[s % 14][1];
            acc[0][0] = MFMA32(w0, hf[s % WIN][0], acc[0][0]); SCHED();
            acc[1][0] = MFMA32(w1, hf[s % WIN][0], acc[1][0]);
            if (s + WIN < 28) hf[s % WIN][0] = ldh(s + WIN, 0);
            SCHED();
            acc[0][1] = MFMA32(w0, hf[s % WIN][1], acc[0][1]);
            if (s >= 12 && s < 26) wreg[s - 12][0] = w2frag(s - 12, 0);
            SCHED();
            acc[1][1] = MFMA32(w1, hf[s % WIN][1], acc[1][1]);
            if (s >= 12 && s < 26) wreg[s - 12][1] = w2frag(s - 12, 1);
            if (s + WIN < 28) hf[s % WIN][1] = ldh(s + WIN, 1);
            if (s + WIN == 28) { LANEVALS(); xe = *(const u32x4*)((const char*)p.x + xo); b2e = *(const u32x4*)((const char*)p.b2 + dt * 128 + (tid & 7) * 16); }
            SCHED();
        }
        TS(29);
        int t2 = threadIdx.x; asm volatile("" : "+v"(t2));
        LAS char* pw = w2a + (t2 & 31) * 272 + ((t2 >> 5) & 1) * 16;
#pragma unroll
        for (int j2 = 0; j2 < 2; j2++)
#pragma unroll
            for (int j = 0; j < 2; j++)
#pragma unroll
                for (int rg = 0; rg < 4; rg++) {
                    f32x4 v = {acc[j2][j][4 * rg], acc[j2][j][4 * rg + 1], acc[j2][j][4 * rg + 2], acc[j2][j][4 * rg + 3]};
                    *(LAS f32x4*)(pw + 32 * j * 272 + (32 * j2 + 8 * rg) * 4) = v;
                }
        TS(32);
        asm volatile("" : "=v"(tge), "=v"(xv[0]), "=v"(xv[1]), "=v"(xv[2]), "=v"(xv[3]), "=v"(xv[4]), "=v"(xv[5]), "=v"(xv[6]), "=v"(xv[7]));
    }
    int wid2 = wid; asm volatile("" : "+s"(wid2));
    if (wid2 >= 4) {
        LANEVALS();
        const int a = lane & 15, q = lane >> 4;
        const int cl = hw & 1, rh = hw >> 1, chn = ch0 + 8 * cl;
        if (wid >= 6) {
            const int kc = wid & 1;
            PRIO(V_KPRIO);
            if (kc == 0) {
                if (V_MALL == 2) { __builtin_amdgcn_s_sleep(V_MDELAY); touch((const char*)p.w2 + (size_t)b * 7168, (u32)(lane < 56 ? lane : 0) * 128u, lds0 + L_JUNK); WAITV(0); }
                READ_TAG();
                TS(41);
            } else {
                asm volatile("s_waitcnt vmcnt(0)" : "+v"(tge), "+v"(xv[0]), "+v"(xv[1]), "+v"(xv[2]), "+v"(xv[3]), "+v"(xv[4]), "+v"(xv[5]), "+v"(xv[6]), "+v"(xv[7]) :: "memory");
                tag1.x = __builtin_amdgcn_readfirstlane(tge.x) + 1u; tag1.y = __builtin_amdgcn_readfirstlane(tge.y);
                TS(41);
                *(volatile LAS u32x2*)(sm + L_TAG + 8) = tag1;
                lds_post(sm + L_TAG + 4, 1);
                float s1[8], s2[8];
#pragma unroll
                for (int i = 0; i < 8; i++) { s1[i] = 0.f; s2[i] = 0.f; }
#pragma unroll
                for (int j = 0; j < 4; j++)
#pragma unroll
                    for (int i = 0; i < 8; i++) { s1[i] = dot2(xv[i][j], 0x3f803f80u, s1[i]); s2[i] = dot2(xv[i][j], xv[i][j], s2[i]); }
                auto red8 = [&](float (&v)[8]) {
                    float a[4], c[2];
#pragma unroll
                    for (int i = 0; i < 4; i++) {
                        const auto t = __builtin_amdgcn_permlane32_swap(__float_as_uint(v[i]), __float_as_uint(v[i + 4]), false, false);
                        a[i] = __uint_as_float(t[0]) + __uint_as_float(t[1]);
                    }
#pragma unroll
                    for (int i = 0; i < 2; i++) {
                        const auto t = __builtin_amdgcn_permlane16_swap(__float_as_uint(a[i]), __float_as_uint(a[i + 2]), false, false);
                        c[i] = dpp_add<0x128, 0xf>(__uint_as_float(t[0]) + __uint_as_float(t[1]));
                    }
                    float e = (lane & 8) ? c[1] : c[0];
                    e = dpp_add<0x141, 0xf>(e); e = dpp_add<0xB1, 0xf>(e); e = dpp_add<0x4E, 0xf>(e);
                    return e;
                };
                const float a1 = red8(s1), a2 = red8(s2);
                const float mean = a1 * (1.f / 512.f);
                const float v1 = fmaxf(a2 * (1.f / 512.f) - mean * mean, 0.f);
                const float q1 = v1 + 1e-5f;
                const float rs = __builtin_amdgcn_rsqf(q1);
                const float q2 = v1 * rs * rs + 1e-5f;
                {
                    u32x4 o = {__float_as_uint(-mean), tag1.x, __float_as_uint(__builtin_amdgcn_sqrtf(q1 * q2)), tag1.x};
                    astore_b128_sc1(flags, GR_ST + (u32)(tb8 * 8 + (lane >> 3)) * 16u, o);
                }
                TS(42);
                f32x2 sc[4], of[4];
                const float nmr = -mean * rs;
#pragma unroll
                for (int i = 0; i < 4; i++) {
                    sc[i] = (f32x2){__builtin_bit_cast(float, __builtin_amdgcn_readlane(__builtin_bit_cast(int, rs), 16 * i)), __builtin_bit_cast(float, __builtin_amdgcn_readlane(__builtin_bit_cast(int, rs), 16 * i + 8))};
                    of[i] = (f32x2){__builtin_bit_cast(float, __builtin_amdgcn_readlane(__builtin_bit_cast(int, nmr), 16 * i)), __builtin_bit_cast(float, __builtin_amdgcn_readlane(__builtin_bit_cast(int, nmr), 16 * i + 8))};
                }
#pragma unroll
                for (int c = 0; c < 8; c++) {
                    u32x4 o;
#pragma unroll
                    for (int i = 0; i < 4; i++) {
                        const u32 a = xv[2 * i][c >> 1], d = xv[2 * i + 1][c >> 1];
                        const f32x2 v = (c & 1) ? (f32x2){bfhi(a), bfhi(d)} : (f32x2){bflo(a), bflo(d)};
                        const f32x2 n = __builtin_elementwise_fma(v, sc[i], of[i]);
                        o[i] = pkh(n.x, n.y);
                    }
                    astore_b128_sc1(p.ws + WS_U + (size_t)tb8 * 8192, (u32)(c * 1024 + lane * 16), o);
                }
                TS(45);
            }
            const int rS = (int)((0x8080840876543210ull >> (4 * a)) & 15), rB = 17 + (int)((0xfa540000u >> (2 * a)) & 3);
            const int rT1 = (a & 3) ? 7 + (a & 3) : 0, rT2 = (a >> 2) ? 10 + (a >> 2) : 0;
            LAS char* tb = sm + L_TAB + kc * TAB_SZ + q * 16;
            LAS char *pS = tb + rS * TAB_ROW, *pB = tb + rB * TAB_ROW, *pT1 = tb + rT1 * TAB_ROW, *pT2 = tb + rT2 * TAB_ROW, *pZ = tb + 14 * TAB_ROW;
            const int hd = tab_off(kc + 2) - tab_off(kc);
            f32x4 G[2][4] = {};
            u32x4 T[2][7];
            auto ldtab = [&](int k) {
                if (k == 4) { pS += hd; pB += hd; pT1 += hd; pT2 += hd; pZ += hd; }
                u32x4* t = T[k & 1];
                const int o = (k & 3) * 64;
                t[0] = *(LAS u32x4*)(pS + o); t[1] = *(LAS u32x4*)(pB + o); t[2] = *(LAS u32x4*)(pT1 + o); t[3] = *(LAS u32x4*)(pT2 + o);
                t[4] = *(LAS u32x4*)(pZ + o); t[5] = *(LAS u32x4*)(pZ + o + TAB_ROW); t[6] = *(LAS u32x4*)(pZ + o + 2 * TAB_ROW);
            };
            lds_wait(sm + F_M + (lane & 3) * 4, 1);
            TS(3);
            ldtab(0);
#pragma unroll
            for (int k = 0; k < 8; k++) {
                u32x4* t = T[k & 1];
                f32x4* Gh = G[k >> 2];
                u32x4 A, B0, B1, B2, B3;
                asm volatile("" :: "v"(t[0]), "v"(t[1]), "v"(t[2]), "v"(t[3]), "v"(t[4]), "v"(t[5]), "v"(t[6]));
                if (k < 7) ldtab(k + 1);
                SCHED();
#pragma unroll
                for (int j = 0; j < 4; j++) { B0[j] = cmh2<false>(t[2][j], t[3][j], cmh1<false>(t[2][j], t[3][j])); A[j] = cmh2<true>(t[0][j], t[1][j], cmh1<true>(t[0][j], t[1][j])); }
                SCHED();
#pragma unroll
                for (int j = 0; j < 4; j++) B1[j] = cmh2<false>(B0[j], t[4][j], cmh1<false>(B0[j], t[4][j]));
                SCHED();
                Gh[0] = MFMA16H(A, B0, Gh[0]);
#pragma unroll
                for (int j = 0; j < 4; j++) B2[j] = cmh2<false>(B0[j], t[5][j], cmh1<false>(B0[j], t[5][j]));
                SCHED();
                Gh[1] = MFMA16H(A, B1, Gh[1]);
#pragma unroll
                for (int j = 0; j < 4; j++) B3[j] = cmh2<false>(B0[j], t[6][j], cmh1<false>(B0[j], t[6][j]));
                SCHED();
                Gh[2] = MFMA16H(A, B2, Gh[2]);
                asm volatile("s_nop 1" : "+v"(B3));
                Gh[3] = MFMA16H(A, B3, Gh[3]);
                SCHED();
                if (k == 1 && kc) { WAITV(0); astore_b64_sc1(flags, (u32)(FL_LN + tb8 * 8), tag1); TS(47); }
            }
            lds_post(sm + F_K + kc * 4, 1);
            lds_wait(sm + F_K + (lane & 1) * 4, 1);
            TS(20);
            {
                const float M0 = *(LAS float*)(sm + L_MISC + kc * 4), M1 = *(LAS float*)(sm + L_MISC + 8 + kc * 4), M = fmaxf(M0, M1);
                const float s0 = __builtin_amdgcn_exp2f(M0 - M), s1 = __builtin_amdgcn_exp2f(M1 - M);
                const float dx = *(LAS float*)(sm + L_MISC + 16 + kc * 4) * __builtin_amdgcn_exp2f(-M), d0 = lane == 0 ? dx : 0.f;
                LAS char* gx = sm + L_XCH + kc * 2048;
#pragma unroll
                for (int ct = 0; ct < 4; ct++) {
                    float v[4];
#pragma unroll
                    for (int e = 0; e < 4; e++) v[e] = opq(fmaf(G[1][ct][e], s1, (ct | e) ? opq(G[0][ct][e] * s0) : fmaf(G[0][ct][e], s0, d0)));
                    u32x2 o = {pkh(v[0], v[1]), pkh(v[2], v[3])};
                    *(LAS u32x2*)(gx + (16 * ct + a) * 32 + q * 8) = o;
                }
                WAITL0();
                const u32x4 u = *(LAS u32x4*)(gx + lane * 32), v = *(LAS u32x4*)(gx + lane * 32 + 16);
                LAS char* kp = sm + L_KP + kc * KP_CH + (lane & 1) * KP_PLANE + (16 + (lane >> 1)) * 16;
                const u32 LL = 0x01000100u, HH = 0x03020302u, SW = 0x01000302u;
                const u32 sa = h ? 0x05040100u : 0x05040706u, sb = h ? LL : SW, tie = (lane & 1) ? HH : SW;
                u32x4 w0 = {perm(u.w, v.x, sa), perm(0, u.z, sb), perm(u.y, u.z, sa), perm(0, u.x, sb)};
                u32x4 w1 = {perm(0, v.y, HH), perm(0, v.y, LL), perm(v.x, v.y, 0x07060100u), perm(0, v.x, HH)};
                u32x4 w2 = {perm(0, v.z, HH), perm(0, v.z, tie), perm(0, v.z, LL), perm(0, v.z, LL)};
                u32x4 w3 = {perm(0, v.w, HH), perm(0, v.w, tie), perm(0, v.w, LL), perm(0, v.w, LL)};
                *(LAS u32x4*)kp = w0; *(LAS u32x4*)(kp + 512) = w1; *(LAS u32x4*)(kp + 1024) = w2; *(LAS u32x4*)(kp + 1536) = w3;
                u32x4 z = {0, 0, 0, 0};
                *(LAS u32x4*)(sm + L_KP + kc * KP_CH + (q & 1) * KP_PLANE + a * 16) = z;
            }
            TS(21);
            if (kc == 0) {
                lds_wait(sm + F_M + (lane & 3) * 4, V_UAT);
#pragma unroll 1
                for (int spin = 0; spin < (1 << 16); ++spin) {
                    u32x4 f0 = aload_b128_sc1(flags, (u32)(FL_LN + lane * 16)), f1 = aload_b128_sc1(flags, (u32)(FL_LN + 1024 + lane * 16));
                    asm volatile("s_waitcnt vmcnt(0)" : "+v"(f0), "+v"(f1) :: "memory");
                    if (__builtin_amdgcn_ballot_w64((((f0.x ^ tag1.x) | (f0.z ^ tag1.x) | (f1.x ^ tag1.x) | (f1.z ^ tag1.x)) | ((f0.y ^ tag1.y) | (f0.w ^ tag1.y) | (f1.y ^ tag1.y) | (f1.w ^ tag1.y))) != 0u) == 0) break;
                    __builtin_amdgcn_s_sleep(V_PSLEEP);
                }
                lds_post(sm + F_P, 1);
                if (b == 0) astore_b64_sc1(flags, FL_E * 128, tag1);
            }
            else lds_wait(sm + F_P, 1);
            TS(22);
            const int tb0 = kc * 128 + (lane >> 1);
            const u32 ob = (u32)((r >> 2) * 1024 + (8 * g + 2 * (r & 3) + (lane & 1)) * 16), o0 = ob + (u32)tb0 * 8192u;
            u32x4 uv[4];
#pragma unroll
            for (int i = 0; i < 4; i++) uv[i] = aload_b128_sc1(p.ws + WS_U, o0 + i * 32 * 8192);
            u32x2 pv = aload_b64_sc1(p.ws + WS_U, ob + 127u * 8192u + 8u);
            asm volatile("s_waitcnt vmcnt(0)" : "+v"(uv[0]), "+v"(uv[1]), "+v"(uv[2]), "+v"(uv[3]), "+v"(pv) :: "memory");
            lds_post(sm + F_K + kc * 4, 2);
            PRIO(3);
            lds_wait(sm + F_M + (lane & 3) * 4, 9);
            TS(26);
            u32 r2[5], r3[5];
            r2[0] = kc ? pv.x : 0u; r3[0] = kc ? pv.y : 0u;
#pragma unroll
            for (int i = 0; i < 4; i++) {
                r2[i + 1] = __builtin_amdgcn_ds_bpermute(((lane - 2) & 63) * 4, uv[i].z);
                r3[i + 1] = __builtin_amdgcn_ds_bpermute(((lane - 2) & 63) * 4, uv[i].w);
            }
            LAS char* d = sm + L_UC + (lane & 1) * UC_CH + (tb0 + 4) * 16;
#pragma unroll
            for (int i = 0; i < 4; i++) *(LAS u32x4*)(d + i * 512) = uv[i];
#pragma unroll
            for (int i = 0; i < 4; i++) {
                const u32x4 t = uv[i];
                const u32 q2 = lane < 2 ? r2[i] : r2[i + 1], q3 = lane < 2 ? r3[i] : r3[i + 1];
                const u32 a0 = __builtin_amdgcn_alignbit(q3, q2, 16), a1 = __builtin_amdgcn_alignbit(t.x, q3, 16), a2 = __builtin_amdgcn_alignbit(t.y, t.x, 16),
                          a3 = __builtin_amdgcn_alignbit(t.z, t.y, 16), a4 = __builtin_amdgcn_alignbit(t.w, t.z, 16);
                u32x4 c1 = {a1, a2, a3, a4}, c2 = {q3, t.x, t.y, t.z}, c3 = {a0, a1, a2, a3};
                *(LAS u32x4*)(d + i * 512 + UC_COPY) = c1;
                *(LAS u32x4*)(d + i * 512 + 2 * UC_COPY) = c2;
                *(LAS u32x4*)(d + i * 512 + 3 * UC_COPY) = c3;
            }
            u32x4 z = {0, 0, 0, 0};
            *(LAS u32x4*)(sm + L_UC + kc * UC_CH + ((lane >> 2) & 3) * UC_COPY + (lane & 3) * 16) = z;
            lds_post(sm + F_K + kc * 4, 3);
            TS(35);
        } else {
            PRIO(3);
            LOADER_VALS();
            LAS char* fm = sm + F_M + (lane & 3) * 4;
            LAS char* fl = sm + F_L + hw * 4;
            u32 junk = 0;
            TS(4); WAITV(0);
            if (!isG) {
                f32x16 c0 = {}, c1 = {};
                const u32x4 ones = {0x3f803f80u, 0x3f803f80u, 0x3f803f80u, 0x3f803f80u};
#pragma unroll 4
                for (int s = 0; s < 16; s++) {
                    LAS char* a = sm + hw * 32768 + s * 2048;
                    c0 = MFMA32(ones, trfrag(a + tra0, a + tra0 + 1024), c0);
                    c1 = MFMA32(ones, trfrag(a + (tra0 ^ 64), a + (tra0 ^ 64) + 1024), c1);
                }
                const float c = h ? c1[0] : c0[0];
                *(LAS float*)(sm + L_UC + hw * 256 + lane * 4) = c;
                lds_post(fl, 1);
                READ_TAG();
                lds_wait(sm + F_L + (lane & 1) * 4, 1);
                u32x2 o = {__float_as_uint(*(LAS float*)(sm + L_UC + lane * 4) + *(LAS float*)(sm + L_UC + 256 + lane * 4)), tag1.x};
                if (hw == 0 && lj < 28) astore_b64_sc1(flags, GR_CS + (u32)(lj * 64 + lane) * 8u, o);
            } else lds_post(fl, 1);
            TS(5);
#pragma unroll 1
            for (int kc = isG ? 2 + hw : 8; kc < 8; kc += 2) {
                if (kc >= 4) lds_wait(fm, kc - 2);
                TF(2 * kc + 2);
                stage(kc);
                TF(2 * kc + 3);
                WAITV(0); lds_post(fl, (kc >> 1) + 1); TF(48 + kc);
                if (V_TOUCH && kc == V_TOUCH + r / 7) {
                    u32 o = (u32)lane * 128u;
                    asm volatile("global_load_dword %0, %1, %2\n\t.rept 7\n\tv_add_u32 %1, 0x2000, %1\n\tglobal_load_dword %0, %1, %2\n\t.endr"
                                 : "+v"(junk), "+v"(o) : "s"(uni((const char*)p.w2 + (size_t)r * 65536)) : "memory");
                }
            }
#pragma unroll 1
            for (int kq = hw; kq < 4; kq += 2) {
                lds_wait(fm, 6 + kq);
                w2_dma(kq, 0); w2_dma(kq, 1);
                WAITV(0);
            }
            asm volatile("" : "+v"(junk));
            READ_TAG();
            TS(24);
            WAITV(0);
            lds_post(fl, 5);
            TS(25);
        }
        lds_wait(sm + F_K + (lane & 1) * 4, 3);
        TS(36);
        LAS char* ub = sm + L_UC + cl * UC_CH + (3 - (a & 3)) * UC_COPY + (32 * rh + 2 * q + 5 + (a >> 2)) * 8;
        LAS char* kb = sm + L_KP + cl * KP_CH + ((q + 1) & 1) * KP_PLANE + (16 - 8 * rh - 6 + a - (q >> 1)) * 16;
        f32x4 y[8] = {};
        auto ldA = [&](int dl, int i) {
            const u32x2 lo = *(volatile LAS u32x2*)(ub + dl * 64 + i * 512), hi = *(volatile LAS u32x2*)(ub + dl * 64 + i * 512 + 8);
            return (u32x4){lo.x, lo.y, hi.x, hi.y};
        };
        auto ldB = [&](int dl, int i) { return *(LAS u32x4*)(kb + (3 - dl) * 32 + i * 256); };
        u32x4 A0[8], B0[8], A1[8], B1[8];
#pragma unroll
        for (int i = 0; i < 8; i++) { B0[i] = ldB(0, i); A0[i] = ldA(0, i); }
        lds_wait(sm + F_M + (lane & 3) * 4, 10);
        PRIO(0);
        TS(27);
        auto cstep = [&](u32x4 (&A)[8], u32x4 (&B)[8], u32x4 (&An)[8], u32x4 (&Bn)[8], int dn) {
            asm volatile("" :: "v"(A[0]), "v"(A[1]), "v"(A[2]), "v"(A[3]), "v"(A[4]), "v"(A[5]), "v"(A[6]), "v"(A[7]));
            asm volatile("" :: "v"(B[0]), "v"(B[1]), "v"(B[2]), "v"(B[3]), "v"(B[4]), "v"(B[5]), "v"(B[6]), "v"(B[7]));
            SCHED();
            int n = 0;
#pragma unroll
            for (int gm = 0; gm < 8; gm++)
#pragma unroll
                for (int c = gm; c < 8; c++) {
                    y[c] = MFMA16H(A[gm], B[c - gm], y[c]);
                    if (dn < 4 && n < 24) { if (n % 3 == 0) Bn[n / 3] = ldB(dn, n / 3); else if (n % 3 == 1) An[n / 3] = ldA(dn, n / 3); }
                    n++;
                    SCHED();
                }
        };
        cstep(A0, B0, A1, B1, 1); cstep(A1, B1, A0, B0, 2); cstep(A0, B0, A1, B1, 3); cstep(A1, B1, A0, B0, 4);
        PRIO(3);
        TS(28);
        const float ksc = __builtin_amdgcn_exp2f(fmaxf(*(LAS float*)(sm + L_MISC + cl * 4), *(LAS float*)(sm + L_MISC + 8 + cl * 4)));
        LAS char* fh = sm + F_H + hw * 4;
        LAS char* fp = sm + F_H + (hw ^ 2) * 4;
        lds_post(fh, 1); lds_wait(fp, 1);
        TS(37);
        LAS char* xb = sm + L_UC + cl * UC_CH + lane * 16;
        if (rh) {
#pragma unroll
            for (int c = 0; c < 4; c++) *(LAS f32x4*)(xb + 4096 + c * 1024) = y[c];
            lds_post(fh, 2);
#pragma unroll
            for (int c = 0; c < 4; c++)
#pragma unroll
                for (int e = 0; e < 4; e++) y[c][e] = opq(y[4 + c][e] * ksc);
        } else {
#pragma unroll
            for (int c = 0; c < 4; c++) *(LAS f32x4*)(xb + c * 1024) = y[4 + c];
            lds_post(fh, 2);
#pragma unroll
            for (int c = 0; c < 4; c++)
#pragma unroll
                for (int e = 0; e < 4; e++) y[c][e] = opq(y[c][e] * ksc);
        }
        char* sdst = p.ws + WS_S + (size_t)chn * 4096 + rh * 2048;
        TS(38);
        lds_wait(fp, 2);
        TS(39);
        {
            u32x2 o[4];
#pragma unroll
            for (int c = 0; c < 4; c++) {
                const f32x4 v = *(LAS f32x4*)(xb + (rh ^ 1) * 4096 + c * 1024);
                float f[4];
#pragma unroll
                for (int e = 0; e < 4; e++) f[e] = opq(fmaf(v[e], ksc, y[c][e]));
                o[c] = (u32x2){pkbf(f[0], f[1]), pkbf(f[2], f[3])};
            }
#pragma unroll
            for (int cp = 0; cp < 2; cp++) {
                const auto lo = __builtin_amdgcn_permlane16_swap(o[2 * cp].x, o[2 * cp + 1].x, false, false);
                const auto hi = __builtin_amdgcn_permlane16_swap(o[2 * cp].y, o[2 * cp + 1].y, false, false);
                astore_b128_sc1(sdst, (u32)(cp * 1024 + lane * 16), (u32x4){lo[0], hi[0], lo[1], hi[1]});
            }
        }
        TS(29);
        WAITV(0);
        lds_post(fh, 3);
        lds_wait(sm + F_H + (lane & 3) * 4, 3);
        if (wid == 4) astore_b64_sc1(flags, (u32)(FL_S + b) * 128u, tag1);
        PRIO(0);
        TS(30);
        xe = *(const u32x4*)((const char*)p.x + xo); b2e = *(const u32x4*)((const char*)p.b2 + dt * 128 + (tid & 7) * 16);
        if (wid == 4) { lds_wait(sm + F_M + (lane & 3) * 4, 11); poll_flags(flags, FL_S + dt, 8, 32, tag1, lane); lds_post(sm + F_P, 2); }
        else lds_wait(sm + F_P, 2);
        TS(31);
        u32x4 sv[2];
#pragma unroll
        for (int i = 0; i < 2; i++) {
            const int d = hw * 16 + i * 8 + (lane >> 3);
            sv[i] = aload_b128_sc1(p.ws + WS_S, (u32)((dt * 64 + d) * 4096 + (tt >> 3) * 1024 + ((tt >> 2) & 1) * 256 + ((lane >> 2) & 1) * 512 + (tt & 3) * 64 + (lane & 3) * 16));
        }
        asm volatile("s_waitcnt vmcnt(0)" : "+v"(sv[0]), "+v"(sv[1]) :: "memory");
#pragma unroll
        for (int i = 0; i < 2; i++) {
            const int d = hw * 16 + i * 8 + (lane >> 3);
            *(LAS u32x4*)(sm + L_UC + d * 128 + (((2 * (lane & 3) + ((lane >> 2) & 1)) ^ (d >> 3)) << 4)) = sv[i];
        }
        TS(32);
    }
    BAR();
    TS(33);
    LANEVALS();
    {
        const int tok = tid >> 3, dg = tid & 7;
        float m[8];
#pragma unroll
        for (int e = 0; e < 4; e++) { m[2 * e] = bflo(b2e[e]); m[2 * e + 1] = bfhi(b2e[e]); }
#pragma unroll
        for (int i = 0; i < 4; i++) {
            const f32x4 lo = *(LAS f32x4*)(sm + L_W2 + i * W2_REG + tok * 272 + dg * 32), hi = *(LAS f32x4*)(sm + L_W2 + i * W2_REG + tok * 272 + dg * 32 + 16);
#pragma unroll
            for (int e = 0; e < 4; e++) { m[e] += lo[e]; m[4 + e] += hi[e]; }
        }
        float o[8];
#pragma unroll
        for (int e = 0; e < 8; e++) {
            const float s = bf2f(*(LAS u16*)(sm + L_UC + (8 * dg + e) * 128 + (((tok >> 3) ^ dg) << 4) + (tok & 7) * 2));
            const float gt = m[e] * __builtin_amdgcn_rcpf(1.f + __builtin_amdgcn_exp2f(-1.4426950408889634f * m[e]));
            const float xv = (e & 1) ? bfhi(xe[e >> 1]) : bflo(xe[e >> 1]);
            o[e] = fmaf(s, gt, xv);
        }
        u32x4 ov = {pkbf(o[0], o[1]), pkbf(o[2], o[3]), pkbf(o[4], o[5]), pkbf(o[6], o[7])};
        *(u32x4*)((char*)p.out + xo) = ov;
    }
    TS(34); TR(63);
    TSOUT();
}

extern "C" void kernel_launch(void* const* d_in, const int* in_sizes, int n_in, void* d_out, int out_size, void* d_ws, size_t ws_size,
                              hipStream_t stream) {
    Params p;
    p.x = (const u16*)d_in[0]; p.Are = (const u16*)d_in[1]; p.Aim = (const u16*)d_in[2]; p.Bre = (const u16*)d_in[3]; p.Bim = (const u16*)d_in[4];
    p.Cre = (const u16*)d_in[5]; p.Cim = (const u16*)d_in[6]; p.logdt = (const u16*)d_in[7]; p.Dskip = (const u16*)d_in[8];
    p.w1 = (const u16*)d_in[10]; p.b1 = (const u16*)d_in[11]; p.w2 = (const u16*)d_in[12]; p.b2 = (const u16*)d_in[13];
    p.out = (u16*)d_out; p.ws = (char*)d_ws;
    fused_kernel<<<256, 512, 0, stream>>>(p);
}
```

```cpp
#define V_KROT 0
#define V_MALL 5
#include <hip/hip_runtime.h>
#include <stdint.h>
template <int N> struct IC { static constexpr int value = N; };
#define V_W2ROT 0
#define V_TOUCH 0
#define V_CODE 1
#define V_MPRIO 0
#define V_KPRIO 1
#define V_PSLEEP 6
#define V_SSTEP 14
#define V_UAT 6
#define V_FSP 8
#define V_SPD 16
#define V_MDELAY 16
#define V_LDELAY 2
#define V_LNK 1
#define V_LPRIO 0
#define V_PACE 16
#define V_NOP 0
#define V_MTHR 10
#define V_WIN 8
#define V_SDELAY 16
#define V_UB 5
#define V_CPRIO 2
#define V_EARLY 1
#define V_HROT 0
#define V_UPRIO 1
#define V_LEAD 0
#define V_S0 0
#define V_S1 0
#define CODE_LINES 224

typedef unsigned short u16;
typedef unsigned u32;
typedef unsigned long long u64;
typedef u32 u32x2 __attribute__((ext_vector_type(2)));
typedef u32 u32x4 __attribute__((ext_vector_type(4)));
typedef short s16x4 __attribute__((ext_vector_type(4)));
typedef __bf16 bf16x2 __attribute__((ext_vector_type(2)));
typedef __bf16 bf16x8 __attribute__((ext_vector_type(8)));
typedef _Float16 f16x2 __attribute__((ext_vector_type(2)));
typedef _Float16 f16x4 __attribute__((ext_vector_type(4)));
typedef _Float16 f16x8 __attribute__((ext_vector_type(8)));
typedef float f32x2 __attribute__((ext_vector_type(2)));
typedef float f32x4 __attribute__((ext_vector_type(4)));
typedef float f32x16 __attribute__((ext_vector_type(16)));
#define LAS __attribute__((address_space(3)))
#define DEV __device__ __forceinline__

DEV int tab_off(int w);
constexpr size_t WS_H = 12u << 20;
constexpr size_t WS_U = 8u << 20;
constexpr size_t WS_S = 10u << 20;
constexpr size_t WS_FLAG = 0;
constexpr size_t WS_STAMP = 20u << 20;
constexpr int FL_RDY = 0, FL_LN = 1344, FL_S = 32, FL_E = 288, FL_H = 320;
constexpr u32 GR_ST = 256u << 10, GR_CS = 320u << 10;

constexpr int L_W2 = 0, W2_REG = 28672;
constexpr int L_UC = 114688, UC_COPY = 4160, UC_CH = 4 * UC_COPY + 64;
constexpr int L_TAB = 131072, TAB_ROW = 288, TAB_SZ = 6144;
constexpr int L_MISC = 149504;
constexpr int L_KP = 150784, KP_PLANE = 2304, KP_CH = 2 * KP_PLANE;
constexpr int F_M = L_MISC + 64, F_L = L_MISC + 80, F_K = L_MISC + 88, F_H = L_MISC + 96, C_CS = L_MISC + 112, F_D = L_MISC + 128, F_P = L_MISC + 160;
constexpr int L_TAG = L_MISC + 112, L_JUNK = L_MISC + 256;
DEV int tab_off(int w) { return w == 3 ? L_KP + KP_CH : L_TAB + w * TAB_SZ; }

struct Params {
    const u16 *x, *Are, *Aim, *Bre, *Bim, *Cre, *Cim, *logdt, *Dskip, *w1, *b1, *w2, *b2;
    u16* out;
    char* ws;
};

DEV float bf2f(u32 v) { return __uint_as_float(v << 16); }
DEV float bflo(u32 v) { return __uint_as_float(v << 16); }
DEV float bfhi(u32 v) { return __uint_as_float(v & 0xffff0000u); }
DEV u32 pkbf(float a, float b) { f32x2 f = {a, b}; return __builtin_bit_cast(u32, __builtin_convertvector(f, bf16x2)); }
DEV u32 pkh(float a, float b) { f32x2 f = {a, b}; return __builtin_bit_cast(u32, __builtin_convertvector(f, f16x2)); }
DEV float opq(float a) { asm("" : "+v"(a)); return a; }
DEV float rbf(float a) { return bflo(pkbf(a, a)); }
DEV float dot2(u32 a, u32 b, float c) { return __builtin_amdgcn_fdot2_f32_bf16(__builtin_bit_cast(bf16x2, a), __builtin_bit_cast(bf16x2, b), c, false); }

#define WAITV(n) asm volatile("s_waitcnt vmcnt(" #n ")" ::: "memory")
#define WAITL0() asm volatile("s_waitcnt lgkmcnt(0)" ::: "memory")
#define BAR() do { asm volatile("s_waitcnt lgkmcnt(0)" ::: "memory"); __builtin_amdgcn_s_barrier(); asm volatile("" ::: "memory"); } while (0)
#define RBAR() do { asm volatile("" ::: "memory"); __builtin_amdgcn_s_barrier(); asm volatile("" ::: "memory"); } while (0)
#define SCHED() __builtin_amdgcn_sched_barrier(0)
#define PRIO(n) __builtin_amdgcn_s_setprio(n)
#define MFMA32(a, b, c) __builtin_amdgcn_mfma_f32_32x32x16_bf16(__builtin_bit_cast(bf16x8, a), __builtin_bit_cast(bf16x8, b), c, 0, 0, 0)
#define MFMA16H(a, b, c) __builtin_amdgcn_mfma_f32_16x16x32_f16(__builtin_bit_cast(f16x8, a), __builtin_bit_cast(f16x8, b), c, 0, 0, 0)

DEV const void* uni(const void* q) {
    const u64 a = (u64)q;
    return (const void*)(((u64)(u32)__builtin_amdgcn_readfirstlane((u32)(a >> 32)) << 32) | (u32)__builtin_amdgcn_readfirstlane((u32)a));
}
#define GLP ".if %c[q]\n\ts_nop %c[q]\n\t.endif\n\t"
#define GL4(B) ".if %c[p]\n\ts_waitcnt vmcnt(%c[p])\n\t.endif\n\tglobal_load_lds_dwordx4 %[v0], " B "\n\t" GLP "global_load_lds_dwordx4 %[v1], " B " offset:1024\n\t" GLP \
               "global_load_lds_dwordx4 %[v2], " B " offset:2048\n\t" GLP "global_load_lds_dwordx4 %[v3], " B " offset:3072\n\t"
template <int NG, bool HALF, int PACE> DEV void glds_run(const void* base, u32 step, u32 v0, u32 v1, u32 v2, u32 v3, u32 dst) {
    u32 keep;
    dst = __builtin_amdgcn_readfirstlane(dst);
    base = uni(base);
    asm volatile("s_mov_b32 %[k], m0\n\ts_mov_b32 m0, %[d]\n\ts_mov_b64 vcc, %[b]\n\ts_nop 1\n\t" GL4("vcc")
                 ".rept %c[n]\n\ts_add_u32 m0, m0, 0x1000\n\ts_add_u32 vcc_lo, vcc_lo, %[s]\n\ts_addc_u32 vcc_hi, vcc_hi, 0\n\t" GL4("vcc") ".endr\n\t"
                 ".if %c[h]\n\ts_add_u32 m0, m0, 0x1000\n\ts_add_u32 vcc_lo, vcc_lo, %[s]\n\ts_addc_u32 vcc_hi, vcc_hi, 0\n\t"
                 "global_load_lds_dwordx4 %[v0], vcc\n\t" GLP "global_load_lds_dwordx4 %[v1], vcc offset:1024\n\t.endif\n\t"
                 "s_mov_b32 m0, %[k]"
                 : [k] "=&s"(keep) : [v0] "v"(v0), [v1] "v"(v1), [v2] "v"(v2), [v3] "v"(v3), [b] "s"(base), [d] "s"(dst), [n] "i"(NG - 1), [s] "s"(step), [h] "i"(HALF ? 1 : 0), [p] "i"(PACE), [q] "i"(V_NOP)
                 : "memory", "scc", "vcc");
}
DEV void touch(const void* base, u32 voff, u32 junk) {
    u32 keep;
    base = uni(base);
    asm volatile("s_mov_b32 %0, m0\n\ts_mov_b32 m0, %3\n\ts_nop 2\n\tglobal_load_lds_dword %1, %2\n\ts_mov_b32 m0, %0"
                 : "=&s"(keep) : "v"(voff), "s"(base), "s"(junk) : "memory");
}
DEV u32 aload_u16(const void* base, u32 voff) { u32 r; asm volatile("global_load_ushort %0, %1, %2" : "=v"(r) : "v"(voff), "s"(base) : "memory"); return r; }
DEV u32 aload_b32(const void* base, u32 voff) { u32 r; asm volatile("global_load_dword %0, %1, %2" : "=v"(r) : "v"(voff), "s"(base) : "memory"); return r; }
DEV u32x2 aload_b64_sc1(const void* base, u32 voff) { u32x2 r; asm volatile("global_load_dwordx2 %0, %1, %2 sc1" : "=v"(r) : "v"(voff), "s"(base) : "memory"); return r; }
DEV u32x4 aload_b128_sc1(const void* base, u32 voff) { u32x4 r; asm volatile("global_load_dwordx4 %0, %1, %2 sc1" : "=v"(r) : "v"(voff), "s"(base) : "memory"); return r; }
DEV u32x4 aload_b128(const void* base, u32 voff) { u32x4 r; asm volatile("global_load_dwordx4 %0, %1, %2" : "=v"(r) : "v"(voff), "s"(base) : "memory"); return r; }
DEV void astore_b128_sc1(void* base, u32 voff, u32x4 v) { asm volatile("global_store_dwordx4 %0, %1, %2 sc1\n\ts_nop 1" :: "v"(voff), "v"(v), "s"(base) : "memory"); }
DEV void astore_b64_sc1(void* base, u32 voff, u32x2 v) { asm volatile("global_store_dwordx2 %0, %1, %2 sc1\n\ts_nop 1" :: "v"(voff), "v"(v), "s"(base) : "memory"); }
DEV void flag_store(void* base, u32 off, u32x2 v) {
    u64 keep;
    asm volatile("s_mov_b64 %0, exec\n\ts_mov_b64 exec, 1\n\tglobal_store_dwordx2 %1, %2, %3 sc1\n\ts_mov_b64 exec, %0\n\ts_nop 0"
                 : "=&s"(keep) : "v"(off), "v"(v), "s"(base) : "memory");
}
DEV u32x4 trfrag(LAS char* a, LAS char* b) {
    const u32x2 lo = __builtin_bit_cast(u32x2, __builtin_amdgcn_ds_read_tr16_b64_v4i16((LAS s16x4*)a));
    const u32x2 hi = __builtin_bit_cast(u32x2, __builtin_amdgcn_ds_read_tr16_b64_v4i16((LAS s16x4*)b));
    return (u32x4){lo.x, lo.y, hi.x, hi.y};
}

#define TS(i)
#define TF(i)
#define TL(i)
#define TR(i)
#define TSOUT()

DEV void poll_flags(const char* flags, int first, int stride, int nfl, u32x2 tag, int lane) {
    const u32 off = (u32)(first + (lane < nfl ? lane : 0) * stride) * 128u;
#pragma unroll 1
    for (int spin = 0; spin < (1 << 16); ++spin) {
        u32x2 v = aload_b64_sc1(flags, off);
        asm volatile("s_waitcnt vmcnt(0)" : "+v"(v) :: "memory");
        if (__builtin_amdgcn_ballot_w64(v.x != tag.x || v.y != tag.y) == 0) break;
        __builtin_amdgcn_s_sleep(V_PSLEEP);
    }
}
DEV bool all_ge(u32 v, u32 target) { return __builtin_amdgcn_ballot_w64(v < target) == 0; }
DEV void lds_wait(LAS char* w, u32 target) {
#pragma unroll 1
    for (int spin = 0; spin < (1 << 21); ++spin) {
        if (all_ge(*(volatile LAS u32*)w, target)) break;
        __builtin_amdgcn_s_sleep(1);
    }
    asm volatile("" ::: "memory");
}
DEV void lds_post(LAS char* w, u32 v) { WAITL0(); *(volatile LAS u32*)w = v; }
template <int CTRL, int RM> DEV float dpp_add(float v) {
    return v + __builtin_bit_cast(float, __builtin_amdgcn_update_dpp(0, __builtin_bit_cast(int, v), CTRL, RM, 0xf, false));
}
template <int CTRL, int RM> DEV u32 dpp_umax(u32 v) { const u32 o = __builtin_amdgcn_update_dpp(0u, v, CTRL, RM, 0xf, false); return v > o ? v : o; }
DEV u32 wave_umax(u32 v) {
    v = dpp_umax<0xB1, 0xf>(v); v = dpp_umax<0x4E, 0xf>(v); v = dpp_umax<0x141, 0xf>(v); v = dpp_umax<0x140, 0xf>(v);
    v = dpp_umax<0x142, 0xa>(v); v = dpp_umax<0x143, 0xc>(v);
    return __builtin_amdgcn_readlane(v, 63);
}
DEV f32x2 cexp_f(float e2, float rev) {
    const float m = __builtin_amdgcn_exp2f(e2);
    return (f32x2){m * __builtin_amdgcn_cosf(rev), m * __builtin_amdgcn_sinf(rev)};
}
DEV f32x2 cmul_f(f32x2 a, f32x2 z) {
    f32x2 t, r;
    asm("v_pk_mul_f32 %0, %1, %2 op_sel_hi:[1,0]" : "=v"(t) : "v"(a), "v"(z));
    asm("v_pk_fma_f32 %0, %1, %2, %3 op_sel:[1,1,0] op_sel_hi:[0,1,1] neg_lo:[0,1,0]" : "=v"(r) : "v"(a), "v"(z), "v"(t));
    return r;
}
template <bool CONJ> DEV u32 cmh1(u32 a, u32 z) {
    u32 t;
    if (CONJ) asm("v_pk_mul_f16 %0, %1, %2 op_sel_hi:[1,0] neg_hi:[1,0]" : "=v"(t) : "v"(a), "v"(z));
    else asm("v_pk_mul_f16 %0, %1, %2 op_sel_hi:[1,0]" : "=v"(t) : "v"(a), "v"(z));
    return t;
}
template <bool CONJ> DEV u32 cmh2(u32 a, u32 z, u32 t) {
    u32 r;
    if (CONJ) asm("v_pk_fma_f16 %0, %1, %2, %3 op_sel:[1,1,0] op_sel_hi:[0,1,1] neg_lo:[0,1,0] neg_hi:[0,1,0]" : "=v"(r) : "v"(a), "v"(z), "v"(t));
    else asm("v_pk_fma_f16 %0, %1, %2, %3 op_sel:[1,1,0] op_sel_hi:[0,1,1] neg_lo:[0,1,0]" : "=v"(r) : "v"(a), "v"(z), "v"(t));
    return r;
}
DEV void build_table(LAS char* sm, int w, int lane, u32 are, u32 aim, u32 bre, u32 bim, u32 cre, u32 cim, u32 ldt, u32 dsk) {
    LAS char* t = sm + tab_off(w) + lane * 4;
    *(LAS u32*)(sm + L_MISC + 16 + w * 4) = dsk << 16;
    auto put = [&](int row, f32x2 v) { *(LAS u32*)(t + row * TAB_ROW) = pkh(v.x, v.y); };
    const float dtv = rbf(__builtin_amdgcn_exp2f(bf2f(ldt) * 1.4426950408889634f));
    const float a2 = dtv * bf2f(are) * 1.4426950408889634f;
    float rev = dtv * bf2f(aim) * 0.15915494309189535f;
    rev -= __builtin_rintf(rev);
    f32x2 wv = cmul_f((f32x2){bf2f(cre), bf2f(cim)}, (f32x2){bf2f(bre), bf2f(bim)});
    u32 E = wave_umax(__float_as_uint(fmaxf(dtv * fmaxf(fabsf(wv.x), fabsf(wv.y)) * __builtin_amdgcn_exp2f(fmaxf(a2, 0.f) * 2048.f), fabsf(bf2f(dsk)) * 0.0078125f))) >> 23;
    E = E < 16u ? 16u : (E > 236u ? 236u : E);
    wv *= dtv * __uint_as_float((260u - E) << 23);
    *(LAS float*)(sm + L_MISC + w * 4) = (float)(int)E - 133.f;
    const f32x2 z1 = cexp_f(a2, rev), z64 = cexp_f(a2 * 64.f, rev * 64.f);
    const f32x2 z2 = cmul_f(z1, z1), z3 = cmul_f(z2, z1), z4 = cmul_f(z2, z2), z8 = cmul_f(z4, z4), z16 = cmul_f(z8, z8), z32 = cmul_f(z16, z16);
    const f32x2 z128 = cmul_f(z64, z64), z256 = cmul_f(z128, z128), z512 = cmul_f(z256, z256);
    const f32x2 w1 = cmul_f(wv, z512), w2 = cmul_f(w1, z512);
    *(LAS u32*)t = 0x00003c00u;
    put(1, z1); put(2, z2); put(3, z3); put(4, z4); put(5, cmul_f(z4, z1)); put(6, cmul_f(z3, z3)); put(7, cmul_f(z4, z3)); put(8, z8);
    put(9, z16); put(10, cmul_f(z16, z8)); put(11, z32); put(12, z64); put(13, cmul_f(z64, z32));
    put(14, z128); put(15, z256); put(16, cmul_f(z256, z128));
    put(17, wv); put(18, w1); put(19, w2); put(20, cmul_f(w2, z512));
}
DEV u32 perm(u32 hi, u32 lo, u32 sel) { return __builtin_amdgcn_perm(hi, lo, sel); }

__global__ __launch_bounds__(512, 2) void fused_kernel(Params p) {
    __shared__ __attribute__((aligned(1024))) char shm[163840];
    LAS char* sm = (LAS char*)shm;
    const u32 lds0 = (u32)(uintptr_t)sm;
    const int wid = __builtin_amdgcn_readfirstlane(threadIdx.x >> 6);
    const int b = blockIdx.x, g = b & 7, r = b >> 3, hw = wid & 3;
    char* flags = p.ws + WS_FLAG;
    const int ch0 = 64 * g + 16 * (r & 3) + (r >> 2);
    const int tt = g * 4 + (r >> 3), dt = r & 7;
    const int qo = V_W2ROT ? 2 * (tt & 1) : 0;
    const int k0 = V_KROT == 2 ? (r % 7 + 2 * (r / 7)) % 7 : (V_KROT ? r % 7 + 2 * (r / 7) : 0);
    const int rot = V_HROT == 0 ? 0 : (V_HROT == 2 ? (dt & 1) * 2 : (dt + (V_W2ROT ? tt & 2 : 0)) & 3);
    const bool isG = r < 28;
    const int mt = (g >> 1) * 4 + r / 7, ft = (g & 1) * 7 + r % 7;
#define LANEVALS() \
    int tid = threadIdx.x; asm volatile("" : "+v"(tid)); \
    const int lane = tid & 63, h = lane >> 5, l31 = lane & 31; \
    const u32 vsw = (u32)(((lane & 7) ^ (((lane >> 4) & 1) << 2)) << 4);          \
    const u32 voffw2 = (u32)((lane >> 3) * 1024 + dt * 128) + vsw; \
    auto w2_dma = [&](auto pace, int kq, int sg) {        \
        glds_run<3, true, decltype(pace)::value>((const char*)p.w2 + (size_t)(4 * ((kq + qo) & 3) + ((sg + rot) & 3)) * (112 * 1024), 32768u, voffw2, voffw2 + 7168u, voffw2 + 14336u, voffw2 + 21504u, \
                                 lds0 + L_W2 + kq * W2_REG + (sg & 1) * 14336); \
    }; \
    const int blk = (lane >> 4) & 1, q4 = (lane & 15) >> 2, pp = lane & 3; \
      \
    const u32 tra0 = (u32)((4 * h + q4) * 128 + (((2 * blk + (pp >> 1)) ^ (((q4 >> 1) & 1) << 2)) << 4) + 8 * (pp & 1)); \
    const u32 xo = (u32)((tt * 64 + (tid >> 3)) * 1024 + dt * 128 + (tid & 7) * 16); \
    (void)l31; (void)voffw2; (void)w2_dma; (void)tra0; (void)xo;
    if (V_CODE && wid == 6) { const u32 ln = (u32)(r + 32 * (threadIdx.x & 7)); touch((const char*)(__builtin_amdgcn_s_getpc() & ~127ull), (ln < CODE_LINES ? ln : 0u) * 128u, lds0 + L_JUNK); }
    if (wid == 0) *(LAS u32*)(sm + L_MISC + 64 + (threadIdx.x & 31) * 4) = 0;
    u32x2 tag1;
    u32x4 xe, b2e;
#define WAIT_PARAMS(n) asm volatile("s_waitcnt vmcnt(" #n ")" : "+v"(are), "+v"(aim), "+v"(bre), "+v"(bim), "+v"(cre), "+v"(cim), "+v"(ldt), "+v"(dsk) :: "memory")
#define LOAD_PARAMS(po, c2) \
    asm volatile("global_load_ushort %0, %8, %10\n\tglobal_load_ushort %1, %8, %11\n\tglobal_load_ushort %2, %8, %12\n\tglobal_load_ushort %3, %8, %13\n\t" \
                 "global_load_ushort %4, %8, %14\n\tglobal_load_ushort %5, %8, %15\n\tglobal_load_ushort %6, %9, %16\n\tglobal_load_ushort %7, %9, %17" \
                 : "=&v"(are), "=&v"(aim), "=&v"(bre), "=&v"(bim), "=&v"(cre), "=&v"(cim), "=&v"(ldt), "=&v"(dsk) \
                 : "v"(po), "v"(c2), "s"(p.Are), "s"(p.Aim), "s"(p.Bre), "s"(p.Bim), "s"(p.Cre), "s"(p.Cim), "s"(p.logdt), "s"(p.Dskip) : "memory")
#define LOADER_VALS() \
    const bool lx = wid == 4;                  \
    const u32 lR = lx ? (u32)(lane >> 3) * 1024u : (u32)(lane >> 4) * 3584u; \
    const u32 lS = lx ? (u32)(((lane & 7) ^ (lane >> 4)) << 4) : (u32)(((lane & 15) ^ ((lane >> 4) << 2)) << 4); \
    const u32 lst = lx ? 7168u : 13312u, x1 = lx ? 64u : 16u, x2 = lx ? 0u : 32u; \
    const u32 vo0 = lR + lS, vo1 = lR + (lS ^ x1) + lst, vo2 = lR + (lS ^ x2) + 2 * lst, vo3 = lR + (lS ^ (x1 | x2)) + 3 * lst; \
    const char* lsrc = lx ? (const char*)p.x + (size_t)mt * 128 * 1024 : (const char*)p.w1 + ft * 256; \
    const u32 lgs = lx ? 32768u : 16u * 3584u, lcs = lx ? 128u : 64u * 3584u; \
    auto chunk_src = [&](int kc) { return lsrc + (V_KROT == 2 ? (kc == 0 ? 0 : (kc + k0 > 7 ? kc + k0 - 7 : kc + k0)) : (kc + k0) & 7) * lcs; }; \
    auto stage = [&](int kc) { glds_run<4, false, V_PACE>(chunk_src(kc), lgs, vo0, vo1, vo2, vo3, lds0 + (kc & 3) * 32768 + hw * 16384); };
#define READ_TAG() do { u32x4 t_ = *(volatile LAS u32x4*)(sm + L_TAG); \
        if (!all_ge(t_.y, 1)) { lds_wait(sm + L_TAG + 4, 1); t_ = *(volatile LAS u32x4*)(sm + L_TAG); } \
        tag1.x = __builtin_amdgcn_readfirstlane(t_.z); tag1.y = __builtin_amdgcn_readfirstlane(t_.w); } while (0)
    const int lj = g * 4 + (r - 28);

    const int tb8 = g * 32 + r;

    if (wid < 4) {
        LANEVALS();
        const int kq = wid;
        u32x4 wreg[14][2];
        LAS char* w2a = sm + L_W2 + kq * W2_REG;
        auto w2frag = [&](int s, int j2) { LAS char* a = w2a + (j2 ? tra0 ^ 64 : tra0) + s * 2048; return trfrag(a, a + 1024); };
        auto w2load = [&]() {
#pragma unroll
            for (int s = 0; s < 14; s++) { wreg[s][0] = w2frag(s, 0); wreg[s][1] = w2frag(s, 1); }
        };
#define W2PIN() _Pragma("unroll") for (int s = 0; s < 14; s++) asm volatile("" : "+v"(wreg[s][0]), "+v"(wreg[s][1]))
        LAS char* fw = sm + F_L + (wid & 2) * 4 + (lane & 1) * 4;
        bool early = false;
        const int th = wid & 1, fh = wid >> 1;
        u32 are, aim, bre, bim, cre, cim, ldt, dsk, b1a, b1b;
        {
            const int chp = ch0 + 8 * th;
            const u32 po = (u32)(chp * 128 + fh * 64 + lane) * 2u, c2 = (u32)chp * 2u, bo = (u32)(ft * 128 + fh * 64 + l31) * 2u;
            if (wid < 3) LOAD_PARAMS(po, c2);
            asm volatile("global_load_ushort %0, %2, %3\n\tglobal_load_ushort %1, %2, %3 offset:64" : "=&v"(b1a), "=&v"(b1b) : "v"(bo), "s"(p.b1) : "memory");
            TS(40);
            BAR();
            SCHED();
        }
        PRIO(V_MPRIO);
        u32 xa[4], wa[2][2];
#pragma unroll
        for (int s = 0; s < 4; s++) xa[s] = (u32)((64 * th + l31) * 128 + (((2 * s + h) ^ ((l31 >> 1) & 7)) << 4));
#pragma unroll
        for (int j2 = 0; j2 < 2; j2++)
#pragma unroll
            for (int t = 0; t < 2; t++)
                wa[j2][t] = (u32)(16384 + 256 * (8 * h + 4 * t + q4) + (((4 * (2 * fh + j2) + 2 * blk + (pp >> 1)) ^ ((q4 << 2) | (2 * h + t))) << 4) + 8 * (pp & 1));
        u32 so = GR_ST + (u32)(mt * 128 + th * 64 + l31) * 16u, co = GR_CS + (u32)(ft * 128 + fh * 64 + l31) * 8u;
        asm volatile("" : "+v"(so), "+v"(co), "+v"(xa[0]), "+v"(xa[1]), "+v"(xa[2]), "+v"(xa[3]), "+v"(wa[0][0]), "+v"(wa[0][1]), "+v"(wa[1][0]), "+v"(wa[1][1]));
        WAIT_PARAMS(2);
        TS(1);
        if (wid < 3) build_table(sm, wid, lane, are, aim, bre, bim, cre, cim, ldt, dsk);
        LAS char* fm = sm + F_M + wid * 4;
        LAS char* fl = sm + F_L + (lane & 1) * 4;
        lds_post(fm, 1);
        TS(2);
        if (isG) {
            f32x16 acc[2][2] = {};
            lds_wait(fl, 1);
            TS(3);
            u32x4 X[4][2], W[4][2];
            u32 wp[2][2], xp;
#define RX(s, j) X[s][j] = *(LAS u32x4*)(sm + xp + (j) * 4096)
#define RW(s, j) W[s][j] = trfrag(sm + wp[j][0] + (s) * 4096, sm + wp[j][1] + (s) * 4096)
#define OPQ(v) asm volatile("" : "+v"(v))
#define MM(s, j2, j) acc[j2][j] = MFMA32(W[s][j2], X[s][j], acc[j2][j])
            u32x4 st0, st1; u32x2 cs0, cs1;
#define LOAD_GRAN() asm volatile("global_load_dwordx4 %0, %4, %6 sc1\n\tglobal_load_dwordx4 %1, %4, %6 offset:512 sc1\n\tglobal_load_dwordx2 %2, %5, %6 sc1\n\tglobal_load_dwordx2 %3, %5, %6 offset:256 sc1" \
                                 : "=&v"(st0), "=&v"(st1), "=&v"(cs0), "=&v"(cs1) : "v"(so), "v"(co), "s"(flags) : "memory")
            wp[0][0] = wa[0][0]; wp[0][1] = wa[0][1]; wp[1][0] = wa[1][0]; wp[1][1] = wa[1][1];
            xp = xa[0]; RX(0, 0); SCHED(); RW(0, 0); SCHED(); RW(0, 1); SCHED(); RX(0, 1); SCHED();
            xp = xa[1]; RX(1, 0); SCHED(); RW(1, 0); SCHED(); RW(1, 1); SCHED(); RX(1, 1); SCHED();
            xp = xa[2];
#pragma unroll
            for (int kc = 0; kc < 7; kc++) {
                const u32 sl = (u32)(kc & 3) * 32768u, sn = (u32)((kc + 1) & 3) * 32768u;
                if (kc == 5) LOAD_GRAN();
                MM(0, 0, 0); RX(2, 0); SCHED(); MM(0, 1, 0); RW(2, 0); SCHED(); MM(0, 0, 1); RW(2, 1); SCHED();
                MM(0, 1, 1); RX(2, 1); xp = xa[3] + sl; OPQ(xp); SCHED();
                MM(1, 0, 0); RX(3, 0);
                SCHED();
                MM(1, 1, 0); RW(3, 0); SCHED(); MM(1, 0, 1); RW(3, 1); SCHED();
                MM(1, 1, 1); RX(3, 1); xp = xa[0] + sn; wp[0][0] = wa[0][0] + sn; wp[0][1] = wa[0][1] + sn; OPQ(xp); OPQ(wp[0][0]); OPQ(wp[0][1]); SCHED();
                MM(2, 0, 0); SCHED();
                if (kc == 0) lds_wait(fl, 2); else RBAR();
                SCHED();
                RX(0, 0); wp[1][0] = wa[1][0] + sn; wp[1][1] = wa[1][1] + sn; OPQ(wp[1][0]); OPQ(wp[1][1]); SCHED();
                MM(2, 1, 0); RW(0, 0); SCHED(); MM(2, 0, 1); RW(0, 1); SCHED();
                MM(2, 1, 1); RX(0, 1); xp = xa[1] + sn; OPQ(xp); SCHED();
                MM(3, 0, 0); RX(1, 0); SCHED(); MM(3, 1, 0); RW(1, 0); SCHED(); MM(3, 0, 1); RW(1, 1); SCHED();
                MM(3, 1, 1); RX(1, 1); xp = xa[2] + sn; OPQ(xp);
                SCHED();
                TF(4 + kc);
            }
            MM(0, 0, 0); RX(2, 0); RW(2, 0); SCHED(); MM(0, 1, 0); RW(2, 1); RX(2, 1); xp = xa[3] + 3 * 32768; SCHED();
            MM(0, 0, 1); RX(3, 0); RW(3, 0); SCHED(); MM(0, 1, 1); RW(3, 1); RX(3, 1); SCHED();
            MM(1, 0, 0); MM(1, 1, 0); SCHED();
            READ_TAG();
            lds_post(fm, 9);
            asm volatile("s_waitcnt vmcnt(0)" : "+v"(st0), "+v"(st1), "+v"(cs0), "+v"(cs1), "+v"(b1a), "+v"(b1b) :: "memory");
#pragma unroll 1
            for (int spin = 0; spin < (1 << 16); ++spin) {
                if (__builtin_amdgcn_ballot_w64((((st0.y ^ tag1.x) | (st0.w ^ tag1.x) | (st1.y ^ tag1.x)) | ((st1.w ^ tag1.x) | (cs0.y ^ tag1.x) | (cs1.y ^ tag1.x))) != 0u) == 0) break;
                __builtin_amdgcn_s_sleep(2);
                LOAD_GRAN();
                asm volatile("s_waitcnt vmcnt(0)" : "+v"(st0), "+v"(st1), "+v"(cs0), "+v"(cs1) :: "memory");
            }
            TS(19);
            float rinv[2] = {__uint_as_float(st0.z), __uint_as_float(st1.z)};
            float k1[2], k2[2];
            {
                u32x4 Ax[2], Bx[2];
                auto prepB = [&](int j) {
                    const float nm = __uint_as_float(j ? st1.x : st0.x), mh = rbf(nm), ih = rbf(rinv[j]);
                    const u32 mm = pkbf(mh, opq(nm - mh));
                    const u32 hm = (u32)h - 1u;
                    Bx[j] = (u32x4){mm & hm, mm & hm, pkbf(ih, opq(rinv[j] - ih)) & hm, 0u};
                };
                auto prepA = [&](int j) {
                    const float c = __uint_as_float(j ? cs1.x : cs0.x), chh = rbf(c), cl = opq(c - chh);
                    const u32 bb = j ? b1b : b1a;
                    Ax[j] = (u32x4){pkbf(chh, chh), pkbf(cl, cl), bb | (bb << 16), 0u};
                };
                auto prepK = [&](int j) {
                    const float rr = __builtin_amdgcn_rcpf(rinv[j]);
                    k1[j] = opq(-2.302208198144325f * rr); k2[j] = opq(-0.10294323958002349f * rr * rr * rr);
                };
                MM(1, 0, 1); MM(1, 1, 1); prepB(0); SCHED();
                MM(2, 0, 0); prepA(0); SCHED(); MM(2, 1, 0); prepA(1); SCHED(); MM(2, 0, 1); MM(2, 1, 1); prepB(1); SCHED();
                MM(3, 0, 0); prepK(0); SCHED(); acc[0][0] = MFMA32(Ax[0], Bx[0], acc[0][0]); prepK(1); SCHED();
                MM(3, 1, 0); acc[1][0] = MFMA32(Ax[1], Bx[0], acc[1][0]);
                MM(3, 0, 1); acc[0][1] = MFMA32(Ax[0], Bx[1], acc[0][1]);
                MM(3, 1, 1); acc[1][1] = MFMA32(Ax[1], Bx[1], acc[1][1]);
                SCHED();

            }
            PRIO(2);
            TS(20);
            char* hdst = p.ws + WS_H + (size_t)((mt * 4 + th * 2) * 112 + ft * 8 + fh * 4) * 1024;
#pragma unroll
            for (int j = 0; j < 2; j++) {
                const f32x2 K1 = {k1[j], k1[j]}, K2 = {k2[j], k2[j]}, RI = {rinv[j], rinv[j]};
#pragma unroll
                for (int j2 = 0; j2 < 2; j2++) {
#pragma unroll
                    for (int s = 0; s < 2; s++) {
                        u32x4 o;
#pragma unroll
                        for (int i = 0; i < 4; i++) {
                            const f32x2 u = {acc[j2][j][8 * s + 2 * i], acc[j2][j][8 * s + 2 * i + 1]};
                            const f32x2 z = __builtin_elementwise_fma(u * u, K2, K1) * u;
                            const f32x2 e = {__builtin_amdgcn_exp2f(z.x), __builtin_amdgcn_exp2f(z.y)};
                            const f32x2 dn = __builtin_elementwise_fma(e, RI, RI);
                            const f32x2 gl = u * (f32x2){__builtin_amdgcn_rcpf(dn.x), __builtin_amdgcn_rcpf(dn.y)};
                            o[i] = pkbf(gl.x, gl.y);
                        }
                        asm volatile("global_store_dwordx4 %0, %1, %2 offset:%3 sc1\n\ts_nop 1" :: "v"(lane * 16 + j * 112 * 1024), "v"(o), "s"(hdst), "i"((j2 * 2 + s) * 1024) : "memory");
                        SCHED();
                        if (V_EARLY && j == 1 && j2 == 1 && s == 0) { early = all_ge(*(volatile LAS u32*)fw, 12); if (early) w2load(); SCHED(); }
                    }
                }
            }
            if (early) { W2PIN(); }
            lds_post(fm, 10);
            TS(21);
        } else {
            lds_wait(sm + F_P + 12, 1);
            if (V_MALL & 4) {
                const u32 v0 = (u32)lane * 16u;
                glds_run<3, true, V_MTHR>((const char*)p.w2 + (size_t)(lj * 4 + wid) * 14336, 4096u, v0, v0, v0, v0, lds0 + 65536);
            }
            WAITV(0); lds_post(fm, 10); READ_TAG();
        }
        PRIO(1);
        if (!early) {
            if (isG) { WAITV(0); flag_store(flags, (u32)(FL_H + (mt * 2 + (wid & 1)) * 32 + ft * 2 + (wid >> 1)) * 128u, tag1); }
            lds_wait(fw, 12);
            w2load();
            W2PIN();
            if (!isG) lds_wait(fl, 13);
            WAITL0();
        }
        TS(22);
        w2_dma(IC<V_MTHR>(), kq, 2);
        if (early) { WAITV(14); flag_store(flags, (u32)(FL_H + (mt * 2 + (wid & 1)) * 32 + ft * 2 + (wid >> 1)) * 128u, tag1); }
        TS(24);
        w2_dma(IC<V_MTHR>(), kq, 3);
        TS(18);
        poll_flags(flags, FL_H + tt * 32 + 7 * ((kq + qo) & 3), 1, 7, tag1, lane);
        TS(25);
        const __amdgpu_buffer_rsrc_t hr = __builtin_amdgcn_make_buffer_rsrc(p.ws + WS_H + (size_t)(tt * 2 * 112 + ((kq + qo) & 3) * 28) * 1024, 0, 2 * 112 * 1024, 0x00020000);
        constexpr int WIN = V_WIN;
        u32x4 hf[WIN][2];
        f32x16 acc[2][2] = {};
        auto ldh = [&](int s, int j) {
            return __builtin_amdgcn_raw_buffer_load_b128(hr, lane * 16 + j * 112 * 1024 + ((s % 7) & 3) * 1024, ((s / 7 + rot) & 3) * 7168 + ((s % 7) >> 2) * 4096, 16);
        };
#pragma unroll
        for (int s = 0; s < WIN; s++) { hf[s][0] = ldh(s, 0); SCHED(); hf[s][1] = ldh(s, 1); SCHED(); }
        TS(26);
#pragma unroll
        for (int s = 0; s < 28; s++) {
            if (s == 12) { TS(27); }
            const u32x4 w0 = wreg[s % 14][0], w1 = wreg[s % 14][1];
            acc[0][0] = MFMA32(w0, hf[s % WIN][0], acc[0][0]); SCHED();
            acc[1][0] = MFMA32(w1, hf[s % WIN][0], acc[1][0]);
            if (s + WIN < 28) hf[s % WIN][0] = ldh(s + WIN, 0);
            SCHED();
            acc[0][1] = MFMA32(w0, hf[s % WIN][1], acc[0][1]);
            if (s >= 12 && s < 26) wreg[s - 12][0] = w2frag(s - 12, 0);
            SCHED();
            acc[1][1] = MFMA32(w1, hf[s % WIN][1], acc[1][1]);
            if (s >= 12 && s < 26) wreg[s - 12][1] = w2frag(s - 12, 1);
            if (s + WIN < 28) hf[s % WIN][1] = ldh(s + WIN, 1);
            if (s + WIN == 28) { LANEVALS(); xe = *(const u32x4*)((const char*)p.x + xo); b2e = *(const u32x4*)((const char*)p.b2 + dt * 128 + (tid & 7) * 16); }
            SCHED();
        }
        TS(29);
        int t2 = threadIdx.x; asm volatile("" : "+v"(t2));
        LAS char* pw = w2a + (t2 & 31) * 272 + ((t2 >> 5) & 1) * 16;
#pragma unroll
        for (int j2 = 0; j2 < 2; j2++)
#pragma unroll
            for (int j = 0; j < 2; j++)
#pragma unroll
                for (int rg = 0; rg < 4; rg++) {
                    f32x4 v = {acc[j2][j][4 * rg], acc[j2][j][4 * rg + 1], acc[j2][j][4 * rg + 2], acc[j2][j][4 * rg + 3]};
                    *(LAS f32x4*)(pw + 32 * j * 272 + (32 * j2 + 8 * rg) * 4) = v;
                }
        TS(32);
    } else {
        LANEVALS();
        const int a = lane & 15, q = lane >> 4;
        const int cl = hw & 1, rh = hw >> 1, chn = ch0 + 8 * cl;
        if (wid >= 6) {
            const int kc = wid & 1;
            PRIO(V_KPRIO);
            u32 junk = 0;
            if (kc == 0) {
                u32 are, aim, bre, bim, cre, cim, ldt, dsk;
                LOAD_PARAMS((u32)((ch0 + 8) * 128 + 64 + lane) * 2u, (u32)(ch0 + 8) * 2u);
                BAR();
                if (V_MALL & 1) asm volatile("global_load_dword %0, %1, %2" : "+v"(junk) : "v"((u32)(lane < 56 ? lane : 0) * 128u), "s"(uni((const char*)p.w1 + (size_t)b * 7168)) : "memory");
                if (V_MALL & 2) {
                    __builtin_amdgcn_s_sleep(V_MDELAY);
                    asm volatile("global_load_dword %0, %1, %2" : "+v"(junk) : "v"((u32)(lane < 56 ? lane : 0) * 128u), "s"(uni((const char*)p.w2 + (size_t)b * 7168)) : "memory");
                }
                if (V_MALL & 3) WAIT_PARAMS(1); else WAIT_PARAMS(0);
                TS(41);
                build_table(sm, 3, lane, are, aim, bre, bim, cre, cim, ldt, dsk);
                lds_post(sm + F_P + 8, 1);
                READ_TAG();
                TS(42);
            } else {
                PRIO(3);
                u32x2 tge;
                u32x4 xv[8];
                asm volatile("global_load_dwordx4 %0, %8, %10\n\tglobal_load_dwordx4 %1, %8, %10 offset:1024\n\tglobal_load_dwordx4 %2, %8, %10 offset:2048\n\tglobal_load_dwordx4 %3, %8, %10 offset:3072\n\t"
                             "global_load_dwordx4 %4, %9, %10\n\tglobal_load_dwordx4 %5, %9, %10 offset:1024\n\tglobal_load_dwordx4 %6, %9, %10 offset:2048\n\tglobal_load_dwordx4 %7, %9, %10 offset:3072"
                             : "=&v"(xv[0]), "=&v"(xv[1]), "=&v"(xv[2]), "=&v"(xv[3]), "=&v"(xv[4]), "=&v"(xv[5]), "=&v"(xv[6]), "=&v"(xv[7])
                             : "v"(lane * 16), "v"(lane * 16 + 4096), "s"((const char*)p.x + (size_t)tb8 * 8192) : "memory");
                asm volatile("global_load_dwordx2 %0, %1, %2 sc1" : "=&v"(tge) : "v"(FL_E * 128), "s"(flags) : "memory");
                TS(40);
                BAR();
                asm volatile("s_waitcnt vmcnt(0)" : "+v"(tge), "+v"(xv[0]), "+v"(xv[1]), "+v"(xv[2]), "+v"(xv[3]), "+v"(xv[4]), "+v"(xv[5]), "+v"(xv[6]), "+v"(xv[7]) :: "memory");
                tag1.x = __builtin_amdgcn_readfirstlane(tge.x) + 1u; tag1.y = __builtin_amdgcn_readfirstlane(tge.y);
                TS(41);
                *(volatile LAS u32x2*)(sm + L_TAG + 8) = tag1;
                lds_post(sm + L_TAG + 4, 1);
                float s1[8], s2[8];
#pragma unroll
                for (int i = 0; i < 8; i++) { s1[i] = 0.f; s2[i] = 0.f; }
#pragma unroll
                for (int j = 0; j < 4; j++)
#pragma unroll
                    for (int i = 0; i < 8; i++) { s1[i] = dot2(xv[i][j], 0x3f803f80u, s1[i]); s2[i] = dot2(xv[i][j], xv[i][j], s2[i]); }
                auto red8 = [&](float (&v)[8]) {
                    float a[4], c[2];
#pragma unroll
                    for (int i = 0; i < 4; i++) {
                        const auto t = __builtin_amdgcn_permlane32_swap(__float_as_uint(v[i]), __float_as_uint(v[i + 4]), false, false);
                        a[i] = __uint_as_float(t[0]) + __uint_as_float(t[1]);
                    }
#pragma unroll
                    for (int i = 0; i < 2; i++) {
                        const auto t = __builtin_amdgcn_permlane16_swap(__float_as_uint(a[i]), __float_as_uint(a[i + 2]), false, false);
                        c[i] = dpp_add<0x128, 0xf>(__uint_as_float(t[0]) + __uint_as_float(t[1]));
                    }
                    float e = (lane & 8) ? c[1] : c[0];
                    e = dpp_add<0x141, 0xf>(e); e = dpp_add<0xB1, 0xf>(e); e = dpp_add<0x4E, 0xf>(e);
                    return e;
                };
                const float a1 = red8(s1), a2 = red8(s2);
                const float mean = a1 * (1.f / 512.f);
                const float v1 = fmaxf(a2 * (1.f / 512.f) - mean * mean, 0.f);
                const float q1 = v1 + 1e-5f;
                const float rs = __builtin_amdgcn_rsqf(q1);
                const float q2 = v1 * rs * rs + 1e-5f;
                {
                    u32x4 o = {__float_as_uint(-mean), tag1.x, __float_as_uint(__builtin_amdgcn_sqrtf(q1 * q2)), tag1.x};
                    u64 keep;
                    asm volatile("s_mov_b64 %0, exec\n\ts_mov_b64 exec, %4\n\tglobal_store_dwordx4 %1, %2, %3 sc1\n\ts_mov_b64 exec, %0\n\ts_nop 1"
                                 : "=&s"(keep) : "v"(GR_ST + (u32)(tb8 * 8 + (lane >> 3)) * 16u), "v"(o), "s"(flags), "s"(0x0101010101010101ull) : "memory");
                }
                TS(42);
                f32x2 sc[4], of[4];
                const float nmr = -mean * rs;
#pragma unroll
                for (int i = 0; i < 4; i++) {
                    sc[i] = (f32x2){__builtin_bit_cast(float, __builtin_amdgcn_readlane(__builtin_bit_cast(int, rs), 16 * i)), __builtin_bit_cast(float, __builtin_amdgcn_readlane(__builtin_bit_cast(int, rs), 16 * i + 8))};
                    of[i] = (f32x2){__builtin_bit_cast(float, __builtin_amdgcn_readlane(__builtin_bit_cast(int, nmr), 16 * i)), __builtin_bit_cast(float, __builtin_amdgcn_readlane(__builtin_bit_cast(int, nmr), 16 * i + 8))};
                }
#pragma unroll
                for (int c = 0; c < 8; c++) {
                    u32x4 o;
#pragma unroll
                    for (int i = 0; i < 4; i++) {
                        const u32 a = xv[2 * i][c >> 1], d = xv[2 * i + 1][c >> 1];
                        const f32x2 v = (c & 1) ? (f32x2){bfhi(a), bfhi(d)} : (f32x2){bflo(a), bflo(d)};
                        const f32x2 n = __builtin_elementwise_fma(v, sc[i], of[i]);
                        o[i] = pkh(n.x, n.y);
                    }
                    astore_b128_sc1(p.ws + WS_U + (size_t)tb8 * 8192, (u32)(c * 1024 + lane * 16), o);
                }
                PRIO(V_KPRIO);
                TS(45);
            }
            const int rS = (int)((0x8080840876543210ull >> (4 * a)) & 15), rB = 17 + (int)((0xfa540000u >> (2 * a)) & 3);
            const int rT1 = (a & 3) ? 7 + (a & 3) : 0, rT2 = (a >> 2) ? 10 + (a >> 2) : 0;
            LAS char* tb = sm + L_TAB + kc * TAB_SZ + q * 16;
            LAS char *pS = tb + rS * TAB_ROW, *pB = tb + rB * TAB_ROW, *pT1 = tb + rT1 * TAB_ROW, *pT2 = tb + rT2 * TAB_ROW, *pZ = tb + 14 * TAB_ROW;
            const int hd = tab_off(kc + 2) - tab_off(kc);
            f32x4 G[2][4] = {};
            u32x4 T[2][7];
            auto ldtab = [&](int k) {
                if (k == 4) { pS += hd; pB += hd; pT1 += hd; pT2 += hd; pZ += hd; }
                u32x4* t = T[k & 1];
                const int o = (k & 3) * 64;
                t[0] = *(LAS u32x4*)(pS + o); t[1] = *(LAS u32x4*)(pB + o); t[2] = *(LAS u32x4*)(pT1 + o); t[3] = *(LAS u32x4*)(pT2 + o);
                t[4] = *(LAS u32x4*)(pZ + o); t[5] = *(LAS u32x4*)(pZ + o + TAB_ROW); t[6] = *(LAS u32x4*)(pZ + o + 2 * TAB_ROW);
            };
            int nb = isG ? 0 : 6; bool go = false;
#define SYNCPT() do { if (nb < 6) { if (go) { RBAR(); nb++; } else go = all_ge(*(volatile LAS u32*)(sm + F_L + (lane & 1) * 4), 2); } } while (0)
            lds_wait(sm + F_M + (lane & 3) * 4, 1);
            TS(3);
            ldtab(0);

#pragma unroll
            for (int k = 0; k < 8; k++) {
                u32x4* t = T[k & 1];
                f32x4* Gh = G[k >> 2];
                u32x4 A, B0, B1, B2, B3;
                asm volatile("" :: "v"(t[0]), "v"(t[1]), "v"(t[2]), "v"(t[3]), "v"(t[4]), "v"(t[5]), "v"(t[6]));
                if (k == 3) lds_wait(sm + F_P + 8, 1);
                TF(4 + 2 * k);
                if (k < 7) ldtab(k + 1);
                TF(5 + 2 * k);
                SCHED();
#pragma unroll
                for (int j = 0; j < 4; j++) { B0[j] = cmh2<false>(t[2][j], t[3][j], cmh1<false>(t[2][j], t[3][j])); A[j] = cmh2<true>(t[0][j], t[1][j], cmh1<true>(t[0][j], t[1][j])); }
                SCHED();
#pragma unroll
                for (int j = 0; j < 4; j++) B1[j] = cmh2<false>(B0[j], t[4][j], cmh1<false>(B0[j], t[4][j]));
                SCHED();
                Gh[0] = MFMA16H(A, B0, Gh[0]);
#pragma unroll
                for (int j = 0; j < 4; j++) B2[j] = cmh2<false>(B0[j], t[5][j], cmh1<false>(B0[j], t[5][j]));
                SCHED();
                Gh[1] = MFMA16H(A, B1, Gh[1]);
#pragma unroll
                for (int j = 0; j < 4; j++) B3[j] = cmh2<false>(B0[j], t[6][j], cmh1<false>(B0[j], t[6][j]));
                SCHED();
                Gh[2] = MFMA16H(A, B2, Gh[2]);
                asm volatile("s_nop 1" : "+v"(B3));
                Gh[3] = MFMA16H(A, B3, Gh[3]);
                SCHED();
                if (V_S1) SYNCPT();
                if (k == V_LNK && kc) { WAITV(0); flag_store(flags, (u32)(FL_LN * 128 + tb8 * 8), tag1); if (!isG) lds_post(sm + F_P + 12, 1); TS(47); }
                if (!V_S1) SYNCPT();
            }
            TS(20);
            {
                const float M0 = *(LAS float*)(sm + L_MISC + kc * 4), M1 = *(LAS float*)(sm + L_MISC + 8 + kc * 4), M = fmaxf(M0, M1);
                const float s0 = __builtin_amdgcn_exp2f(M0 - M), s1 = __builtin_amdgcn_exp2f(M1 - M);
                const float dx = *(LAS float*)(sm + L_MISC + 16 + kc * 4) * __builtin_amdgcn_exp2f(-M), d0 = lane == 0 ? dx : 0.f;
                LAS char* gx = sm + L_TAB + kc * TAB_SZ;
#pragma unroll
                for (int ct = 0; ct < 4; ct++) {
                    float v[4];
#pragma unroll
                    for (int e = 0; e < 4; e++) v[e] = opq(fmaf(G[1][ct][e], s1, (ct | e) ? opq(G[0][ct][e] * s0) : fmaf(G[0][ct][e], s0, d0)));
                    u32x2 o = {pkh(v[0], v[1]), pkh(v[2], v[3])};
                    *(LAS u32x2*)(gx + (16 * ct + a) * 32 + q * 8) = o;
                }
                SYNCPT();
                WAITL0();
                const u32x4 u = *(LAS u32x4*)(gx + lane * 32), v = *(LAS u32x4*)(gx + lane * 32 + 16);
                LAS char* kp = sm + L_KP + kc * KP_CH + (lane & 1) * KP_PLANE + (16 + (lane >> 1)) * 16;
                const u32 LL = 0x01000100u, HH = 0x03020302u, SW = 0x01000302u;
                const u32 sa = h ? 0x05040100u : 0x05040706u, sb = h ? LL : SW, tie = (lane & 1) ? HH : SW;
                u32x4 w0 = {perm(u.w, v.x, sa), perm(0, u.z, sb), perm(u.y, u.z, sa), perm(0, u.x, sb)};
                u32x4 w1 = {perm(0, v.y, HH), perm(0, v.y, LL), perm(v.x, v.y, 0x07060100u), perm(0, v.x, HH)};
                u32x4 w2 = {perm(0, v.z, HH), perm(0, v.z, tie), perm(0, v.z, LL), perm(0, v.z, LL)};
                u32x4 w3 = {perm(0, v.w, HH), perm(0, v.w, tie), perm(0, v.w, LL), perm(0, v.w, LL)};
                *(LAS u32x4*)kp = w0; *(LAS u32x4*)(kp + 512) = w1; *(LAS u32x4*)(kp + 1024) = w2; *(LAS u32x4*)(kp + 1536) = w3;
                u32x4 z = {0, 0, 0, 0};
                *(LAS u32x4*)(sm + L_KP + kc * KP_CH + (q & 1) * KP_PLANE + a * 16) = z;
            }
            lds_post(sm + F_K + kc * 4, 1);
            TS(21);
            while (nb < 6) { RBAR(); nb++; }
            lds_wait(sm + F_M + (lane & 3) * 4, 9);
            if (!isG) lds_wait(sm + F_L + (lane & 1) * 4, 2);
            w2_dma(IC<V_PACE>(), 2, kc);
            w2_dma(IC<V_PACE>(), 3, kc);
            TS(24);
            asm volatile("s_waitcnt vmcnt(0)" : "+v"(junk) :: "memory");
            lds_post(sm + F_K + kc * 4, 12);
            TS(25);
        } else {
            PRIO(V_LPRIO);
            LOADER_VALS();
            LAS char* fm = sm + F_M + (lane & 3) * 4;
            LAS char* fl = sm + F_L + hw * 4;
            u32 junk = 0;
            TS(40);
            if (__builtin_expect(isG, 1)) { stage(0); RBAR(); }
            const int tb0 = hw * 128 + (lane >> 1);
            u32 ob = (u32)((r >> 2) * 1024 + (8 * g + 2 * (r & 3) + (lane & 1)) * 16), o0 = ob + (u32)tb0 * 8192u;
            u32x4 uv[4], f0, f1; u32x2 pv;
#define LOAD_LNF() asm volatile("global_load_dwordx4 %0, %2, %3 sc1\n\tglobal_load_dwordx4 %1, %2, %3 offset:1024 sc1" : "=&v"(f0), "=&v"(f1) : "v"(lfo), "s"(flags) : "memory")
#define WAIT_LNF() asm volatile("s_waitcnt vmcnt(0)" : "+v"(f0), "+v"(f1), "+v"(junk) :: "memory")
#define LNF_OK() (__builtin_amdgcn_ballot_w64((((f0.x ^ tag1.x) | (f0.z ^ tag1.x) | (f1.x ^ tag1.x) | (f1.z ^ tag1.x)) | ((f0.y ^ tag1.y) | (f0.w ^ tag1.y) | (f1.y ^ tag1.y) | (f1.w ^ tag1.y))) != 0u) == 0)
#define LOAD_U(B, M) do { _Pragma("unroll") for (int i = 0; i < 4; i++) uv[i] = aload_b128_sc1(B, (o0 + i * 32 * 8192) & (M)); pv = aload_b64_sc1(B, (ob + 127u * 8192u + 8u) & (M)); } while (0)
            u32 lfo = (u32)(FL_LN * 128 + lane * 16), vw2 = voffw2;
            asm volatile("" : "+v"(ob), "+v"(o0), "+v"(lfo), "+v"(vw2));
            if (__builtin_expect(isG, 1)) {
                const char* nsrc = chunk_src(1);
#pragma unroll 1
                for (int kc = 1; kc < 8; kc++) {
                    u32 dst = lds0 + (kc & 3) * 32768 + hw * 16384;
                    asm volatile("" : "+s"(nsrc), "+s"(dst));
                    if (kc >= 4) RBAR();
                    TL(kc + 6);
                    glds_run<4, false, V_PACE>(nsrc, lgs, vo0, vo1, vo2, vo3, dst);
                    nsrc = chunk_src(kc + 1);
                    WAITV(16);
                    if (kc < 3) *(volatile LAS u32*)fl = kc;
                    if (kc == 3) READ_TAG();
                }
                RBAR();
                TL(14);
            } else {
                BAR();
                const char* wb = (const char*)p.w1 + (lj < 28 ? lj : 0) * 128 + (size_t)hw * 256 * 3584;
                const u32 v0 = (u32)((lane >> 3) * 3584) + vsw;
                glds_run<8, false, V_PACE>(wb, 32u * 3584u, v0, v0 + 27648u, v0 + 55296u, v0 + 82944u, lds0 + hw * 32768);
                f32x16 c0 = {}, c1 = {};
                const u32x4 ones = {0x3f803f80u, 0x3f803f80u, 0x3f803f80u, 0x3f803f80u};
#pragma unroll 1
                for (int s0 = 0; s0 < 16; s0 += 8) {
                    u32x4 fa[8], fb[8];
                    if (s0 == 0) WAITV(16); else WAITV(0);
#pragma unroll
                    for (int s = 0; s < 8; s++) {
                        LAS char* a = sm + hw * 32768 + (s0 + s) * 2048;
                        fa[s] = trfrag(a + tra0, a + tra0 + 1024); fb[s] = trfrag(a + (tra0 ^ 64), a + (tra0 ^ 64) + 1024);
                    }
                    SCHED();
#pragma unroll
                    for (int s = 0; s < 8; s++) { c0 = MFMA32(ones, fa[s], c0); c1 = MFMA32(ones, fb[s], c1); }
                }
                const float c = h ? c1[0] : c0[0];
                *(LAS float*)(sm + L_MISC + 512 + hw * 256 + lane * 4) = c;
                lds_post(fl, 1);
                READ_TAG();
                lds_wait(sm + F_L + (lane & 1) * 4, 1);
                u32x2 o = {__float_as_uint(*(LAS float*)(sm + L_MISC + 512 + lane * 4) + *(LAS float*)(sm + L_MISC + 768 + lane * 4)), tag1.x};
                if (hw == 0 && lj < 28) astore_b64_sc1(flags, GR_CS + (u32)(lj * 64 + lane) * 8u, o);
                lds_post(fl, 2); lds_wait(fm, 10);
                __builtin_amdgcn_s_sleep(V_SPD);
            }
            LOAD_LNF();
            w2_dma(IC<V_PACE>(), 0, hw);
            WAITV(16);
            if (isG) RBAR();
            TL(15);
            asm volatile("s_waitcnt vmcnt(14)" : "+v"(f0), "+v"(f1) :: "memory");
            const bool uok = LNF_OK();
            { const char* ub = uok ? (const char*)p.ws + WS_U : (const char*)p.b2; LOAD_U(ub, uok ? ~0u : 0u); }
            w2_dma(IC<V_PACE>(), 1, hw);
            TS(24);
            asm volatile("s_waitcnt vmcnt(0)" : "+v"(uv[0]), "+v"(uv[1]), "+v"(uv[2]), "+v"(uv[3]), "+v"(pv) :: "memory");
            lds_post(fl, 12);
            TS(25);
            if (!uok) {
#pragma unroll 1
                for (int spin = 0; spin < (1 << 16); ++spin) {
                    LOAD_LNF(); WAIT_LNF();
                    if (LNF_OK()) break;
                    __builtin_amdgcn_s_sleep(V_PSLEEP);
                }
                TS(22);
                LOAD_U(p.ws + WS_U, ~0u);
                asm volatile("s_waitcnt vmcnt(0)" : "+v"(uv[0]), "+v"(uv[1]), "+v"(uv[2]), "+v"(uv[3]), "+v"(pv) :: "memory");
            }
            if (b == 0 && hw == 0) flag_store(flags, FL_E * 128, tag1);
            PRIO(V_UPRIO);
            lds_wait(fm, 9);
            lds_wait(sm + F_K + (lane & 1) * 4, 1);
            TS(26);
            u32 r2[5], r3[5];
            r2[0] = hw ? pv.x : 0u; r3[0] = hw ? pv.y : 0u;
#pragma unroll
            for (int i = 0; i < 4; i++) {
                r2[i + 1] = __builtin_amdgcn_ds_bpermute(((lane - 2) & 63) * 4, uv[i].z);
                r3[i + 1] = __builtin_amdgcn_ds_bpermute(((lane - 2) & 63) * 4, uv[i].w);
            }
            LAS char* d = sm + L_UC + (lane & 1) * UC_CH + (tb0 + 4) * 16;
#pragma unroll
            for (int i = 0; i < 4; i++) *(LAS u32x4*)(d + i * 512) = uv[i];
#pragma unroll
            for (int i = 0; i < 4; i++) {
                const u32x4 t = uv[i];
                const u32 q2 = lane < 2 ? r2[i] : r2[i + 1], q3 = lane < 2 ? r3[i] : r3[i + 1];
                const u32 a0 = __builtin_amdgcn_alignbit(q3, q2, 16), a1 = __builtin_amdgcn_alignbit(t.x, q3, 16), a2 = __builtin_amdgcn_alignbit(t.y, t.x, 16),
                          a3 = __builtin_amdgcn_alignbit(t.z, t.y, 16), a4 = __builtin_amdgcn_alignbit(t.w, t.z, 16);
                u32x4 c1 = {a1, a2, a3, a4}, c2 = {q3, t.x, t.y, t.z}, c3 = {a0, a1, a2, a3};
                *(LAS u32x4*)(d + i * 512 + UC_COPY) = c1;
                *(LAS u32x4*)(d + i * 512 + 2 * UC_COPY) = c2;
                *(LAS u32x4*)(d + i * 512 + 3 * UC_COPY) = c3;
            }
            u32x4 z = {0, 0, 0, 0};
            *(LAS u32x4*)(sm + L_UC + hw * UC_CH + ((lane >> 2) & 3) * UC_COPY + (lane & 3) * 16) = z;
            lds_post(fl, 13);
            TS(35);
        }
        lds_wait(sm + F_L + (lane & 1) * 4, 13);
        TS(36);
        LAS char* ub = sm + L_UC + cl * UC_CH + (3 - (a & 3)) * UC_COPY + (32 * rh + 2 * q + 5 + (a >> 2)) * 8;
        LAS char* kb = sm + L_KP + cl * KP_CH + ((q + 1) & 1) * KP_PLANE + (16 - 8 * rh - 6 + a - (q >> 1)) * 16;
        f32x4 y[8] = {};
        auto ldA = [&](int dl, int i) {
            const u32x2 lo = *(volatile LAS u32x2*)(ub + dl * 64 + i * 512), hi = *(volatile LAS u32x2*)(ub + dl * 64 + i * 512 + 8);
            return (u32x4){lo.x, lo.y, hi.x, hi.y};
        };
        auto ldB = [&](int dl, int i) { return *(LAS u32x4*)(kb + (3 - dl) * 32 + i * 256); };
        u32x4 A0[8], B0[8], A1[8], B1[8];
#pragma unroll
        for (int i = 0; i < 8; i++) { B0[i] = ldB(0, i); A0[i] = ldA(0, i); }
        lds_wait(sm + F_M + (lane & 3) * 4, 10);
        PRIO(V_CPRIO);
        TS(27);
        auto cstep = [&](u32x4 (&A)[8], u32x4 (&B)[8], u32x4 (&An)[8], u32x4 (&Bn)[8], int dn) {
            asm volatile("" :: "v"(A[0]), "v"(A[1]), "v"(A[2]), "v"(A[3]), "v"(A[4]), "v"(A[5]), "v"(A[6]), "v"(A[7]));
            asm volatile("" :: "v"(B[0]), "v"(B[1]), "v"(B[2]), "v"(B[3]), "v"(B[4]), "v"(B[5]), "v"(B[6]), "v"(B[7]));
            SCHED();
            int n = 0;
#pragma unroll
            for (int gm = 0; gm < 8; gm++)
#pragma unroll
                for (int c = gm; c < 8; c++) {
                    y[c] = MFMA16H(A[gm], B[c - gm], y[c]);
                    if (dn < 4 && n < 24) { if (n % 3 == 0) Bn[n / 3] = ldB(dn, n / 3); else if (n % 3 == 1) An[n / 3] = ldA(dn, n / 3); }
                    n++;
                    SCHED();
                }
        };
        cstep(A0, B0, A1, B1, 1); cstep(A1, B1, A0, B0, 2); cstep(A0, B0, A1, B1, 3); cstep(A1, B1, A0, B0, 4);
        PRIO(3);
        TS(28);
        const float ksc = __builtin_amdgcn_exp2f(fmaxf(*(LAS float*)(sm + L_MISC + cl * 4), *(LAS float*)(sm + L_MISC + 8 + cl * 4)));
        LAS char* fh = sm + F_H + hw * 4;
        LAS char* fp = sm + F_H + (hw ^ 2) * 4;
        lds_post(fh, 1); lds_wait(fp, 1);
        TS(37);
        LAS char* xb = sm + L_UC + cl * UC_CH + lane * 16;
        if (rh) {
#pragma unroll
            for (int c = 0; c < 4; c++) *(LAS f32x4*)(xb + 4096 + c * 1024) = y[c];
            lds_post(fh, 2);
#pragma unroll
            for (int c = 0; c < 4; c++)
#pragma unroll
                for (int e = 0; e < 4; e++) y[c][e] = opq(y[4 + c][e] * ksc);
        } else {
#pragma unroll
            for (int c = 0; c < 4; c++) *(LAS f32x4*)(xb + c * 1024) = y[4 + c];
            lds_post(fh, 2);
#pragma unroll
            for (int c = 0; c < 4; c++)
#pragma unroll
                for (int e = 0; e < 4; e++) y[c][e] = opq(y[c][e] * ksc);
        }
        char* sdst = p.ws + WS_S + (size_t)chn * 4096 + rh * 2048;
        TS(38);
        lds_wait(fp, 2);
        TS(39);
        {
            u32x2 o[4];
#pragma unroll
            for (int c = 0; c < 4; c++) {
                const f32x4 v = *(LAS f32x4*)(xb + (rh ^ 1) * 4096 + c * 1024);
                float f[4];
#pragma unroll
                for (int e = 0; e < 4; e++) f[e] = opq(fmaf(v[e], ksc, y[c][e]));
                o[c] = (u32x2){pkbf(f[0], f[1]), pkbf(f[2], f[3])};
            }
#pragma unroll
            for (int cp = 0; cp < 2; cp++) {
                const auto lo = __builtin_amdgcn_permlane16_swap(o[2 * cp].x, o[2 * cp + 1].x, false, false);
                const auto hi = __builtin_amdgcn_permlane16_swap(o[2 * cp].y, o[2 * cp + 1].y, false, false);
                astore_b128_sc1(sdst, (u32)(cp * 1024 + lane * 16), (u32x4){lo[0], hi[0], lo[1], hi[1]});
            }
        }
        TS(29);
        WAITV(0);
        lds_post(fh, 3);
        lds_wait(sm + F_H + (lane & 3) * 4, 3);
        if (wid == 4) flag_store(flags, (u32)(FL_S + b) * 128u, tag1);
        PRIO(0);
        TS(30);
        xe = *(const u32x4*)((const char*)p.x + xo); b2e = *(const u32x4*)((const char*)p.b2 + dt * 128 + (tid & 7) * 16);
        __builtin_amdgcn_s_sleep(V_SDELAY);
        poll_flags(flags, FL_S + dt + 8 * hw, 32, 8, tag1, lane);
        TS(31);
        u32x4 sv[2];
#pragma unroll
        for (int i = 0; i < 2; i++) {
            const int d = hw * 16 + i * 8 + (lane >> 3);
            sv[i] = aload_b128_sc1(p.ws + WS_S, (u32)((dt * 64 + d) * 4096 + (tt >> 3) * 1024 + ((tt >> 2) & 1) * 256 + ((lane >> 2) & 1) * 512 + (tt & 3) * 64 + (lane & 3) * 16));
        }
        asm volatile("s_waitcnt vmcnt(0)" : "+v"(sv[0]), "+v"(sv[1]) :: "memory");
#pragma unroll
        for (int i = 0; i < 2; i++) {
            const int d = hw * 16 + i * 8 + (lane >> 3);
            *(LAS u32x4*)(sm + L_UC + d * 128 + (((2 * (lane & 3) + ((lane >> 2) & 1)) ^ (d >> 3)) << 4)) = sv[i];
        }
        TS(32);
    }
    {
        LANEVALS();
        const int tok = tid >> 3, dg = tid & 7;
        float m[8], xf[8];
#pragma unroll
        for (int e = 0; e < 4; e++) { m[2 * e] = bflo(b2e[e]); m[2 * e + 1] = bfhi(b2e[e]); xf[2 * e] = bflo(xe[e]); xf[2 * e + 1] = bfhi(xe[e]); }
        u32 pa = (u32)(L_W2 + tok * 272 + dg * 32), pb = pa + 2 * W2_REG, sa = (u32)(L_UC + dg * 1024 + (((tok >> 3) ^ dg) << 4) + (tok & 7) * 2), oo = xo;
        asm volatile("" : "+v"(m[0]), "+v"(m[1]), "+v"(m[2]), "+v"(m[3]), "+v"(m[4]), "+v"(m[5]), "+v"(m[6]), "+v"(m[7]), "+v"(pa), "+v"(pb), "+v"(sa), "+v"(oo));
        asm volatile("" : "+v"(xf[0]), "+v"(xf[1]), "+v"(xf[2]), "+v"(xf[3]), "+v"(xf[4]), "+v"(xf[5]), "+v"(xf[6]), "+v"(xf[7]));
        BAR();
        TS(33);
#pragma unroll
        for (int i = 0; i < 4; i++) {
            LAS char* q = sm + (i < 2 ? pa : pb) + (i & 1) * W2_REG;
            const f32x4 lo = *(LAS f32x4*)q, hi = *(LAS f32x4*)(q + 16);
#pragma unroll
            for (int e = 0; e < 4; e++) { m[e] += lo[e]; m[4 + e] += hi[e]; }
        }
        float o[8];
#pragma unroll
        for (int e = 0; e < 8; e++) {
            const float s = bf2f(*(LAS u16*)(sm + sa + e * 128));
            o[e] = fmaf(s * m[e], __builtin_amdgcn_rcpf(1.f + __builtin_amdgcn_exp2f(-1.4426950408889634f * m[e])), xf[e]);
        }
        u32x4 ov = {pkbf(o[0], o[1]), pkbf(o[2], o[3]), pkbf(o[4], o[5]), pkbf(o[6], o[7])};
        *(u32x4*)((char*)p.out + oo) = ov;
    }
    TS(34); TR(63);
    TSOUT();
}

extern "C" void kernel_launch(void* const* d_in, const int* in_sizes, int n_in, void* d_out, int out_size, void* d_ws, size_t ws_size,
                              hipStream_t stream) {
    Params p;
    p.x = (const u16*)d_in[0]; p.Are = (const u16*)d_in[1]; p.Aim = (const u16*)d_in[2]; p.Bre = (const u16*)d_in[3]; p.Bim = (const u16*)d_in[4];
    p.Cre = (const u16*)d_in[5]; p.Cim = (const u16*)d_in[6]; p.logdt = (const u16*)d_in[7]; p.Dskip = (const u16*)d_in[8];
    p.w1 = (const u16*)d_in[10]; p.b1 = (const u16*)d_in[11]; p.w2 = (const u16*)d_in[12]; p.b2 = (const u16*)d_in[13];
    p.out = (u16*)d_out; p.ws = (char*)d_ws;
    fused_kernel<<<256, 512, 0, stream>>>(p);
}
```

```cpp
#define V_OST 1
#define V_KROT 0
#define V_MALL 5
#include <hip/hip_runtime.h>
#include <stdint.h>
template <int N> struct IC { static constexpr int value = N; };
#define V_W2ROT 0
#define V_TOUCH 0
#define V_CODE 1
#define V_MPRIO 0
#define V_KPRIO 1
#define V_PSLEEP 6
#define V_SSTEP 14
#define V_UAT 6
#define V_FSP 8
#define V_SPD 16
#define V_MDELAY 16
#define V_LDELAY 2
#define V_LNK 1
#define V_LPRIO 0
#define V_PACE 16
#define V_NOP 0
#define V_MTHR 10
#define V_WIN 8
#define V_SDELAY 16
#define V_UB 5
#define V_CPRIO 2
#define V_EARLY 1
#define V_HROT 0
#define V_UPRIO 1
#define V_LEAD 0
#define V_S0 0
#define V_S1 0
#define V_TLB 0
#define CODE_LINES 224

typedef unsigned short u16;
typedef unsigned u32;
typedef unsigned long long u64;
typedef u32 u32x2 __attribute__((ext_vector_type(2)));
typedef u32 u32x4 __attribute__((ext_vector_type(4)));
typedef short s16x4 __attribute__((ext_vector_type(4)));
typedef __bf16 bf16x2 __attribute__((ext_vector_type(2)));
typedef __bf16 bf16x8 __attribute__((ext_vector_type(8)));
typedef _Float16 f16x2 __attribute__((ext_vector_type(2)));
typedef _Float16 f16x4 __attribute__((ext_vector_type(4)));
typedef _Float16 f16x8 __attribute__((ext_vector_type(8)));
typedef float f32x2 __attribute__((ext_vector_type(2)));
typedef float f32x4 __attribute__((ext_vector_type(4)));
typedef float f32x16 __attribute__((ext_vector_type(16)));
#define LAS __attribute__((address_space(3)))
#define DEV __device__ __forceinline__

DEV int tab_off(int w);
constexpr size_t WS_H = 12u << 20;
constexpr size_t WS_U = 8u << 20;
constexpr size_t WS_S = 10u << 20;
constexpr size_t WS_FLAG = 0;
constexpr size_t WS_STAMP = 20u << 20;
constexpr int FL_RDY = 0, FL_LN = 1344, FL_S = 32, FL_E = 288, FL_H = 320;
constexpr u32 GR_ST = 256u << 10, GR_CS = 320u << 10;

constexpr int L_W2 = 0, W2_REG = 28672;
constexpr int L_UC = 114688, UC_COPY = 4160, UC_CH = 4 * UC_COPY + 64;
constexpr int L_TAB = 131072, TAB_ROW = 288, TAB_SZ = 6144;
constexpr int L_MISC = 149504;
constexpr int L_KP = 150784, KP_PLANE = 2304, KP_CH = 2 * KP_PLANE;
constexpr int F_M = L_MISC + 64, F_L = L_MISC + 80, F_K = L_MISC + 88, F_H = L_MISC + 96, C_CS = L_MISC + 112, F_D = L_MISC + 128, F_P = L_MISC + 160;
constexpr int L_TAG = L_MISC + 112, L_JUNK = L_MISC + 256;
DEV int tab_off(int w) { return w == 3 ? L_KP + KP_CH : L_TAB + w * TAB_SZ; }

struct Params {
    const u16 *x, *Are, *Aim, *Bre, *Bim, *Cre, *Cim, *logdt, *Dskip, *w1, *b1, *w2, *b2;
    u16* out;
    char* ws;
};

DEV float bf2f(u32 v) { return __uint_as_float(v << 16); }
DEV float bflo(u32 v) { return __uint_as_float(v << 16); }
DEV float bfhi(u32 v) { return __uint_as_float(v & 0xffff0000u); }
DEV u32 pkbf(float a, float b) { f32x2 f = {a, b}; return __builtin_bit_cast(u32, __builtin_convertvector(f, bf16x2)); }
DEV u32 pkh(float a, float b) { f32x2 f = {a, b}; return __builtin_bit_cast(u32, __builtin_convertvector(f, f16x2)); }
DEV float opq(float a) { asm("" : "+v"(a)); return a; }
DEV float rbf(float a) { return bflo(pkbf(a, a)); }
DEV float dot2(u32 a, u32 b, float c) { return __builtin_amdgcn_fdot2_f32_bf16(__builtin_bit_cast(bf16x2, a), __builtin_bit_cast(bf16x2, b), c, false); }

#define WAITV(n) asm volatile("s_waitcnt vmcnt(" #n ")" ::: "memory")
#define WAITL0() asm volatile("s_waitcnt lgkmcnt(0)" ::: "memory")
#define BAR() do { asm volatile("s_waitcnt lgkmcnt(0)" ::: "memory"); __builtin_amdgcn_s_barrier(); asm volatile("" ::: "memory"); } while (0)
#define RBAR() do { asm volatile("" ::: "memory"); __builtin_amdgcn_s_barrier(); asm volatile("" ::: "memory"); } while (0)
#define SCHED() __builtin_amdgcn_sched_barrier(0)
#define PRIO(n) __builtin_amdgcn_s_setprio(n)
#define MFMA32(a, b, c) __builtin_amdgcn_mfma_f32_32x32x16_bf16(__builtin_bit_cast(bf16x8, a), __builtin_bit_cast(bf16x8, b), c, 0, 0, 0)
#define MFMA16H(a, b, c) __builtin_amdgcn_mfma_f32_16x16x32_f16(__builtin_bit_cast(f16x8, a), __builtin_bit_cast(f16x8, b), c, 0, 0, 0)

DEV const void* uni(const void* q) {
    const u64 a = (u64)q;
    return (const void*)(((u64)(u32)__builtin_amdgcn_readfirstlane((u32)(a >> 32)) << 32) | (u32)__builtin_amdgcn_readfirstlane((u32)a));
}
#define GLP ".if %c[q]\n\ts_nop %c[q]\n\t.endif\n\t"
#define GL4(B) ".if %c[p]\n\ts_waitcnt vmcnt(%c[p])\n\t.endif\n\tglobal_load_lds_dwordx4 %[v0], " B "\n\t" GLP "global_load_lds_dwordx4 %[v1], " B " offset:1024\n\t" GLP \
               "global_load_lds_dwordx4 %[v2], " B " offset:2048\n\t" GLP "global_load_lds_dwordx4 %[v3], " B " offset:3072\n\t"
template <int NG, bool HALF, int PACE> DEV void glds_run(const void* base, u32 step, u32 v0, u32 v1, u32 v2, u32 v3, u32 dst) {
    u32 keep;
    dst = __builtin_amdgcn_readfirstlane(dst);
    base = uni(base);
    asm volatile("s_mov_b32 %[k], m0\n\ts_mov_b32 m0, %[d]\n\ts_mov_b64 vcc, %[b]\n\ts_nop 1\n\t" GL4("vcc")
                 ".rept %c[n]\n\ts_add_u32 m0, m0, 0x1000\n\ts_add_u32 vcc_lo, vcc_lo, %[s]\n\ts_addc_u32 vcc_hi, vcc_hi, 0\n\t" GL4("vcc") ".endr\n\t"
                 ".if %c[h]\n\ts_add_u32 m0, m0, 0x1000\n\ts_add_u32 vcc_lo, vcc_lo, %[s]\n\ts_addc_u32 vcc_hi, vcc_hi, 0\n\t"
                 "global_load_lds_dwordx4 %[v0], vcc\n\t" GLP "global_load_lds_dwordx4 %[v1], vcc offset:1024\n\t.endif\n\t"
                 "s_mov_b32 m0, %[k]"
                 : [k] "=&s"(keep) : [v0] "v"(v0), [v1] "v"(v1), [v2] "v"(v2), [v3] "v"(v3), [b] "s"(base), [d] "s"(dst), [n] "i"(NG - 1), [s] "s"(step), [h] "i"(HALF ? 1 : 0), [p] "i"(PACE), [q] "i"(V_NOP)
                 : "memory", "scc", "vcc");
}
DEV void touch(const void* base, u32 voff, u32 junk) {
    u32 keep;
    base = uni(base);
    asm volatile("s_mov_b32 %0, m0\n\ts_mov_b32 m0, %3\n\ts_nop 2\n\tglobal_load_lds_dword %1, %2\n\ts_mov_b32 m0, %0"
                 : "=&s"(keep) : "v"(voff), "s"(base), "s"(junk) : "memory");
}
DEV u32 aload_u16(const void* base, u32 voff) { u32 r; asm volatile("global_load_ushort %0, %1, %2" : "=v"(r) : "v"(voff), "s"(base) : "memory"); return r; }
DEV u32 aload_b32(const void* base, u32 voff) { u32 r; asm volatile("global_load_dword %0, %1, %2" : "=v"(r) : "v"(voff), "s"(base) : "memory"); return r; }
DEV u32x2 aload_b64_sc1(const void* base, u32 voff) { u32x2 r; asm volatile("global_load_dwordx2 %0, %1, %2 sc1" : "=v"(r) : "v"(voff), "s"(base) : "memory"); return r; }
DEV u32x4 aload_b128_sc1(const void* base, u32 voff) { u32x4 r; asm volatile("global_load_dwordx4 %0, %1, %2 sc1" : "=v"(r) : "v"(voff), "s"(base) : "memory"); return r; }
DEV u32x4 aload_b128(const void* base, u32 voff) { u32x4 r; asm volatile("global_load_dwordx4 %0, %1, %2" : "=v"(r) : "v"(voff), "s"(base) : "memory"); return r; }
DEV void astore_b128_sc1(void* base, u32 voff, u32x4 v) { asm volatile("global_store_dwordx4 %0, %1, %2 sc1\n\ts_nop 1" :: "v"(voff), "v"(v), "s"(base) : "memory"); }
DEV void astore_b64_sc1(void* base, u32 voff, u32x2 v) { asm volatile("global_store_dwordx2 %0, %1, %2 sc1\n\ts_nop 1" :: "v"(voff), "v"(v), "s"(base) : "memory"); }
DEV void flag_store(void* base, u32 off, u32x2 v) {
    u64 keep;
    asm volatile("s_mov_b64 %0, exec\n\ts_mov_b64 exec, 1\n\tglobal_store_dwordx2 %1, %2, %3 sc1\n\ts_mov_b64 exec, %0\n\ts_nop 0"
                 : "=&s"(keep) : "v"(off), "v"(v), "s"(base) : "memory");
}
DEV u32x4 trfrag(LAS char* a, LAS char* b) {
    const u32x2 lo = __builtin_bit_cast(u32x2, __builtin_amdgcn_ds_read_tr16_b64_v4i16((LAS s16x4*)a));
    const u32x2 hi = __builtin_bit_cast(u32x2, __builtin_amdgcn_ds_read_tr16_b64_v4i16((LAS s16x4*)b));
    return (u32x4){lo.x, lo.y, hi.x, hi.y};
}

#define TS(i)
#define TF(i)
#define TL(i)
#define TR(i)
#define TSOUT()

DEV void poll_flags(const char* flags, int first, int stride, int nfl, u32x2 tag, int lane) {
    const u32 off = (u32)(first + (lane < nfl ? lane : 0) * stride) * 128u;
#pragma unroll 1
    for (int spin = 0; spin < (1 << 16); ++spin) {
        u32x2 v = aload_b64_sc1(flags, off);
        asm volatile("s_waitcnt vmcnt(0)" : "+v"(v) :: "memory");
        if (__builtin_amdgcn_ballot_w64(v.x != tag.x || v.y != tag.y) == 0) break;
        __builtin_amdgcn_s_sleep(V_PSLEEP);
    }
}
DEV bool all_ge(u32 v, u32 target) { return __builtin_amdgcn_ballot_w64(v < target) == 0; }
DEV void lds_wait(LAS char* w, u32 target) {
#pragma unroll 1
    for (int spin = 0; spin < (1 << 21); ++spin) {
        if (all_ge(*(volatile LAS u32*)w, target)) break;
        __builtin_amdgcn_s_sleep(1);
    }
    asm volatile("" ::: "memory");
}
DEV void lds_post(LAS char* w, u32 v) { WAITL0(); *(volatile LAS u32*)w = v; }
template <int CTRL, int RM> DEV float dpp_add(float v) {
    return v + __builtin_bit_cast(float, __builtin_amdgcn_update_dpp(0, __builtin_bit_cast(int, v), CTRL, RM, 0xf, false));
}
template <int CTRL, int RM> DEV u32 dpp_umax(u32 v) { const u32 o = __builtin_amdgcn_update_dpp(0u, v, CTRL, RM, 0xf, false); return v > o ? v : o; }
DEV u32 wave_umax(u32 v) {
    v = dpp_umax<0xB1, 0xf>(v); v = dpp_umax<0x4E, 0xf>(v); v = dpp_umax<0x141, 0xf>(v); v = dpp_umax<0x140, 0xf>(v);
    v = dpp_umax<0x142, 0xa>(v); v = dpp_umax<0x143, 0xc>(v);
    return __builtin_amdgcn_readlane(v, 63);
}
DEV f32x2 cexp_f(float e2, float rev) {
    const float m = __builtin_amdgcn_exp2f(e2);
    return (f32x2){m * __builtin_amdgcn_cosf(rev), m * __builtin_amdgcn_sinf(rev)};
}
DEV f32x2 cmul_f(f32x2 a, f32x2 z) {
    f32x2 t, r;
    asm("v_pk_mul_f32 %0, %1, %2 op_sel_hi:[1,0]" : "=v"(t) : "v"(a), "v"(z));
    asm("v_pk_fma_f32 %0, %1, %2, %3 op_sel:[1,1,0] op_sel_hi:[0,1,1] neg_lo:[0,1,0]" : "=v"(r) : "v"(a), "v"(z), "v"(t));
    return r;
}
template <bool CONJ> DEV u32 cmh1(u32 a, u32 z) {
    u32 t;
    if (CONJ) asm("v_pk_mul_f16 %0, %1, %2 op_sel_hi:[1,0] neg_hi:[1,0]" : "=v"(t) : "v"(a), "v"(z));
    else asm("v_pk_mul_f16 %0, %1, %2 op_sel_hi:[1,0]" : "=v"(t) : "v"(a), "v"(z));
    return t;
}
template <bool CONJ> DEV u32 cmh2(u32 a, u32 z, u32 t) {
    u32 r;
    if (CONJ) asm("v_pk_fma_f16 %0, %1, %2, %3 op_sel:[1,1,0] op_sel_hi:[0,1,1] neg_lo:[0,1,0] neg_hi:[0,1,0]" : "=v"(r) : "v"(a), "v"(z), "v"(t));
    else asm("v_pk_fma_f16 %0, %1, %2, %3 op_sel:[1,1,0] op_sel_hi:[0,1,1] neg_lo:[0,1,0]" : "=v"(r) : "v"(a), "v"(z), "v"(t));
    return r;
}
DEV void build_table(LAS char* sm, int w, int lane, u32 are, u32 aim, u32 bre, u32 bim, u32 cre, u32 cim, u32 ldt, u32 dsk) {
    LAS char* t = sm + tab_off(w) + lane * 4;
    *(LAS u32*)(sm + L_MISC + 16 + w * 4) = dsk << 16;
    auto put = [&](int row, f32x2 v) { *(LAS u32*)(t + row * TAB_ROW) = pkh(v.x, v.y); };
    const float dtv = rbf(__builtin_amdgcn_exp2f(bf2f(ldt) * 1.4426950408889634f));
    const float a2 = dtv * bf2f(are) * 1.4426950408889634f;
    float rev = dtv * bf2f(aim) * 0.15915494309189535f;
    rev -= __builtin_rintf(rev);
    f32x2 wv = cmul_f((f32x2){bf2f(cre), bf2f(cim)}, (f32x2){bf2f(bre), bf2f(bim)});
    u32 E = wave_umax(__float_as_uint(fmaxf(dtv * fmaxf(fabsf(wv.x), fabsf(wv.y)) * __builtin_amdgcn_exp2f(fmaxf(a2, 0.f) * 2048.f), fabsf(bf2f(dsk)) * 0.0078125f))) >> 23;
    E = E < 16u ? 16u : (E > 236u ? 236u : E);
    wv *= dtv * __uint_as_float((260u - E) << 23);
    *(LAS float*)(sm + L_MISC + w * 4) = (float)(int)E - 133.f;
    const f32x2 z1 = cexp_f(a2, rev), z64 = cexp_f(a2 * 64.f, rev * 64.f);
    const f32x2 z2 = cmul_f(z1, z1), z3 = cmul_f(z2, z1), z4 = cmul_f(z2, z2), z8 = cmul_f(z4, z4), z16 = cmul_f(z8, z8), z32 = cmul_f(z16, z16);
    const f32x2 z128 = cmul_f(z64, z64), z256 = cmul_f(z128, z128), z512 = cmul_f(z256, z256);
    const f32x2 w1 = cmul_f(wv, z512), w2 = cmul_f(w1, z512);
    *(LAS u32*)t = 0x00003c00u;
    put(1, z1); put(2, z2); put(3, z3); put(4, z4); put(5, cmul_f(z4, z1)); put(6, cmul_f(z3, z3)); put(7, cmul_f(z4, z3)); put(8, z8);
    put(9, z16); put(10, cmul_f(z16, z8)); put(11, z32); put(12, z64); put(13, cmul_f(z64, z32));
    put(14, z128); put(15, z256); put(16, cmul_f(z256, z128));
    put(17, wv); put(18, w1); put(19, w2); put(20, cmul_f(w2, z512));
}
DEV u32 perm(u32 hi, u32 lo, u32 sel) { return __builtin_amdgcn_perm(hi, lo, sel); }

__global__ __launch_bounds__(512, 2) void fused_kernel(Params p) {
    __shared__ __attribute__((aligned(1024))) char shm[163840];
    LAS char* sm = (LAS char*)shm;
    const u32 lds0 = (u32)(uintptr_t)sm;
    const int wid = __builtin_amdgcn_readfirstlane(threadIdx.x >> 6);
    const int b = blockIdx.x, g = b & 7, r = b >> 3, hw = wid & 3;
    char* flags = p.ws + WS_FLAG;
    const int ch0 = 64 * g + 16 * (r & 3) + (r >> 2);
    const int tt = g * 4 + (r >> 3), dt = r & 7;
    const int qo = V_W2ROT ? 2 * (tt & 1) : 0;
    const int k0 = V_KROT == 2 ? (r % 7 + 2 * (r / 7)) % 7 : (V_KROT ? r % 7 + 2 * (r / 7) : 0);
    const int rot = V_HROT == 0 ? 0 : (V_HROT == 2 ? (dt & 1) * 2 : (dt + (V_W2ROT ? tt & 2 : 0)) & 3);
    const bool isG = r < 28;
    const int mt = (g >> 1) * 4 + r / 7, ft = (g & 1) * 7 + r % 7;
#define LANEVALS() \
    int tid = threadIdx.x; asm volatile("" : "+v"(tid)); \
    const int lane = tid & 63, h = lane >> 5, l31 = lane & 31; \
    const u32 vsw = (u32)(((lane & 7) ^ (((lane >> 4) & 1) << 2)) << 4);          \
    const u32 voffw2 = (u32)((lane >> 3) * 1024 + dt * 128) + vsw; \
    auto w2_dma = [&](auto pace, int kq, int sg) {        \
        glds_run<3, true, decltype(pace)::value>((const char*)p.w2 + (size_t)(4 * ((kq + qo) & 3) + ((sg + rot) & 3)) * (112 * 1024), 32768u, voffw2, voffw2 + 7168u, voffw2 + 14336u, voffw2 + 21504u, \
                                 lds0 + L_W2 + kq * W2_REG + (sg & 1) * 14336); \
    }; \
    const int blk = (lane >> 4) & 1, q4 = (lane & 15) >> 2, pp = lane & 3; \
      \
    const u32 tra0 = (u32)((4 * h + q4) * 128 + (((2 * blk + (pp >> 1)) ^ (((q4 >> 1) & 1) << 2)) << 4) + 8 * (pp & 1)); \
    const u32 xo = (u32)((tt * 64 + (tid >> 3)) * 1024 + dt * 128 + (tid & 7) * 16); \
    (void)l31; (void)voffw2; (void)w2_dma; (void)tra0; (void)xo;
    if (V_CODE && wid == 6) { const u32 ln = (u32)(r + 32 * (threadIdx.x & 7)); touch((const char*)(__builtin_amdgcn_s_getpc() & ~127ull), (ln < CODE_LINES ? ln : 0u) * 128u, lds0 + L_JUNK); }
    if (wid == 0) *(LAS u32*)(sm + L_MISC + 64 + (threadIdx.x & 31) * 4) = 0;
    u32x2 tag1;
    u32x4 xe, b2e;
#define WAIT_PARAMS(n) asm volatile("s_waitcnt vmcnt(" #n ")" : "+v"(are), "+v"(aim), "+v"(bre), "+v"(bim), "+v"(cre), "+v"(cim), "+v"(ldt), "+v"(dsk) :: "memory")
#define LOAD_PARAMS(po, c2) \
    asm volatile("global_load_ushort %0, %8, %10\n\tglobal_load_ushort %1, %8, %11\n\tglobal_load_ushort %2, %8, %12\n\tglobal_load_ushort %3, %8, %13\n\t" \
                 "global_load_ushort %4, %8, %14\n\tglobal_load_ushort %5, %8, %15\n\tglobal_load_ushort %6, %9, %16\n\tglobal_load_ushort %7, %9, %17" \
                 : "=&v"(are), "=&v"(aim), "=&v"(bre), "=&v"(bim), "=&v"(cre), "=&v"(cim), "=&v"(ldt), "=&v"(dsk) \
                 : "v"(po), "v"(c2), "s"(p.Are), "s"(p.Aim), "s"(p.Bre), "s"(p.Bim), "s"(p.Cre), "s"(p.Cim), "s"(p.logdt), "s"(p.Dskip) : "memory")
#define LOADER_VALS() \
    const bool lx = wid == 4;                  \
    const u32 lR = lx ? (u32)(lane >> 3) * 1024u : (u32)(lane >> 4) * 3584u; \
    const u32 lS = lx ? (u32)(((lane & 7) ^ (lane >> 4)) << 4) : (u32)(((lane & 15) ^ ((lane >> 4) << 2)) << 4); \
    const u32 lst = lx ? 7168u : 13312u, x1 = lx ? 64u : 16u, x2 = lx ? 0u : 32u; \
    const u32 vo0 = lR + lS, vo1 = lR + (lS ^ x1) + lst, vo2 = lR + (lS ^ x2) + 2 * lst, vo3 = lR + (lS ^ (x1 | x2)) + 3 * lst; \
    const char* lsrc = lx ? (const char*)p.x + (size_t)mt * 128 * 1024 : (const char*)p.w1 + ft * 256; \
    const u32 lgs = lx ? 32768u : 16u * 3584u, lcs = lx ? 128u : 64u * 3584u; \
    auto chunk_src = [&](int kc) { return lsrc + (V_KROT == 2 ? (kc == 0 ? 0 : (kc + k0 > 7 ? kc + k0 - 7 : kc + k0)) : (kc + k0) & 7) * lcs; }; \
    auto stage = [&](int kc) { glds_run<4, false, V_PACE>(chunk_src(kc), lgs, vo0, vo1, vo2, vo3, lds0 + (kc & 3) * 32768 + hw * 16384); };
#define READ_TAG() do { u32x4 t_ = *(volatile LAS u32x4*)(sm + L_TAG); \
        if (!all_ge(t_.y, 1)) { lds_wait(sm + L_TAG + 4, 1); t_ = *(volatile LAS u32x4*)(sm + L_TAG); } \
        tag1.x = __builtin_amdgcn_readfirstlane(t_.z); tag1.y = __builtin_amdgcn_readfirstlane(t_.w); } while (0)
    const int lj = g * 4 + (r - 28);

    const int tb8 = g * 32 + r;

    if (wid < 4) {
        LANEVALS();
        const int kq = wid;
        u32x4 wreg[14][2];
        LAS char* w2a = sm + L_W2 + kq * W2_REG;
        auto w2frag = [&](int s, int j2) { LAS char* a = w2a + (j2 ? tra0 ^ 64 : tra0) + s * 2048; return trfrag(a, a + 1024); };
        auto w2load = [&]() {
#pragma unroll
            for (int s = 0; s < 14; s++) { wreg[s][0] = w2frag(s, 0); wreg[s][1] = w2frag(s, 1); }
        };
#define W2PIN() _Pragma("unroll") for (int s = 0; s < 14; s++) asm volatile("" : "+v"(wreg[s][0]), "+v"(wreg[s][1]))
        LAS char* fw = sm + F_L + (wid & 2) * 4 + (lane & 1) * 4;
        bool early = false;
        const int th = wid & 1, fh = wid >> 1;
        u32 are, aim, bre, bim, cre, cim, ldt, dsk, b1a, b1b;
        {
            const int chp = ch0 + 8 * th;
            const u32 po = (u32)(chp * 128 + fh * 64 + lane) * 2u, c2 = (u32)chp * 2u, bo = (u32)(ft * 128 + fh * 64 + l31) * 2u;
            if (wid < 3) LOAD_PARAMS(po, c2);
            asm volatile("global_load_ushort %0, %2, %3\n\tglobal_load_ushort %1, %2, %3 offset:64" : "=&v"(b1a), "=&v"(b1b) : "v"(bo), "s"(p.b1) : "memory");
            TS(40);
            BAR();
            SCHED();
        }
        PRIO(V_MPRIO);
        u32 xa[4], wa[2][2];
#pragma unroll
        for (int s = 0; s < 4; s++) xa[s] = (u32)((64 * th + l31) * 128 + (((2 * s + h) ^ ((l31 >> 1) & 7)) << 4));
#pragma unroll
        for (int j2 = 0; j2 < 2; j2++)
#pragma unroll
            for (int t = 0; t < 2; t++)
                wa[j2][t] = (u32)(16384 + 256 * (8 * h + 4 * t + q4) + (((4 * (2 * fh + j2) + 2 * blk + (pp >> 1)) ^ ((q4 << 2) | (2 * h + t))) << 4) + 8 * (pp & 1));
        u32 so = GR_ST + (u32)(mt * 128 + th * 64 + l31) * 16u, co = GR_CS + (u32)(ft * 128 + fh * 64 + l31) * 8u;
        asm volatile("" : "+v"(so), "+v"(co), "+v"(xa[0]), "+v"(xa[1]), "+v"(xa[2]), "+v"(xa[3]), "+v"(wa[0][0]), "+v"(wa[0][1]), "+v"(wa[1][0]), "+v"(wa[1][1]));
        WAIT_PARAMS(2);
        TS(1);
        if (wid < 3) build_table(sm, wid, lane, are, aim, bre, bim, cre, cim, ldt, dsk);
        LAS char* fm = sm + F_M + wid * 4;
        LAS char* fl = sm + F_L + (lane & 1) * 4;
        lds_post(fm, 1);
        TS(2);
        if (isG) {
            f32x16 acc[2][2] = {};
            lds_wait(fl, 1);
            TS(3);
            u32x4 X[4][2], W[4][2];
            u32 wp[2][2], xp;
#define RX(s, j) X[s][j] = *(LAS u32x4*)(sm + xp + (j) * 4096)
#define RW(s, j) W[s][j] = trfrag(sm + wp[j][0] + (s) * 4096, sm + wp[j][1] + (s) * 4096)
#define OPQ(v) asm volatile("" : "+v"(v))
#define MM(s, j2, j) acc[j2][j] = MFMA32(W[s][j2], X[s][j], acc[j2][j])
            u32x4 st0, st1; u32x2 cs0, cs1;
#define LOAD_GRAN() asm volatile("global_load_dwordx4 %0, %4, %6 sc1\n\tglobal_load_dwordx4 %1, %4, %6 offset:512 sc1\n\tglobal_load_dwordx2 %2, %5, %6 sc1\n\tglobal_load_dwordx2 %3, %5, %6 offset:256 sc1" \
                                 : "=&v"(st0), "=&v"(st1), "=&v"(cs0), "=&v"(cs1) : "v"(so), "v"(co), "s"(flags) : "memory")
            wp[0][0] = wa[0][0]; wp[0][1] = wa[0][1]; wp[1][0] = wa[1][0]; wp[1][1] = wa[1][1];
            xp = xa[0]; RX(0, 0); SCHED(); RW(0, 0); SCHED(); RW(0, 1); SCHED(); RX(0, 1); SCHED();
            xp = xa[1]; RX(1, 0); SCHED(); RW(1, 0); SCHED(); RW(1, 1); SCHED(); RX(1, 1); SCHED();
            xp = xa[2];
#pragma unroll
            for (int kc = 0; kc < 7; kc++) {
                const u32 sl = (u32)(kc & 3) * 32768u, sn = (u32)((kc + 1) & 3) * 32768u;
                if (kc == 5) LOAD_GRAN();
                MM(0, 0, 0); RX(2, 0); SCHED(); MM(0, 1, 0); RW(2, 0); SCHED(); MM(0, 0, 1); RW(2, 1); SCHED();
                MM(0, 1, 1); RX(2, 1); xp = xa[3] + sl; OPQ(xp); SCHED();
                MM(1, 0, 0); RX(3, 0);
                SCHED();
                MM(1, 1, 0); RW(3, 0); SCHED(); MM(1, 0, 1); RW(3, 1); SCHED();
                MM(1, 1, 1); RX(3, 1); xp = xa[0] + sn; wp[0][0] = wa[0][0] + sn; wp[0][1] = wa[0][1] + sn; OPQ(xp); OPQ(wp[0][0]); OPQ(wp[0][1]); SCHED();
                MM(2, 0, 0); SCHED();
                if (kc == 0) lds_wait(fl, 2); else RBAR();
                SCHED();
                RX(0, 0); wp[1][0] = wa[1][0] + sn; wp[1][1] = wa[1][1] + sn; OPQ(wp[1][0]); OPQ(wp[1][1]); SCHED();
                MM(2, 1, 0); RW(0, 0); SCHED(); MM(2, 0, 1); RW(0, 1); SCHED();
                MM(2, 1, 1); RX(0, 1); xp = xa[1] + sn; OPQ(xp); SCHED();
                MM(3, 0, 0); RX(1, 0); SCHED(); MM(3, 1, 0); RW(1, 0); SCHED(); MM(3, 0, 1); RW(1, 1); SCHED();
                MM(3, 1, 1); RX(1, 1); xp = xa[2] + sn; OPQ(xp);
                SCHED();
                TF(4 + kc);
            }
            MM(0, 0, 0); RX(2, 0); RW(2, 0); SCHED(); MM(0, 1, 0); RW(2, 1); RX(2, 1); xp = xa[3] + 3 * 32768; SCHED();
            MM(0, 0, 1); RX(3, 0); RW(3, 0); SCHED(); MM(0, 1, 1); RW(3, 1); RX(3, 1); SCHED();
            MM(1, 0, 0); MM(1, 1, 0); SCHED();
            READ_TAG();
            lds_post(fm, 9);
            asm volatile("s_waitcnt vmcnt(0)" : "+v"(st0), "+v"(st1), "+v"(cs0), "+v"(cs1), "+v"(b1a), "+v"(b1b) :: "memory");
#pragma unroll 1
            for (int spin = 0; spin < (1 << 16); ++spin) {
                if (__builtin_amdgcn_ballot_w64((((st0.y ^ tag1.x) | (st0.w ^ tag1.x) | (st1.y ^ tag1.x)) | ((st1.w ^ tag1.x) | (cs0.y ^ tag1.x) | (cs1.y ^ tag1.x))) != 0u) == 0) break;
                __builtin_amdgcn_s_sleep(2);
                LOAD_GRAN();
                asm volatile("s_waitcnt vmcnt(0)" : "+v"(st0), "+v"(st1), "+v"(cs0), "+v"(cs1) :: "memory");
            }
            TS(19);
            float rinv[2] = {__uint_as_float(st0.z), __uint_as_float(st1.z)};
            float k1[2], k2[2];
            {
                u32x4 Ax[2], Bx[2];
                auto prepB = [&](int j) {
                    const float nm = __uint_as_float(j ? st1.x : st0.x), mh = rbf(nm), ih = rbf(rinv[j]);
                    const u32 mm = pkbf(mh, opq(nm - mh));
                    const u32 hm = (u32)h - 1u;
                    Bx[j] = (u32x4){mm & hm, mm & hm, pkbf(ih, opq(rinv[j] - ih)) & hm, 0u};
                };
                auto prepA = [&](int j) {
                    const float c = __uint_as_float(j ? cs1.x : cs0.x), chh = rbf(c), cl = opq(c - chh);
                    const u32 bb = j ? b1b : b1a;
                    Ax[j] = (u32x4){pkbf(chh, chh), pkbf(cl, cl), bb | (bb << 16), 0u};
                };
                auto prepK = [&](int j) {
                    const float rr = __builtin_amdgcn_rcpf(rinv[j]);
                    k1[j] = opq(-2.302208198144325f * rr); k2[j] = opq(-0.10294323958002349f * rr * rr * rr);
                };
                MM(1, 0, 1); MM(1, 1, 1); prepB(0); SCHED();
                MM(2, 0, 0); prepA(0); SCHED(); MM(2, 1, 0); prepA(1); SCHED(); MM(2, 0, 1); MM(2, 1, 1); prepB(1); SCHED();
                MM(3, 0, 0); prepK(0); SCHED(); acc[0][0] = MFMA32(Ax[0], Bx[0], acc[0][0]); prepK(1); SCHED();
                MM(3, 1, 0); acc[1][0] = MFMA32(Ax[1], Bx[0], acc[1][0]);
                MM(3, 0, 1); acc[0][1] = MFMA32(Ax[0], Bx[1], acc[0][1]);
                MM(3, 1, 1); acc[1][1] = MFMA32(Ax[1], Bx[1], acc[1][1]);
                SCHED();

            }
            PRIO(2);
            TS(20);
            char* hdst = p.ws + WS_H + (size_t)((mt * 4 + th * 2) * 112 + ft * 8 + fh * 4) * 1024;
#pragma unroll
            for (int j = 0; j < 2; j++) {
                const f32x2 K1 = {k1[j], k1[j]}, K2 = {k2[j], k2[j]}, RI = {rinv[j], rinv[j]};
#pragma unroll
                for (int j2 = 0; j2 < 2; j2++) {
#pragma unroll
                    for (int s = 0; s < 2; s++) {
                        u32x4 o;
#pragma unroll
                        for (int i = 0; i < 4; i++) {
                            const f32x2 u = {acc[j2][j][8 * s + 2 * i], acc[j2][j][8 * s + 2 * i + 1]};
                            const f32x2 z = __builtin_elementwise_fma(u * u, K2, K1) * u;
                            const f32x2 e = {__builtin_amdgcn_exp2f(z.x), __builtin_amdgcn_exp2f(z.y)};
                            const f32x2 dn = __builtin_elementwise_fma(e, RI, RI);
                            const f32x2 gl = u * (f32x2){__builtin_amdgcn_rcpf(dn.x), __builtin_amdgcn_rcpf(dn.y)};
                            o[i] = pkbf(gl.x, gl.y);
                        }
                        asm volatile("global_store_dwordx4 %0, %1, %2 offset:%3 sc1\n\ts_nop 1" :: "v"(lane * 16 + j * 112 * 1024), "v"(o), "s"(hdst), "i"((j2 * 2 + s) * 1024) : "memory");
                        SCHED();
                        if (V_EARLY && j == 1 && j2 == 1 && s == 0) { early = all_ge(*(volatile LAS u32*)fw, 12); if (early) w2load(); SCHED(); }
                    }
                }
            }
            if (early) { W2PIN(); }
            lds_post(fm, 10);
            TS(21);
        } else {
            lds_wait(sm + F_P + 12, 1);
            if (V_MALL & 4) {
                const u32 v0 = (u32)lane * 16u;
                glds_run<3, true, V_MTHR>((const char*)p.w2 + (size_t)(lj * 4 + wid) * 14336, 4096u, v0, v0, v0, v0, lds0 + 65536);
            }
            WAITV(0); lds_post(fm, 10); READ_TAG();
        }
        PRIO(1);
        if (!early) {
            if (isG) { WAITV(0); flag_store(flags, (u32)(FL_H + (mt * 2 + (wid & 1)) * 32 + ft * 2 + (wid >> 1)) * 128u, tag1); }
            lds_wait(fw, 12);
            w2load();
            W2PIN();
            if (!isG) lds_wait(fl, 13);
            WAITL0();
        }
        TS(22);
        w2_dma(IC<V_MTHR>(), kq, 2);
        if (early) { WAITV(14); flag_store(flags, (u32)(FL_H + (mt * 2 + (wid & 1)) * 32 + ft * 2 + (wid >> 1)) * 128u, tag1); }
        TS(24);
        w2_dma(IC<V_MTHR>(), kq, 3);
        TS(18);
        poll_flags(flags, FL_H + tt * 32 + 7 * ((kq + qo) & 3), 1, 7, tag1, lane);
        TS(25);
        const __amdgpu_buffer_rsrc_t hr = __builtin_amdgcn_make_buffer_rsrc(p.ws + WS_H + (size_t)(tt * 2 * 112 + ((kq + qo) & 3) * 28) * 1024, 0, 2 * 112 * 1024, 0x00020000);
        constexpr int WIN = V_WIN;
        u32x4 hf[WIN][2];
        f32x16 acc[2][2] = {};
        auto ldh = [&](int s, int j) {
            return __builtin_amdgcn_raw_buffer_load_b128(hr, lane * 16 + j * 112 * 1024 + ((s % 7) & 3) * 1024, ((s / 7 + rot) & 3) * 7168 + ((s % 7) >> 2) * 4096, 16);
        };
#pragma unroll
        for (int s = 0; s < WIN; s++) { hf[s][0] = ldh(s, 0); SCHED(); hf[s][1] = ldh(s, 1); SCHED(); }
        TS(26);
#pragma unroll
        for (int s = 0; s < 28; s++) {
            if (s == 12) { TS(27); }
            const u32x4 w0 = wreg[s % 14][0], w1 = wreg[s % 14][1];
            acc[0][0] = MFMA32(w0, hf[s % WIN][0], acc[0][0]); SCHED();
            acc[1][0] = MFMA32(w1, hf[s % WIN][0], acc[1][0]);
            if (s + WIN < 28) hf[s % WIN][0] = ldh(s + WIN, 0);
            SCHED();
            acc[0][1] = MFMA32(w0, hf[s % WIN][1], acc[0][1]);
            if (s >= 12 && s < 26) wreg[s - 12][0] = w2frag(s - 12, 0);
            SCHED();
            acc[1][1] = MFMA32(w1, hf[s % WIN][1], acc[1][1]);
            if (s >= 12 && s < 26) wreg[s - 12][1] = w2frag(s - 12, 1);
            if (s + WIN < 28) hf[s % WIN][1] = ldh(s + WIN, 1);
            if (s + WIN == 28) { LANEVALS(); xe = *(const u32x4*)((const char*)p.x + xo); b2e = *(const u32x4*)((const char*)p.b2 + dt * 128 + (tid & 7) * 16); }
            SCHED();
        }
        TS(29);
        int t2 = threadIdx.x; asm volatile("" : "+v"(t2));
        LAS char* pw = w2a + (t2 & 31) * 272 + ((t2 >> 5) & 1) * 16;
#pragma unroll
        for (int j2 = 0; j2 < 2; j2++)
#pragma unroll
            for (int j = 0; j < 2; j++)
#pragma unroll
                for (int rg = 0; rg < 4; rg++) {
                    f32x4 v = {acc[j2][j][4 * rg], acc[j2][j][4 * rg + 1], acc[j2][j][4 * rg + 2], acc[j2][j][4 * rg + 3]};
                    *(LAS f32x4*)(pw + 32 * j * 272 + (32 * j2 + 8 * rg) * 4) = v;
                }
        TS(32);
    } else {
        LANEVALS();
        const int a = lane & 15, q = lane >> 4;
        const int cl = hw & 1, rh = hw >> 1, chn = ch0 + 8 * cl;
        if (wid >= 6) {
            const int kc = wid & 1;
            PRIO(V_KPRIO);
            u32 junk = 0;
            if (kc == 0) {
                u32 are, aim, bre, bim, cre, cim, ldt, dsk;
                LOAD_PARAMS((u32)((ch0 + 8) * 128 + 64 + lane) * 2u, (u32)(ch0 + 8) * 2u);
                BAR();
                if (V_TLB) {
                    asm volatile("global_load_dword %0, %1, %2" : "+v"(junk) : "v"((u32)((lane & 15) < 10 ? (lane & 15) : 0) * 0x200000u + 0x1fff80u), "s"(p.ws) : "memory");
                    asm volatile("global_load_dword %0, %1, %2" : "+v"(junk) : "v"(xo), "s"(p.out) : "memory");
                    asm volatile("global_load_dword %0, %1, %2" : "+v"(junk) : "v"(0), "s"(p.b2) : "memory");
                    asm volatile("global_load_dword %0, %1, %2" : "+v"(junk) : "v"((u32)(lane & 1) * 0x1bff80u), "s"(p.w2) : "memory");
                }
                if (V_MALL & 1) asm volatile("global_load_dword %0, %1, %2" : "+v"(junk) : "v"((u32)(lane < 56 ? lane : 0) * 128u), "s"(uni((const char*)p.w1 + (size_t)b * 7168)) : "memory");
                if (V_MALL & 2) {
                    __builtin_amdgcn_s_sleep(V_MDELAY);
                    asm volatile("global_load_dword %0, %1, %2" : "+v"(junk) : "v"((u32)(lane < 56 ? lane : 0) * 128u), "s"(uni((const char*)p.w2 + (size_t)b * 7168)) : "memory");
                }
                if (V_TLB) WAIT_PARAMS(5); else WAIT_PARAMS(1);
                TS(41);
                build_table(sm, 3, lane, are, aim, bre, bim, cre, cim, ldt, dsk);
                lds_post(sm + F_P + 8, 1);
                READ_TAG();
                TS(42);
            } else {
                PRIO(3);
                u32x2 tge;
                u32x4 xv[8];
                asm volatile("global_load_dwordx4 %0, %8, %10\n\tglobal_load_dwordx4 %1, %8, %10 offset:1024\n\tglobal_load_dwordx4 %2, %8, %10 offset:2048\n\tglobal_load_dwordx4 %3, %8, %10 offset:3072\n\t"
                             "global_load_dwordx4 %4, %9, %10\n\tglobal_load_dwordx4 %5, %9, %10 offset:1024\n\tglobal_load_dwordx4 %6, %9, %10 offset:2048\n\tglobal_load_dwordx4 %7, %9, %10 offset:3072"
                             : "=&v"(xv[0]), "=&v"(xv[1]), "=&v"(xv[2]), "=&v"(xv[3]), "=&v"(xv[4]), "=&v"(xv[5]), "=&v"(xv[6]), "=&v"(xv[7])
                             : "v"(lane * 16), "v"(lane * 16 + 4096), "s"((const char*)p.x + (size_t)tb8 * 8192) : "memory");
                asm volatile("global_load_dwordx2 %0, %1, %2 sc1" : "=&v"(tge) : "v"(FL_E * 128), "s"(flags) : "memory");
                TS(40);
                BAR();
                asm volatile("s_waitcnt vmcnt(0)" : "+v"(tge), "+v"(xv[0]), "+v"(xv[1]), "+v"(xv[2]), "+v"(xv[3]), "+v"(xv[4]), "+v"(xv[5]), "+v"(xv[6]), "+v"(xv[7]) :: "memory");
                tag1.x = __builtin_amdgcn_readfirstlane(tge.x) + 1u; tag1.y = __builtin_amdgcn_readfirstlane(tge.y);
                TS(41);
                *(volatile LAS u32x2*)(sm + L_TAG + 8) = tag1;
                lds_post(sm + L_TAG + 4, 1);
                float s1[8], s2[8];
#pragma unroll
                for (int i = 0; i < 8; i++) { s1[i] = 0.f; s2[i] = 0.f; }
#pragma unroll
                for (int j = 0; j < 4; j++)
#pragma unroll
                    for (int i = 0; i < 8; i++) { s1[i] = dot2(xv[i][j], 0x3f803f80u, s1[i]); s2[i] = dot2(xv[i][j], xv[i][j], s2[i]); }
                auto red8 = [&](float (&v)[8]) {
                    float a[4], c[2];
#pragma unroll
                    for (int i = 0; i < 4; i++) {
                        const auto t = __builtin_amdgcn_permlane32_swap(__float_as_uint(v[i]), __float_as_uint(v[i + 4]), false, false);
                        a[i] = __uint_as_float(t[0]) + __uint_as_float(t[1]);
                    }
#pragma unroll
                    for (int i = 0; i < 2; i++) {
                        const auto t = __builtin_amdgcn_permlane16_swap(__float_as_uint(a[i]), __float_as_uint(a[i + 2]), false, false);
                        c[i] = dpp_add<0x128, 0xf>(__uint_as_float(t[0]) + __uint_as_float(t[1]));
                    }
                    float e = (lane & 8) ? c[1] : c[0];
                    e = dpp_add<0x141, 0xf>(e); e = dpp_add<0xB1, 0xf>(e); e = dpp_add<0x4E, 0xf>(e);
                    return e;
                };
                const float a1 = red8(s1), a2 = red8(s2);
                const float mean = a1 * (1.f / 512.f);
                const float v1 = fmaxf(a2 * (1.f / 512.f) - mean * mean, 0.f);
                const float q1 = v1 + 1e-5f;
                const float rs = __builtin_amdgcn_rsqf(q1);
                const float q2 = v1 * rs * rs + 1e-5f;
                {
                    u32x4 o = {__float_as_uint(-mean), tag1.x, __float_as_uint(__builtin_amdgcn_sqrtf(q1 * q2)), tag1.x};
                    u64 keep;
                    asm volatile("s_mov_b64 %0, exec\n\ts_mov_b64 exec, %4\n\tglobal_store_dwordx4 %1, %2, %3 sc1\n\ts_mov_b64 exec, %0\n\ts_nop 1"
                                 : "=&s"(keep) : "v"(GR_ST + (u32)(tb8 * 8 + (lane >> 3)) * 16u), "v"(o), "s"(flags), "s"(0x0101010101010101ull) : "memory");
                }
                TS(42);
                f32x2 sc[4], of[4];
                const float nmr = -mean * rs;
#pragma unroll
                for (int i = 0; i < 4; i++) {
                    sc[i] = (f32x2){__builtin_bit_cast(float, __builtin_amdgcn_readlane(__builtin_bit_cast(int, rs), 16 * i)), __builtin_bit_cast(float, __builtin_amdgcn_readlane(__builtin_bit_cast(int, rs), 16 * i + 8))};
                    of[i] = (f32x2){__builtin_bit_cast(float, __builtin_amdgcn_readlane(__builtin_bit_cast(int, nmr), 16 * i)), __builtin_bit_cast(float, __builtin_amdgcn_readlane(__builtin_bit_cast(int, nmr), 16 * i + 8))};
                }
#pragma unroll
                for (int c = 0; c < 8; c++) {
                    u32x4 o;
#pragma unroll
                    for (int i = 0; i < 4; i++) {
                        const u32 a = xv[2 * i][c >> 1], d = xv[2 * i + 1][c >> 1];
                        const f32x2 v = (c & 1) ? (f32x2){bfhi(a), bfhi(d)} : (f32x2){bflo(a), bflo(d)};
                        const f32x2 n = __builtin_elementwise_fma(v, sc[i], of[i]);
                        o[i] = pkh(n.x, n.y);
                    }
                    astore_b128_sc1(p.ws + WS_U + (size_t)tb8 * 8192, (u32)(c * 1024 + lane * 16), o);
                }
                PRIO(V_KPRIO);
                TS(45);
            }
            const int rS = (int)((0x8080840876543210ull >> (4 * a)) & 15), rB = 17 + (int)((0xfa540000u >> (2 * a)) & 3);
            const int rT1 = (a & 3) ? 7 + (a & 3) : 0, rT2 = (a >> 2) ? 10 + (a >> 2) : 0;
            LAS char* tb = sm + L_TAB + kc * TAB_SZ + q * 16;
            LAS char *pS = tb + rS * TAB_ROW, *pB = tb + rB * TAB_ROW, *pT1 = tb + rT1 * TAB_ROW, *pT2 = tb + rT2 * TAB_ROW, *pZ = tb + 14 * TAB_ROW;
            const int hd = tab_off(kc + 2) - tab_off(kc);
            f32x4 G[2][4] = {};
            u32x4 T[2][7];
            auto ldtab = [&](int k) {
                if (k == 4) { pS += hd; pB += hd; pT1 += hd; pT2 += hd; pZ += hd; }
                u32x4* t = T[k & 1];
                const int o = (k & 3) * 64;
                t[0] = *(LAS u32x4*)(pS + o); t[1] = *(LAS u32x4*)(pB + o); t[2] = *(LAS u32x4*)(pT1 + o); t[3] = *(LAS u32x4*)(pT2 + o);
                t[4] = *(LAS u32x4*)(pZ + o); t[5] = *(LAS u32x4*)(pZ + o + TAB_ROW); t[6] = *(LAS u32x4*)(pZ + o + 2 * TAB_ROW);
            };
            int nb = isG ? 0 : 6; bool go = false;
#define SYNCPT() do { if (nb < 6) { if (go) { RBAR(); nb++; } else go = all_ge(*(volatile LAS u32*)(sm + F_L + (lane & 1) * 4), 2); } } while (0)
            lds_wait(sm + F_M + (lane & 3) * 4, 1);
            TS(3);
            ldtab(0);

#pragma unroll
            for (int k = 0; k < 8; k++) {
                u32x4* t = T[k & 1];
                f32x4* Gh = G[k >> 2];
                u32x4 A, B0, B1, B2, B3;
                asm volatile("" :: "v"(t[0]), "v"(t[1]), "v"(t[2]), "v"(t[3]), "v"(t[4]), "v"(t[5]), "v"(t[6]));
                if (k == 3) lds_wait(sm + F_P + 8, 1);
                TF(4 + 2 * k);
                if (k < 7) ldtab(k + 1);
                TF(5 + 2 * k);
                SCHED();
#pragma unroll
                for (int j = 0; j < 4; j++) { B0[j] = cmh2<false>(t[2][j], t[3][j], cmh1<false>(t[2][j], t[3][j])); A[j] = cmh2<true>(t[0][j], t[1][j], cmh1<true>(t[0][j], t[1][j])); }
                SCHED();
#pragma unroll
                for (int j = 0; j < 4; j++) B1[j] = cmh2<false>(B0[j], t[4][j], cmh1<false>(B0[j], t[4][j]));
                SCHED();
                Gh[0] = MFMA16H(A, B0, Gh[0]);
#pragma unroll
                for (int j = 0; j < 4; j++) B2[j] = cmh2<false>(B0[j], t[5][j], cmh1<false>(B0[j], t[5][j]));
                SCHED();
                Gh[1] = MFMA16H(A, B1, Gh[1]);
#pragma unroll
                for (int j = 0; j < 4; j++) B3[j] = cmh2<false>(B0[j], t[6][j], cmh1<false>(B0[j], t[6][j]));
                SCHED();
                Gh[2] = MFMA16H(A, B2, Gh[2]);
                asm volatile("s_nop 1" : "+v"(B3));
                Gh[3] = MFMA16H(A, B3, Gh[3]);
                SCHED();
                if (V_S1) SYNCPT();
                if (k == V_LNK && kc) { WAITV(0); flag_store(flags, (u32)(FL_LN * 128 + tb8 * 8), tag1); if (!isG) lds_post(sm + F_P + 12, 1); TS(47); }
                if (!V_S1) SYNCPT();
            }
            TS(20);
            {
                const float M0 = *(LAS float*)(sm + L_MISC + kc * 4), M1 = *(LAS float*)(sm + L_MISC + 8 + kc * 4), M = fmaxf(M0, M1);
                const float s0 = __builtin_amdgcn_exp2f(M0 - M), s1 = __builtin_amdgcn_exp2f(M1 - M);
                const float dx = *(LAS float*)(sm + L_MISC + 16 + kc * 4) * __builtin_amdgcn_exp2f(-M), d0 = lane == 0 ? dx : 0.f;
                LAS char* gx = sm + L_TAB + kc * TAB_SZ;
#pragma unroll
                for (int ct = 0; ct < 4; ct++) {
                    float v[4];
#pragma unroll
                    for (int e = 0; e < 4; e++) v[e] = opq(fmaf(G[1][ct][e], s1, (ct | e) ? opq(G[0][ct][e] * s0) : fmaf(G[0][ct][e], s0, d0)));
                    u32x2 o = {pkh(v[0], v[1]), pkh(v[2], v[3])};
                    *(LAS u32x2*)(gx + (16 * ct + a) * 32 + q * 8) = o;
                }
                SYNCPT();
                WAITL0();
                const u32x4 u = *(LAS u32x4*)(gx + lane * 32), v = *(LAS u32x4*)(gx + lane * 32 + 16);
                LAS char* kp = sm + L_KP + kc * KP_CH + (lane & 1) * KP_PLANE + (16 + (lane >> 1)) * 16;
                const u32 LL = 0x01000100u, HH = 0x03020302u, SW = 0x01000302u;
                const u32 sa = h ? 0x05040100u : 0x05040706u, sb = h ? LL : SW, tie = (lane & 1) ? HH : SW;
                u32x4 w0 = {perm(u.w, v.x, sa), perm(0, u.z, sb), perm(u.y, u.z, sa), perm(0, u.x, sb)};
                u32x4 w1 = {perm(0, v.y, HH), perm(0, v.y, LL), perm(v.x, v.y, 0x07060100u), perm(0, v.x, HH)};
                u32x4 w2 = {perm(0, v.z, HH), perm(0, v.z, tie), perm(0, v.z, LL), perm(0, v.z, LL)};
                u32x4 w3 = {perm(0, v.w, HH), perm(0, v.w, tie), perm(0, v.w, LL), perm(0, v.w, LL)};
                *(LAS u32x4*)kp = w0; *(LAS u32x4*)(kp + 512) = w1; *(LAS u32x4*)(kp + 1024) = w2; *(LAS u32x4*)(kp + 1536) = w3;
                u32x4 z = {0, 0, 0, 0};
                *(LAS u32x4*)(sm + L_KP + kc * KP_CH + (q & 1) * KP_PLANE + a * 16) = z;
            }
            lds_post(sm + F_K + kc * 4, 1);
            TS(21);
            while (nb < 6) { RBAR(); nb++; }
            lds_wait(sm + F_M + (lane & 3) * 4, 9);
            if (!isG) lds_wait(sm + F_L + (lane & 1) * 4, 2);
            w2_dma(IC<V_PACE>(), 2, kc);
            w2_dma(IC<V_PACE>(), 3, kc);
            TS(24);
            asm volatile("s_waitcnt vmcnt(0)" : "+v"(junk) :: "memory");
            lds_post(sm + F_K + kc * 4, 12);
            TS(25);
        } else {
            PRIO(V_LPRIO);
            LOADER_VALS();
            LAS char* fm = sm + F_M + (lane & 3) * 4;
            LAS char* fl = sm + F_L + hw * 4;
            u32 junk = 0;
            TS(40);
            if (__builtin_expect(isG, 1)) { stage(0); RBAR(); }
            const int tb0 = hw * 128 + (lane >> 1);
            u32 ob = (u32)((r >> 2) * 1024 + (8 * g + 2 * (r & 3) + (lane & 1)) * 16), o0 = ob + (u32)tb0 * 8192u;
            u32x4 uv[4], f0, f1; u32x2 pv;
#define LOAD_LNF() asm volatile("global_load_dwordx4 %0, %2, %3 sc1\n\tglobal_load_dwordx4 %1, %2, %3 offset:1024 sc1" : "=&v"(f0), "=&v"(f1) : "v"(lfo), "s"(flags) : "memory")
#define WAIT_LNF() asm volatile("s_waitcnt vmcnt(0)" : "+v"(f0), "+v"(f1), "+v"(junk) :: "memory")
#define LNF_OK() (__builtin_amdgcn_ballot_w64((((f0.x ^ tag1.x) | (f0.z ^ tag1.x) | (f1.x ^ tag1.x) | (f1.z ^ tag1.x)) | ((f0.y ^ tag1.y) | (f0.w ^ tag1.y) | (f1.y ^ tag1.y) | (f1.w ^ tag1.y))) != 0u) == 0)
#define LOAD_U(B, M) do { _Pragma("unroll") for (int i = 0; i < 4; i++) uv[i] = aload_b128_sc1(B, (o0 + i * 32 * 8192) & (M)); pv = aload_b64_sc1(B, (ob + 127u * 8192u + 8u) & (M)); } while (0)
            u32 lfo = (u32)(FL_LN * 128 + lane * 16), vw2 = voffw2;
            asm volatile("" : "+v"(ob), "+v"(o0), "+v"(lfo), "+v"(vw2));
            if (__builtin_expect(isG, 1)) {
                const char* nsrc = chunk_src(1);
#pragma unroll 1
                for (int kc = 1; kc < 8; kc++) {
                    u32 dst = lds0 + (kc & 3) * 32768 + hw * 16384;
                    asm volatile("" : "+s"(nsrc), "+s"(dst));
                    if (kc >= 4) RBAR();
                    TL(kc + 6);
                    glds_run<4, false, V_PACE>(nsrc, lgs, vo0, vo1, vo2, vo3, dst);
                    nsrc = chunk_src(kc + 1);
                    WAITV(16);
                    if (kc < 3) *(volatile LAS u32*)fl = kc;
                    if (kc == 3) READ_TAG();
                }
                RBAR();
                TL(14);
            } else {
                BAR();
                const char* wb = (const char*)p.w1 + (lj < 28 ? lj : 0) * 128 + (size_t)hw * 256 * 3584;
                const u32 v0 = (u32)((lane >> 3) * 3584) + vsw;
                glds_run<8, false, V_PACE>(wb, 32u * 3584u, v0, v0 + 27648u, v0 + 55296u, v0 + 82944u, lds0 + hw * 32768);
                f32x16 c0 = {}, c1 = {};
                const u32x4 ones = {0x3f803f80u, 0x3f803f80u, 0x3f803f80u, 0x3f803f80u};
#pragma unroll 1
                for (int s0 = 0; s0 < 16; s0 += 8) {
                    u32x4 fa[8], fb[8];
                    if (s0 == 0) WAITV(16); else WAITV(0);
#pragma unroll
                    for (int s = 0; s < 8; s++) {
                        LAS char* a = sm + hw * 32768 + (s0 + s) * 2048;
                        fa[s] = trfrag(a + tra0, a + tra0 + 1024); fb[s] = trfrag(a + (tra0 ^ 64), a + (tra0 ^ 64) + 1024);
                    }
                    SCHED();
#pragma unroll
                    for (int s = 0; s < 8; s++) { c0 = MFMA32(ones, fa[s], c0); c1 = MFMA32(ones, fb[s], c1); }
                }
                const float c = h ? c1[0] : c0[0];
                *(LAS float*)(sm + L_MISC + 512 + hw * 256 + lane * 4) = c;
                lds_post(fl, 1);
                READ_TAG();
                lds_wait(sm + F_L + (lane & 1) * 4, 1);
                u32x2 o = {__float_as_uint(*(LAS float*)(sm + L_MISC + 512 + lane * 4) + *(LAS float*)(sm + L_MISC + 768 + lane * 4)), tag1.x};
                if (hw == 0 && lj < 28) astore_b64_sc1(flags, GR_CS + (u32)(lj * 64 + lane) * 8u, o);
                lds_post(fl, 2); lds_wait(fm, 10);
                __builtin_amdgcn_s_sleep(V_SPD);
            }
            LOAD_LNF();
            w2_dma(IC<V_PACE>(), 0, hw);
            WAITV(16);
            if (isG) RBAR();
            TL(15);
            asm volatile("s_waitcnt vmcnt(14)" : "+v"(f0), "+v"(f1) :: "memory");
            const bool uok = LNF_OK();
            { const char* ub = uok ? (const char*)p.ws + WS_U : (const char*)p.b2; LOAD_U(ub, uok ? ~0u : 0u); }
            w2_dma(IC<V_PACE>(), 1, hw);
            TS(24);
            asm volatile("s_waitcnt vmcnt(0)" : "+v"(uv[0]), "+v"(uv[1]), "+v"(uv[2]), "+v"(uv[3]), "+v"(pv) :: "memory");
            lds_post(fl, 12);
            TS(25);
            if (!uok) {
#pragma unroll 1
                for (int spin = 0; spin < (1 << 16); ++spin) {
                    LOAD_LNF(); WAIT_LNF();
                    if (LNF_OK()) break;
                    __builtin_amdgcn_s_sleep(V_PSLEEP);
                }
                TS(22);
                LOAD_U(p.ws + WS_U, ~0u);
                asm volatile("s_waitcnt vmcnt(0)" : "+v"(uv[0]), "+v"(uv[1]), "+v"(uv[2]), "+v"(uv[3]), "+v"(pv) :: "memory");
            }
            if (b == 0 && hw == 0) flag_store(flags, FL_E * 128, tag1);
            PRIO(V_UPRIO);
            lds_wait(fm, 9);
            lds_wait(sm + F_K + (lane & 1) * 4, 1);
            TS(26);
            u32 r2[5], r3[5];
            r2[0] = hw ? pv.x : 0u; r3[0] = hw ? pv.y : 0u;
#pragma unroll
            for (int i = 0; i < 4; i++) {
                r2[i + 1] = __builtin_amdgcn_ds_bpermute(((lane - 2) & 63) * 4, uv[i].z);
                r3[i + 1] = __builtin_amdgcn_ds_bpermute(((lane - 2) & 63) * 4, uv[i].w);
            }
            LAS char* d = sm + L_UC + (lane & 1) * UC_CH + (tb0 + 4) * 16;
#pragma unroll
            for (int i = 0; i < 4; i++) *(LAS u32x4*)(d + i * 512) = uv[i];
#pragma unroll
            for (int i = 0; i < 4; i++) {
                const u32x4 t = uv[i];
                const u32 q2 = lane < 2 ? r2[i] : r2[i + 1], q3 = lane < 2 ? r3[i] : r3[i + 1];
                const u32 a0 = __builtin_amdgcn_alignbit(q3, q2, 16), a1 = __builtin_amdgcn_alignbit(t.x, q3, 16), a2 = __builtin_amdgcn_alignbit(t.y, t.x, 16),
                          a3 = __builtin_amdgcn_alignbit(t.z, t.y, 16), a4 = __builtin_amdgcn_alignbit(t.w, t.z, 16);
                u32x4 c1 = {a1, a2, a3, a4}, c2 = {q3, t.x, t.y, t.z}, c3 = {a0, a1, a2, a3};
                *(LAS u32x4*)(d + i * 512 + UC_COPY) = c1;
                *(LAS u32x4*)(d + i * 512 + 2 * UC_COPY) = c2;
                *(LAS u32x4*)(d + i * 512 + 3 * UC_COPY) = c3;
            }
            u32x4 z = {0, 0, 0, 0};
            *(LAS u32x4*)(sm + L_UC + hw * UC_CH + ((lane >> 2) & 3) * UC_COPY + (lane & 3) * 16) = z;
            lds_post(fl, 13);
            TS(35);
        }
        lds_wait(sm + F_L + (lane & 1) * 4, 13);
        TS(36);
        LAS char* ub = sm + L_UC + cl * UC_CH + (3 - (a & 3)) * UC_COPY + (32 * rh + 2 * q + 5 + (a >> 2)) * 8;
        LAS char* kb = sm + L_KP + cl * KP_CH + ((q + 1) & 1) * KP_PLANE + (16 - 8 * rh - 6 + a - (q >> 1)) * 16;
        f32x4 y[8] = {};
        auto ldA = [&](int dl, int i) {
            const u32x2 lo = *(volatile LAS u32x2*)(ub + dl * 64 + i * 512), hi = *(volatile LAS u32x2*)(ub + dl * 64 + i * 512 + 8);
            return (u32x4){lo.x, lo.y, hi.x, hi.y};
        };
        auto ldB = [&](int dl, int i) { return *(LAS u32x4*)(kb + (3 - dl) * 32 + i * 256); };
        u32x4 A0[8], B0[8], A1[8], B1[8];
#pragma unroll
        for (int i = 0; i < 8; i++) { B0[i] = ldB(0, i); A0[i] = ldA(0, i); }
        lds_wait(sm + F_M + (lane & 3) * 4, 10);
        PRIO(V_CPRIO);
        TS(27);
        auto cstep = [&](u32x4 (&A)[8], u32x4 (&B)[8], u32x4 (&An)[8], u32x4 (&Bn)[8], int dn) {
            asm volatile("" :: "v"(A[0]), "v"(A[1]), "v"(A[2]), "v"(A[3]), "v"(A[4]), "v"(A[5]), "v"(A[6]), "v"(A[7]));
            asm volatile("" :: "v"(B[0]), "v"(B[1]), "v"(B[2]), "v"(B[3]), "v"(B[4]), "v"(B[5]), "v"(B[6]), "v"(B[7]));
            SCHED();
            int n = 0;
#pragma unroll
            for (int gm = 0; gm < 8; gm++)
#pragma unroll
                for (int c = gm; c < 8; c++) {
                    y[c] = MFMA16H(A[gm], B[c - gm], y[c]);
                    if (dn < 4 && n < 24) { if (n % 3 == 0) Bn[n / 3] = ldB(dn, n / 3); else if (n % 3 == 1) An[n / 3] = ldA(dn, n / 3); }
                    n++;
                    SCHED();
                }
        };
        cstep(A0, B0, A1, B1, 1); cstep(A1, B1, A0, B0, 2); cstep(A0, B0, A1, B1, 3); cstep(A1, B1, A0, B0, 4);
        PRIO(3);
        TS(28);
        const float ksc = __builtin_amdgcn_exp2f(fmaxf(*(LAS float*)(sm + L_MISC + cl * 4), *(LAS float*)(sm + L_MISC + 8 + cl * 4)));
        LAS char* fh = sm + F_H + hw * 4;
        LAS char* fp = sm + F_H + (hw ^ 2) * 4;
        lds_post(fh, 1); lds_wait(fp, 1);
        TS(37);
        LAS char* xb = sm + L_UC + cl * UC_CH + lane * 16;
        if (rh) {
#pragma unroll
            for (int c = 0; c < 4; c++) *(LAS f32x4*)(xb + 4096 + c * 1024) = y[c];
            lds_post(fh, 2);
#pragma unroll
            for (int c = 0; c < 4; c++)
#pragma unroll
                for (int e = 0; e < 4; e++) y[c][e] = opq(y[4 + c][e] * ksc);
        } else {
#pragma unroll
            for (int c = 0; c < 4; c++) *(LAS f32x4*)(xb + c * 1024) = y[4 + c];
            lds_post(fh, 2);
#pragma unroll
            for (int c = 0; c < 4; c++)
#pragma unroll
                for (int e = 0; e < 4; e++) y[c][e] = opq(y[c][e] * ksc);
        }
        char* sdst = p.ws + WS_S + (size_t)chn * 4096 + rh * 2048;
        TS(38);
        lds_wait(fp, 2);
        TS(39);
        {
            u32x2 o[4];
#pragma unroll
            for (int c = 0; c < 4; c++) {
                const f32x4 v = *(LAS f32x4*)(xb + (rh ^ 1) * 4096 + c * 1024);
                float f[4];
#pragma unroll
                for (int e = 0; e < 4; e++) f[e] = opq(fmaf(v[e], ksc, y[c][e]));
                o[c] = (u32x2){pkbf(f[0], f[1]), pkbf(f[2], f[3])};
            }
#pragma unroll
            for (int cp = 0; cp < 2; cp++) {
                const auto lo = __builtin_amdgcn_permlane16_swap(o[2 * cp].x, o[2 * cp + 1].x, false, false);
                const auto hi = __builtin_amdgcn_permlane16_swap(o[2 * cp].y, o[2 * cp + 1].y, false, false);
                astore_b128_sc1(sdst, (u32)(cp * 1024 + lane * 16), (u32x4){lo[0], hi[0], lo[1], hi[1]});
            }
        }
        TS(29);
        WAITV(0);
        lds_post(fh, 3);
        lds_wait(sm + F_H + (lane & 3) * 4, 3);
        if (wid == 4) flag_store(flags, (u32)(FL_S + b) * 128u, tag1);
        PRIO(0);
        TS(30);
        xe = *(const u32x4*)((const char*)p.x + xo); b2e = *(const u32x4*)((const char*)p.b2 + dt * 128 + (tid & 7) * 16);
        __builtin_amdgcn_s_sleep(V_SDELAY);
        poll_flags(flags, FL_S + dt + 8 * hw, 32, 8, tag1, lane);
        TS(31);
        u32x4 sv[2];
#pragma unroll
        for (int i = 0; i < 2; i++) {
            const int d = hw * 16 + i * 8 + (lane >> 3);
            sv[i] = aload_b128_sc1(p.ws + WS_S, (u32)((dt * 64 + d) * 4096 + (tt >> 3) * 1024 + ((tt >> 2) & 1) * 256 + ((lane >> 2) & 1) * 512 + (tt & 3) * 64 + (lane & 3) * 16));
        }
        asm volatile("s_waitcnt vmcnt(0)" : "+v"(sv[0]), "+v"(sv[1]) :: "memory");
#pragma unroll
        for (int i = 0; i < 2; i++) {
            const int d = hw * 16 + i * 8 + (lane >> 3);
            *(LAS u32x4*)(sm + L_UC + d * 128 + (((2 * (lane & 3) + ((lane >> 2) & 1)) ^ (d >> 3)) << 4)) = sv[i];
        }
        TS(32);
    }
    {
        LANEVALS();
        const int tok = tid >> 3, dg = tid & 7;
        float m[8], xf[8];
#pragma unroll
        for (int e = 0; e < 4; e++) { m[2 * e] = bflo(b2e[e]); m[2 * e + 1] = bfhi(b2e[e]); xf[2 * e] = bflo(xe[e]); xf[2 * e + 1] = bfhi(xe[e]); }
        u32 pa = (u32)(L_W2 + tok * 272 + dg * 32), pb = pa + 2 * W2_REG, sa = (u32)(L_UC + dg * 1024 + (((tok >> 3) ^ dg) << 4) + (tok & 7) * 2), oo = xo;
        asm volatile("" : "+v"(m[0]), "+v"(m[1]), "+v"(m[2]), "+v"(m[3]), "+v"(m[4]), "+v"(m[5]), "+v"(m[6]), "+v"(m[7]), "+v"(pa), "+v"(pb), "+v"(sa), "+v"(oo));
        asm volatile("" : "+v"(xf[0]), "+v"(xf[1]), "+v"(xf[2]), "+v"(xf[3]), "+v"(xf[4]), "+v"(xf[5]), "+v"(xf[6]), "+v"(xf[7]));
        BAR();
        TS(33);
#pragma unroll
        for (int i = 0; i < 4; i++) {
            LAS char* q = sm + (i < 2 ? pa : pb) + (i & 1) * W2_REG;
            const f32x4 lo = *(LAS f32x4*)q, hi = *(LAS f32x4*)(q + 16);
#pragma unroll
            for (int e = 0; e < 4; e++) { m[e] += lo[e]; m[4 + e] += hi[e]; }
        }
        float o[8];
#pragma unroll
        for (int e = 0; e < 8; e++) {
            const float s = bf2f(*(LAS u16*)(sm + sa + e * 128));
            o[e] = fmaf(s * m[e], __builtin_amdgcn_rcpf(1.f + __builtin_amdgcn_exp2f(-1.4426950408889634f * m[e])), xf[e]);
        }
        u32x4 ov = {pkbf(o[0], o[1]), pkbf(o[2], o[3]), pkbf(o[4], o[5]), pkbf(o[6], o[7])};
        if (V_OST == 0) *(u32x4*)((char*)p.out + oo) = ov;
        else if (V_OST == 1) astore_b128_sc1(p.out, oo, ov);
        else __builtin_nontemporal_store(ov, (u32x4*)((char*)p.out + oo));
    }
    TS(34); TR(63);
    TSOUT();
}

extern "C" void kernel_launch(void* const* d_in, const int* in_sizes, int n_in, void* d_out, int out_size, void* d_ws, size_t ws_size,
                              hipStream_t stream) {
    Params p;
    p.x = (const u16*)d_in[0]; p.Are = (const u16*)d_in[1]; p.Aim = (const u16*)d_in[2]; p.Bre = (const u16*)d_in[3]; p.Bim = (const u16*)d_in[4];
    p.Cre = (const u16*)d_in[5]; p.Cim = (const u16*)d_in[6]; p.logdt = (const u16*)d_in[7]; p.Dskip = (const u16*)d_in[8];
    p.w1 = (const u16*)d_in[10]; p.b1 = (const u16*)d_in[11]; p.w2 = (const u16*)d_in[12]; p.b2 = (const u16*)d_in[13];
    p.out = (u16*)d_out; p.ws = (char*)d_ws;
    fused_kernel<<<256, 512, 0, stream>>>(p);
}
```

```cpp
#define V_TLB 0
#define V_KROT 0
#define V_MALL 5
#include <hip/hip_runtime.h>
#include <stdint.h>
template <int N> struct IC { static constexpr int value = N; };
#define V_W2ROT 0
#define V_TOUCH 0
#define V_CODE 1
#define V_MPRIO 0
#define V_KPRIO 1
#define V_PSLEEP 6
#define V_SSTEP 14
#define V_UAT 6
#define V_FSP 8
#define V_SPD 16
#define V_MDELAY 16
#define V_LDELAY 2
#define V_LNK 1
#define V_LPRIO 0
#define V_PACE 16
#define V_NOP 0
#define V_MTHR 10
#define V_WIN 6
#define V_SDELAY 16
#define V_UB 5
#define V_CPRIO 2
#define V_EARLY 1
#define V_HROT 0
#define V_UPRIO 3
#define V_LEAD 0
#define V_S0 0
#define V_S1 0
#define V_OST 1
#define V_MAP 0
#define V_XP 0
#define CODE_LINES 252
#define V_SAFE 0

typedef unsigned short u16;
typedef unsigned u32;
typedef unsigned long long u64;
typedef u32 u32x2 __attribute__((ext_vector_type(2)));
typedef u32 u32x4 __attribute__((ext_vector_type(4)));
typedef short s16x4 __attribute__((ext_vector_type(4)));
typedef __bf16 bf16x2 __attribute__((ext_vector_type(2)));
typedef __bf16 bf16x8 __attribute__((ext_vector_type(8)));
typedef _Float16 f16x2 __attribute__((ext_vector_type(2)));
typedef _Float16 f16x4 __attribute__((ext_vector_type(4)));
typedef _Float16 f16x8 __attribute__((ext_vector_type(8)));
typedef float f32x2 __attribute__((ext_vector_type(2)));
typedef float f32x4 __attribute__((ext_vector_type(4)));
typedef float f32x16 __attribute__((ext_vector_type(16)));
#define LAS __attribute__((address_space(3)))
#define DEV __device__ __forceinline__

DEV int tab_off(int w);
constexpr size_t WS_H = 12u << 20;
constexpr size_t WS_U = 8u << 20;
constexpr size_t WS_S = 10u << 20;
constexpr size_t WS_FLAG = 0;
constexpr size_t WS_STAMP = 20u << 20;
constexpr int FL_RDY = 0, FL_LN = 1344, FL_S = 32, FL_E = 288, FL_H = 320;
constexpr u32 GR_ST = 256u << 10, GR_CS = 320u << 10;

constexpr int L_W2 = 0, W2_REG = 28672;
constexpr int L_UC = 114688, UC_COPY = 4160, UC_CH = 4 * UC_COPY + 64;
constexpr int L_TAB = 131072, TAB_ROW = 288, TAB_SZ = 6144;
constexpr int L_MISC = 149504;
constexpr int L_KP = 150784, KP_PLANE = 2304, KP_CH = 2 * KP_PLANE;
constexpr int F_M = L_MISC + 64, F_L = L_MISC + 80, F_K = L_MISC + 88, F_H = L_MISC + 96, C_CS = L_MISC + 112, F_D = L_MISC + 128, F_P = L_MISC + 160;
constexpr int L_TAG = L_MISC + 112, L_JUNK = L_MISC + 256;
DEV int tab_off(int w) { return w == 3 ? L_KP + KP_CH : L_TAB + w * TAB_SZ; }

struct Params {
    const u16 *x, *Are, *Aim, *Bre, *Bim, *Cre, *Cim, *logdt, *Dskip, *w1, *b1, *w2, *b2;
    u16* out;
    char* ws;
};

DEV float bf2f(u32 v) { return __uint_as_float(v << 16); }
DEV float bflo(u32 v) { return __uint_as_float(v << 16); }
DEV float bfhi(u32 v) { return __uint_as_float(v & 0xffff0000u); }
DEV u32 pkbf(float a, float b) { f32x2 f = {a, b}; return __builtin_bit_cast(u32, __builtin_convertvector(f, bf16x2)); }
DEV u32 pkh(float a, float b) { f32x2 f = {a, b}; return __builtin_bit_cast(u32, __builtin_convertvector(f, f16x2)); }
DEV float opq(float a) { asm("" : "+v"(a)); return a; }
DEV float rbf(float a) { return bflo(pkbf(a, a)); }
DEV float dot2(u32 a, u32 b, float c) { return __builtin_amdgcn_fdot2_f32_bf16(__builtin_bit_cast(bf16x2, a), __builtin_bit_cast(bf16x2, b), c, false); }

#define WAITV(n) asm volatile("s_waitcnt vmcnt(" #n ")" ::: "memory")
#define WAITL0() asm volatile("s_waitcnt lgkmcnt(0)" ::: "memory")
#define BAR() do { asm volatile("s_waitcnt lgkmcnt(0)" ::: "memory"); __builtin_amdgcn_s_barrier(); asm volatile("" ::: "memory"); } while (0)
#define RBAR() do { asm volatile("" ::: "memory"); __builtin_amdgcn_s_barrier(); asm volatile("" ::: "memory"); } while (0)
#define SCHED() __builtin_amdgcn_sched_barrier(0)
#define PRIO(n) __builtin_amdgcn_s_setprio(n)
#define MFMA32(a, b, c) __builtin_amdgcn_mfma_f32_32x32x16_bf16(__builtin_bit_cast(bf16x8, a), __builtin_bit_cast(bf16x8, b), c, 0, 0, 0)
#define MFMA16H(a, b, c) __builtin_amdgcn_mfma_f32_16x16x32_f16(__builtin_bit_cast(f16x8, a), __builtin_bit_cast(f16x8, b), c, 0, 0, 0)

DEV const void* uni(const void* q) {
    const u64 a = (u64)q;
    return (const void*)(((u64)(u32)__builtin_amdgcn_readfirstlane((u32)(a >> 32)) << 32) | (u32)__builtin_amdgcn_readfirstlane((u32)a));
}
#define GLP ".if %c[q]\n\ts_nop %c[q]\n\t.endif\n\t"
#define GL4(B) ".if %c[p]\n\ts_waitcnt vmcnt(%c[p])\n\t.endif\n\tglobal_load_lds_dwordx4 %[v0], " B "\n\t" GLP "global_load_lds_dwordx4 %[v1], " B " offset:1024\n\t" GLP \
               "global_load_lds_dwordx4 %[v2], " B " offset:2048\n\t" GLP "global_load_lds_dwordx4 %[v3], " B " offset:3072\n\t"
template <int NG, bool HALF, int PACE> DEV void glds_run(const void* base, u32 step, u32 v0, u32 v1, u32 v2, u32 v3, u32 dst) {
    u32 keep;
    dst = __builtin_amdgcn_readfirstlane(dst);
    base = uni(base);
    asm volatile("s_mov_b32 %[k], m0\n\ts_mov_b32 m0, %[d]\n\ts_mov_b64 vcc, %[b]\n\ts_nop 1\n\t" GL4("vcc")
                 ".rept %c[n]\n\ts_add_u32 m0, m0, 0x1000\n\ts_add_u32 vcc_lo, vcc_lo, %[s]\n\ts_addc_u32 vcc_hi, vcc_hi, 0\n\t" GL4("vcc") ".endr\n\t"
                 ".if %c[h]\n\ts_add_u32 m0, m0, 0x1000\n\ts_add_u32 vcc_lo, vcc_lo, %[s]\n\ts_addc_u32 vcc_hi, vcc_hi, 0\n\t"
                 "global_load_lds_dwordx4 %[v0], vcc\n\t" GLP "global_load_lds_dwordx4 %[v1], vcc offset:1024\n\t.endif\n\t"
                 "s_mov_b32 m0, %[k]"
                 : [k] "=&s"(keep) : [v0] "v"(v0), [v1] "v"(v1), [v2] "v"(v2), [v3] "v"(v3), [b] "s"(base), [d] "s"(dst), [n] "i"(NG - 1), [s] "s"(step), [h] "i"(HALF ? 1 : 0), [p] "i"(PACE), [q] "i"(V_NOP)
                 : "memory", "scc", "vcc");
}
DEV void touch(const void* base, u32 voff, u32 junk) {
    u32 keep;
    base = uni(base);
    asm volatile("s_mov_b32 %0, m0\n\ts_mov_b32 m0, %3\n\ts_nop 2\n\tglobal_load_lds_dword %1, %2\n\ts_mov_b32 m0, %0"
                 : "=&s"(keep) : "v"(voff), "s"(base), "s"(junk) : "memory");
}
DEV u32 aload_u16(const void* base, u32 voff) { u32 r; asm volatile("global_load_ushort %0, %1, %2" : "=v"(r) : "v"(voff), "s"(base) : "memory"); return r; }
DEV u32 aload_b32(const void* base, u32 voff) { u32 r; asm volatile("global_load_dword %0, %1, %2" : "=v"(r) : "v"(voff), "s"(base) : "memory"); return r; }
DEV u32x2 aload_b64_sc1(const void* base, u32 voff) { u32x2 r; asm volatile("global_load_dwordx2 %0, %1, %2 sc1" : "=v"(r) : "v"(voff), "s"(base) : "memory"); return r; }
DEV u32x4 aload_b128_sc1(const void* base, u32 voff) { u32x4 r; asm volatile("global_load_dwordx4 %0, %1, %2 sc1" : "=v"(r) : "v"(voff), "s"(base) : "memory"); return r; }
DEV u32x4 aload_b128(const void* base, u32 voff) { u32x4 r; asm volatile("global_load_dwordx4 %0, %1, %2" : "=v"(r) : "v"(voff), "s"(base) : "memory"); return r; }
DEV void astore_b128_sc1(void* base, u32 voff, u32x4 v) { asm volatile("global_store_dwordx4 %0, %1, %2 sc1\n\ts_nop 1" :: "v"(voff), "v"(v), "s"(base) : "memory"); }
DEV void astore_b64_sc1(void* base, u32 voff, u32x2 v) { asm volatile("global_store_dwordx2 %0, %1, %2 sc1\n\ts_nop 1" :: "v"(voff), "v"(v), "s"(base) : "memory"); }
DEV void flag_store(void* base, u32 off, u32x2 v) {
    u64 keep;
    asm volatile("s_mov_b64 %0, exec\n\ts_mov_b64 exec, 1\n\tglobal_store_dwordx2 %1, %2, %3 sc1\n\ts_mov_b64 exec, %0\n\ts_nop 0"
                 : "=&s"(keep) : "v"(off), "v"(v), "s"(base) : "memory");
}
DEV u32x4 trfrag(LAS char* a, LAS char* b) {
    const u32x2 lo = __builtin_bit_cast(u32x2, __builtin_amdgcn_ds_read_tr16_b64_v4i16((LAS s16x4*)a));
    const u32x2 hi = __builtin_bit_cast(u32x2, __builtin_amdgcn_ds_read_tr16_b64_v4i16((LAS s16x4*)b));
    return (u32x4){lo.x, lo.y, hi.x, hi.y};
}

#define TS(i)
#define TF(i)
#define TL(i)
#define TR(i)
#define TSOUT()

DEV void poll_flags(const char* flags, int first, int stride, int nfl, u32x2 tag, int lane) {
    const u32 off = (u32)(first + (lane < nfl ? lane : 0) * stride) * 128u;
#pragma unroll 1
    for (int spin = 0; spin < (1 << 16); ++spin) {
        u32x2 v = aload_b64_sc1(flags, off);
        asm volatile("s_waitcnt vmcnt(0)" : "+v"(v) :: "memory");
        if (__builtin_amdgcn_ballot_w64(v.x != tag.x || v.y != tag.y) == 0) break;
        __builtin_amdgcn_s_sleep(V_PSLEEP);
    }
}
DEV bool all_ge(u32 v, u32 target) { return __builtin_amdgcn_ballot_w64(v < target) == 0; }
DEV void lds_wait(LAS char* w, u32 target) {
#pragma unroll 1
    for (int spin = 0; spin < (1 << 21); ++spin) {
        if (all_ge(*(volatile LAS u32*)w, target)) break;
        __builtin_amdgcn_s_sleep(1);
    }
    asm volatile("" ::: "memory");
}
DEV void lds_post(LAS char* w, u32 v) { WAITL0(); *(volatile LAS u32*)w = v; }
template <int CTRL, int RM> DEV float dpp_add(float v) {
    return v + __builtin_bit_cast(float, __builtin_amdgcn_update_dpp(0, __builtin_bit_cast(int, v), CTRL, RM, 0xf, false));
}
template <int CTRL, int RM> DEV u32 dpp_umax(u32 v) { const u32 o = __builtin_amdgcn_update_dpp(0u, v, CTRL, RM, 0xf, false); return v > o ? v : o; }
DEV u32 wave_umax(u32 v) {
    v = dpp_umax<0xB1, 0xf>(v); v = dpp_umax<0x4E, 0xf>(v); v = dpp_umax<0x141, 0xf>(v); v = dpp_umax<0x140, 0xf>(v);
    v = dpp_umax<0x142, 0xa>(v); v = dpp_umax<0x143, 0xc>(v);
    return __builtin_amdgcn_readlane(v, 63);
}
DEV f32x2 cexp_f(float e2, float rev) {
    const float m = __builtin_amdgcn_exp2f(e2);
    return (f32x2){m * __builtin_amdgcn_cosf(rev), m * __builtin_amdgcn_sinf(rev)};
}
DEV f32x2 cmul_f(f32x2 a, f32x2 z) {
    f32x2 t, r;
    asm("v_pk_mul_f32 %0, %1, %2 op_sel_hi:[1,0]" : "=v"(t) : "v"(a), "v"(z));
    asm("v_pk_fma_f32 %0, %1, %2, %3 op_sel:[1,1,0] op_sel_hi:[0,1,1] neg_lo:[0,1,0]" : "=v"(r) : "v"(a), "v"(z), "v"(t));
    return r;
}
template <bool CONJ> DEV u32 cmh1(u32 a, u32 z) {
    u32 t;
    if (CONJ) asm("v_pk_mul_f16 %0, %1, %2 op_sel_hi:[1,0] neg_hi:[1,0]" : "=v"(t) : "v"(a), "v"(z));
    else asm("v_pk_mul_f16 %0, %1, %2 op_sel_hi:[1,0]" : "=v"(t) : "v"(a), "v"(z));
    return t;
}
template <bool CONJ> DEV u32 cmh2(u32 a, u32 z, u32 t) {
    u32 r;
    if (CONJ) asm("v_pk_fma_f16 %0, %1, %2, %3 op_sel:[1,1,0] op_sel_hi:[0,1,1] neg_lo:[0,1,0] neg_hi:[0,1,0]" : "=v"(r) : "v"(a), "v"(z), "v"(t));
    else asm("v_pk_fma_f16 %0, %1, %2, %3 op_sel:[1,1,0] op_sel_hi:[0,1,1] neg_lo:[0,1,0]" : "=v"(r) : "v"(a), "v"(z), "v"(t));
    return r;
}
DEV void build_table(LAS char* sm, int w, int lane, u32 are, u32 aim, u32 bre, u32 bim, u32 cre, u32 cim, u32 ldt, u32 dsk) {
    LAS char* t = sm + tab_off(w) + lane * 4;
    *(LAS u32*)(sm + L_MISC + 16 + w * 4) = dsk << 16;
    auto put = [&](int row, f32x2 v) { *(LAS u32*)(t + row * TAB_ROW) = pkh(v.x, v.y); };
    const float dtv = rbf(__builtin_amdgcn_exp2f(bf2f(ldt) * 1.4426950408889634f));
    const float a2 = dtv * bf2f(are) * 1.4426950408889634f;
    float rev = dtv * bf2f(aim) * 0.15915494309189535f;
    rev -= __builtin_rintf(rev);
    f32x2 wv = cmul_f((f32x2){bf2f(cre), bf2f(cim)}, (f32x2){bf2f(bre), bf2f(bim)});
    u32 E = wave_umax(__float_as_uint(fmaxf(dtv * fmaxf(fabsf(wv.x), fabsf(wv.y)) * __builtin_amdgcn_exp2f(fmaxf(a2, 0.f) * 2048.f), fabsf(bf2f(dsk)) * 0.0078125f))) >> 23;
    E = E < 16u ? 16u : (E > 236u ? 236u : E);
    wv *= dtv * __uint_as_float((260u - E) << 23);
    *(LAS float*)(sm + L_MISC + w * 4) = (float)(int)E - 133.f;
    const f32x2 z1 = cexp_f(a2, rev), z64 = cexp_f(a2 * 64.f, rev * 64.f);
    const f32x2 z2 = cmul_f(z1, z1), z3 = cmul_f(z2, z1), z4 = cmul_f(z2, z2), z8 = cmul_f(z4, z4), z16 = cmul_f(z8, z8), z32 = cmul_f(z16, z16);
    const f32x2 z128 = cmul_f(z64, z64), z256 = cmul_f(z128, z128), z512 = cmul_f(z256, z256);
    const f32x2 w1 = cmul_f(wv, z512), w2 = cmul_f(w1, z512);
    *(LAS u32*)t = 0x00003c00u;
    put(1, z1); put(2, z2); put(3, z3); put(4, z4); put(5, cmul_f(z4, z1)); put(6, cmul_f(z3, z3)); put(7, cmul_f(z4, z3)); put(8, z8);
    put(9, z16); put(10, cmul_f(z16, z8)); put(11, z32); put(12, z64); put(13, cmul_f(z64, z32));
    put(14, z128); put(15, z256); put(16, cmul_f(z256, z128));
    put(17, wv); put(18, w1); put(19, w2); put(20, cmul_f(w2, z512));
}
DEV u32 perm(u32 hi, u32 lo, u32 sel) { return __builtin_amdgcn_perm(hi, lo, sel); }

__global__ __launch_bounds__(512, 2) void fused_kernel(Params p) {
    __shared__ __attribute__((aligned(1024))) char shm[163840];
    LAS char* sm = (LAS char*)shm;
    const u32 lds0 = (u32)(uintptr_t)sm;
    const int wid = __builtin_amdgcn_readfirstlane(threadIdx.x >> 6);
    const int b = blockIdx.x, g = b & 7, r = b >> 3, hw = wid & 3;
    char* flags = p.ws + WS_FLAG;
    const int ch0 = 64 * g + 16 * (r & 3) + (r >> 2);
    const int tt = V_MAP ? (g >> 1) * 8 + (r >> 2) : g * 4 + (r >> 3), dt = V_MAP ? (g & 1) * 4 + (r & 3) : r & 7;
    const int qo = V_W2ROT ? 2 * (tt & 1) : 0;
    const int k0 = V_KROT == 2 ? (r % 7 + 2 * (r / 7)) % 7 : (V_KROT ? r % 7 + 2 * (r / 7) : 0);
    const int rot = V_HROT == 0 ? 0 : (V_HROT == 2 ? (dt & 1) * 2 : (dt + (V_W2ROT ? tt & 2 : 0)) & 3);
    const bool isG = r < 28;
    const int mt = (g >> 1) * 4 + r / 7, ft = (g & 1) * 7 + r % 7;
#define LANEVALS() \
    int tid = threadIdx.x; asm volatile("" : "+v"(tid)); \
    const int lane = tid & 63, h = lane >> 5, l31 = lane & 31; \
    const u32 vsw = (u32)(((lane & 7) ^ (((lane >> 4) & 1) << 2)) << 4);          \
    const u32 voffw2 = (u32)((lane >> 3) * 1024 + dt * 128) + vsw; \
    auto w2_dma = [&](auto pace, int kq, int sg) {        \
        glds_run<3, true, decltype(pace)::value>((const char*)p.w2 + (size_t)(4 * ((kq + qo) & 3) + ((sg + rot) & 3)) * (112 * 1024), 32768u, voffw2, voffw2 + 7168u, voffw2 + 14336u, voffw2 + 21504u, \
                                 lds0 + L_W2 + kq * W2_REG + (sg & 1) * 14336); \
    }; \
    const int blk = (lane >> 4) & 1, q4 = (lane & 15) >> 2, pp = lane & 3; \
      \
    const u32 tra0 = (u32)((4 * h + q4) * 128 + (((2 * blk + (pp >> 1)) ^ (((q4 >> 1) & 1) << 2)) << 4) + 8 * (pp & 1)); \
    const u32 xo = (u32)((tt * 64 + (tid >> 3)) * 1024 + dt * 128 + (tid & 7) * 16); \
    (void)l31; (void)voffw2; (void)w2_dma; (void)tra0; (void)xo;
    if (V_CODE && wid == 6) { const u32 ln = (u32)(r + 32 * (threadIdx.x & 7)); touch((const char*)(__builtin_amdgcn_s_getpc() & ~127ull), (ln < CODE_LINES ? ln : 0u) * 128u, lds0 + L_JUNK); }
    if (wid == 0) *(LAS u32*)(sm + L_MISC + 64 + (threadIdx.x & 31) * 4) = 0;
    u32x2 tag1;
    u32x4 xe, b2e;
#define WAIT_PARAMS(n) asm volatile("s_waitcnt vmcnt(" #n ")" : "+v"(are), "+v"(aim), "+v"(bre), "+v"(bim), "+v"(cre), "+v"(cim), "+v"(ldt), "+v"(dsk) :: "memory")
#define LOAD_PARAMS(po, c2) \
    asm volatile("global_load_ushort %0, %8, %10\n\tglobal_load_ushort %1, %8, %11\n\tglobal_load_ushort %2, %8, %12\n\tglobal_load_ushort %3, %8, %13\n\t" \
                 "global_load_ushort %4, %8, %14\n\tglobal_load_ushort %5, %8, %15\n\tglobal_load_ushort %6, %9, %16\n\tglobal_load_ushort %7, %9, %17" \
                 : "=&v"(are), "=&v"(aim), "=&v"(bre), "=&v"(bim), "=&v"(cre), "=&v"(cim), "=&v"(ldt), "=&v"(dsk) \
                 : "v"(po), "v"(c2), "s"(p.Are), "s"(p.Aim), "s"(p.Bre), "s"(p.Bim), "s"(p.Cre), "s"(p.Cim), "s"(p.logdt), "s"(p.Dskip) : "memory")
#define LOADER_VALS() \
    const bool lx = wid == 4;                  \
    const u32 lR = lx ? (u32)(lane >> 3) * 1024u : (u32)(lane >> 4) * 3584u; \
    const u32 lS = lx ? (u32)(((lane & 7) ^ (lane >> 4)) << 4) : (u32)(((lane & 15) ^ ((lane >> 4) << 2)) << 4); \
    const u32 lst = lx ? 7168u : 13312u, x1 = lx ? 64u : 16u, x2 = lx ? 0u : 32u; \
    const u32 vo0 = lR + lS, vo1 = lR + (lS ^ x1) + lst, vo2 = lR + (lS ^ x2) + 2 * lst, vo3 = lR + (lS ^ (x1 | x2)) + 3 * lst; \
    const char* lsrc = lx ? (const char*)p.x + (size_t)mt * 128 * 1024 : (const char*)p.w1 + ft * 256; \
    const u32 lgs = lx ? 32768u : 16u * 3584u, lcs = lx ? 128u : 64u * 3584u; \
    auto chunk_src = [&](int kc) { return lsrc + (V_KROT == 2 ? (kc == 0 ? 0 : (kc + k0 > 7 ? kc + k0 - 7 : kc + k0)) : (kc + k0) & 7) * lcs; }; \
    auto stage = [&](int kc) { glds_run<4, false, V_PACE>(chunk_src(kc), lgs, vo0, vo1, vo2, vo3, lds0 + (kc & 3) * 32768 + hw * 16384); };
#define READ_TAG() do { u32x4 t_ = *(volatile LAS u32x4*)(sm + L_TAG); \
        if (!all_ge(t_.y, 1)) { lds_wait(sm + L_TAG + 4, 1); t_ = *(volatile LAS u32x4*)(sm + L_TAG); } \
        tag1.x = __builtin_amdgcn_readfirstlane(t_.z); tag1.y = __builtin_amdgcn_readfirstlane(t_.w); } while (0)
    const int lj = g * 4 + (r - 28);

    const int tb8 = g * 32 + r;

    if (wid < 4) {
        LANEVALS();
        const int kq = wid;
        u32x4 wreg[14][2];
        LAS char* w2a = sm + L_W2 + kq * W2_REG;
        auto w2frag = [&](int s, int j2) { LAS char* a = w2a + (j2 ? tra0 ^ 64 : tra0) + s * 2048; return trfrag(a, a + 1024); };
        auto w2load = [&]() {
#pragma unroll
            for (int s = 0; s < 14; s++) { wreg[s][0] = w2frag(s, 0); wreg[s][1] = w2frag(s, 1); }
        };
#define W2PIN() _Pragma("unroll") for (int s = 0; s < 14; s++) asm volatile("" : "+v"(wreg[s][0]), "+v"(wreg[s][1]))
        LAS char* fw = sm + F_L + (wid & 2) * 4 + (lane & 1) * 4;
        bool early = false;
        const int th = wid & 1, fh = wid >> 1;
        u32 are, aim, bre, bim, cre, cim, ldt, dsk, b1a, b1b;
        {
            const int chp = ch0 + 8 * th;
            const u32 po = (u32)(chp * 128 + fh * 64 + lane) * 2u, c2 = (u32)chp * 2u, bo = (u32)(ft * 128 + fh * 64 + l31) * 2u;
            if (wid < 3) LOAD_PARAMS(po, c2);
            asm volatile("global_load_ushort %0, %2, %3\n\tglobal_load_ushort %1, %2, %3 offset:64" : "=&v"(b1a), "=&v"(b1b) : "v"(bo), "s"(p.b1) : "memory");
            TS(40);
            BAR();
            SCHED();
        }
        PRIO(V_MPRIO);
        u32 xa[4], wa[2][2];
#pragma unroll
        for (int s = 0; s < 4; s++) xa[s] = (u32)((64 * th + l31) * 128 + (((2 * s + h) ^ ((l31 >> 1) & 7)) << 4));
#pragma unroll
        for (int j2 = 0; j2 < 2; j2++)
#pragma unroll
            for (int t = 0; t < 2; t++)
                wa[j2][t] = (u32)(16384 + 256 * (8 * h + 4 * t + q4) + (((4 * (2 * fh + j2) + 2 * blk + (pp >> 1)) ^ ((q4 << 2) | (2 * h + t))) << 4) + 8 * (pp & 1));
        u32 so = GR_ST + (u32)(mt * 128 + th * 64 + l31) * 16u, co = GR_CS + (u32)(ft * 128 + fh * 64 + l31) * 8u;
        asm volatile("" : "+v"(so), "+v"(co), "+v"(xa[0]), "+v"(xa[1]), "+v"(xa[2]), "+v"(xa[3]), "+v"(wa[0][0]), "+v"(wa[0][1]), "+v"(wa[1][0]), "+v"(wa[1][1]));
        WAIT_PARAMS(2);
        TS(1);
        if (wid < 3) build_table(sm, wid, lane, are, aim, bre, bim, cre, cim, ldt, dsk);
        LAS char* fm = sm + F_M + wid * 4;
        LAS char* fl = sm + F_L + (lane & 1) * 4;
        lds_post(fm, 1);
        TS(2);
        if (isG) {
            f32x16 acc[2][2] = {};
            lds_wait(fl, 1);
            TS(3);
            u32x4 X[4][2], W[4][2];
            u32 wp[2][2], xp;
#define RX(s, j) X[s][j] = *(LAS u32x4*)(sm + xp + (j) * 4096)
#define RW(s, j) W[s][j] = trfrag(sm + wp[j][0] + (s) * 4096, sm + wp[j][1] + (s) * 4096)
#define OPQ(v) asm volatile("" : "+v"(v))
#define MM(s, j2, j) acc[j2][j] = MFMA32(W[s][j2], X[s][j], acc[j2][j])
            u32x4 st0, st1; u32x2 cs0, cs1;
#define LOAD_GRAN() asm volatile("global_load_dwordx4 %0, %4, %6 sc1\n\tglobal_load_dwordx4 %1, %4, %6 offset:512 sc1\n\tglobal_load_dwordx2 %2, %5, %6 sc1\n\tglobal_load_dwordx2 %3, %5, %6 offset:256 sc1" \
                                 : "=&v"(st0), "=&v"(st1), "=&v"(cs0), "=&v"(cs1) : "v"(so), "v"(co), "s"(flags) : "memory")
            wp[0][0] = wa[0][0]; wp[0][1] = wa[0][1]; wp[1][0] = wa[1][0]; wp[1][1] = wa[1][1];
            xp = xa[0]; RX(0, 0); SCHED(); RW(0, 0); SCHED(); RW(0, 1); SCHED(); RX(0, 1); SCHED();
            xp = xa[1]; RX(1, 0); SCHED(); RW(1, 0); SCHED(); RW(1, 1); SCHED(); RX(1, 1); SCHED();
            xp = xa[2];
#pragma unroll
            for (int kc = 0; kc < 7; kc++) {
                const u32 sl = (u32)(kc & 3) * 32768u, sn = (u32)((kc + 1) & 3) * 32768u;
                if (kc == 5) LOAD_GRAN();
                MM(0, 0, 0); RX(2, 0); SCHED(); MM(0, 1, 0); RW(2, 0); SCHED(); MM(0, 0, 1); RW(2, 1); SCHED();
                MM(0, 1, 1); RX(2, 1); xp = xa[3] + sl; OPQ(xp); SCHED();
                MM(1, 0, 0); RX(3, 0);
                SCHED();
                MM(1, 1, 0); RW(3, 0); SCHED(); MM(1, 0, 1); RW(3, 1); SCHED();
                MM(1, 1, 1); RX(3, 1); xp = xa[0] + sn; wp[0][0] = wa[0][0] + sn; wp[0][1] = wa[0][1] + sn; OPQ(xp); OPQ(wp[0][0]); OPQ(wp[0][1]); SCHED();
                MM(2, 0, 0); SCHED();
                if (kc == 0) lds_wait(fl, 2); else RBAR();
                SCHED();
                RX(0, 0); wp[1][0] = wa[1][0] + sn; wp[1][1] = wa[1][1] + sn; OPQ(wp[1][0]); OPQ(wp[1][1]); SCHED();
                MM(2, 1, 0); RW(0, 0); SCHED(); MM(2, 0, 1); RW(0, 1); SCHED();
                MM(2, 1, 1); RX(0, 1); xp = xa[1] + sn; OPQ(xp); SCHED();
                MM(3, 0, 0); RX(1, 0); SCHED(); MM(3, 1, 0); RW(1, 0); SCHED(); MM(3, 0, 1); RW(1, 1); SCHED();
                MM(3, 1, 1); RX(1, 1); xp = xa[2] + sn; OPQ(xp);
                SCHED();
                TF(4 + kc);
            }
            MM(0, 0, 0); RX(2, 0); RW(2, 0); SCHED(); MM(0, 1, 0); RW(2, 1); RX(2, 1); xp = xa[3] + 3 * 32768; SCHED();
            MM(0, 0, 1); RX(3, 0); RW(3, 0); SCHED(); MM(0, 1, 1); RW(3, 1); RX(3, 1); SCHED();
            MM(1, 0, 0); MM(1, 1, 0); SCHED();
            READ_TAG();
            lds_post(fm, 9);
            asm volatile("s_waitcnt vmcnt(0)" : "+v"(st0), "+v"(st1), "+v"(cs0), "+v"(cs1), "+v"(b1a), "+v"(b1b) :: "memory");
#pragma unroll 1
            for (int spin = 0; spin < (1 << 16); ++spin) {
                if (__builtin_amdgcn_ballot_w64((((st0.y ^ tag1.x) | (st0.w ^ tag1.x) | (st1.y ^ tag1.x)) | ((st1.w ^ tag1.x) | (cs0.y ^ tag1.x) | (cs1.y ^ tag1.x))) != 0u) == 0) break;
                __builtin_amdgcn_s_sleep(2);
                LOAD_GRAN();
                asm volatile("s_waitcnt vmcnt(0)" : "+v"(st0), "+v"(st1), "+v"(cs0), "+v"(cs1) :: "memory");
            }
            TS(19);
            float rinv[2] = {__uint_as_float(st0.z), __uint_as_float(st1.z)};
            float k1[2], k2[2];
            {
                u32x4 Ax[2], Bx[2];
                auto prepB = [&](int j) {
                    const float nm = __uint_as_float(j ? st1.x : st0.x), m1 = rbf(nm), mr = opq(nm - m1), m2 = rbf(mr), ih = rbf(rinv[j]);
                    const u32 mm = pkbf(m1, m2);
                    const u32 hm = (u32)h - 1u;
                    Bx[j] = (u32x4){mm & hm, mm & hm, pkbf(opq(mr - m2), m1) & hm, pkbf(ih, opq(rinv[j] - ih)) & hm};
                };
                auto prepA = [&](int j) {
                    const float c = __uint_as_float(j ? cs1.x : cs0.x), c1 = rbf(c), cr = opq(c - c1), c2 = rbf(cr);
                    const u32 bb = j ? b1b : b1a;
                    Ax[j] = (u32x4){pkbf(c1, c1), pkbf(c2, c2), pkbf(c1, opq(cr - c2)), bb | (bb << 16)};
                };
                auto prepK = [&](int j) {
                    const float rr = __builtin_amdgcn_rcpf(rinv[j]);
                    k1[j] = opq(-2.302208198144325f * rr); k2[j] = opq(-0.10294323958002349f * rr * rr * rr);
                };
                MM(1, 0, 1); MM(1, 1, 1); prepB(0); SCHED();
                MM(2, 0, 0); prepA(0); SCHED(); MM(2, 1, 0); prepA(1); SCHED(); MM(2, 0, 1); MM(2, 1, 1); prepB(1); SCHED();
                MM(3, 0, 0); prepK(0); SCHED(); acc[0][0] = MFMA32(Ax[0], Bx[0], acc[0][0]); prepK(1); SCHED();
                MM(3, 1, 0); acc[1][0] = MFMA32(Ax[1], Bx[0], acc[1][0]);
                MM(3, 0, 1); acc[0][1] = MFMA32(Ax[0], Bx[1], acc[0][1]);
                MM(3, 1, 1); acc[1][1] = MFMA32(Ax[1], Bx[1], acc[1][1]);
                SCHED();

            }
            PRIO(2);
            TS(20);
            char* hdst = p.ws + WS_H + (size_t)((mt * 4 + th * 2) * 112 + ft * 8 + fh * 4) * 1024;
#pragma unroll
            for (int j = 0; j < 2; j++) {
                const f32x2 K1 = {k1[j], k1[j]}, K2 = {k2[j], k2[j]}, RI = {rinv[j], rinv[j]};
#pragma unroll
                for (int j2 = 0; j2 < 2; j2++) {
#pragma unroll
                    for (int s = 0; s < 2; s++) {
                        u32x4 o;
#pragma unroll
                        for (int i = 0; i < 4; i++) {
                            const f32x2 u = {acc[j2][j][8 * s + 2 * i], acc[j2][j][8 * s + 2 * i + 1]};
                            const f32x2 z = __builtin_elementwise_fma(u * u, K2, K1) * u;
                            const f32x2 e = {__builtin_amdgcn_exp2f(z.x), __builtin_amdgcn_exp2f(z.y)};
                            const f32x2 dn = __builtin_elementwise_fma(e, RI, RI);
                            const f32x2 gl = u * (f32x2){__builtin_amdgcn_rcpf(dn.x), __builtin_amdgcn_rcpf(dn.y)};
                            o[i] = pkbf(gl.x, gl.y);
                        }
                        asm volatile("global_store_dwordx4 %0, %1, %2 offset:%3 sc1\n\ts_nop 1" :: "v"(lane * 16 + j * 112 * 1024), "v"(o), "s"(hdst), "i"((j2 * 2 + s) * 1024) : "memory");
                        SCHED();
                        if (V_EARLY && j == 1 && j2 == 1 && s == 0) { early = all_ge(*(volatile LAS u32*)fw, 12); if (early) w2load(); SCHED(); }
                    }
                }
            }
            if (early) { W2PIN(); }
            lds_post(fm, 10);
            TS(21);
        } else {
            lds_wait(sm + F_P + 12, 1);
            if (V_MALL & 4) {
                const u32 v0 = (u32)lane * 16u;
                glds_run<3, true, V_MTHR>((const char*)p.w2 + (size_t)(lj * 4 + wid) * 14336, 4096u, v0, v0, v0, v0, lds0 + 65536);
            }
            WAITV(0); lds_post(fm, 10); READ_TAG();
        }
        PRIO(1);
        if (!early) {
            if (isG) { WAITV(0); flag_store(flags, (u32)(FL_H + (mt * 2 + (wid & 1)) * 32 + ft * 2 + (wid >> 1)) * 128u, tag1); }
            lds_wait(fw, 12);
            w2load();
            W2PIN();
            if (!isG) lds_wait(fl, 13);
            WAITL0();
        }
        TS(22);
        if (V_SAFE && early) { WAITV(0); flag_store(flags, (u32)(FL_H + (mt * 2 + (wid & 1)) * 32 + ft * 2 + (wid >> 1)) * 128u, tag1); }
        w2_dma(IC<V_MTHR>(), kq, 2);
        if (!V_SAFE && early) { WAITV(14); flag_store(flags, (u32)(FL_H + (mt * 2 + (wid & 1)) * 32 + ft * 2 + (wid >> 1)) * 128u, tag1); }
        TS(24);
        w2_dma(IC<V_MTHR>(), kq, 3);
        TS(18);
        poll_flags(flags, FL_H + tt * 32 + 7 * ((kq + qo) & 3), 1, 7, tag1, lane);
        TS(25);
        const __amdgpu_buffer_rsrc_t hr = __builtin_amdgcn_make_buffer_rsrc(p.ws + WS_H + (size_t)(tt * 2 * 112 + ((kq + qo) & 3) * 28) * 1024, 0, 2 * 112 * 1024, 0x00020000);
        constexpr int WIN = V_WIN;
        u32x4 hf[WIN][2];
        f32x16 acc[2][2] = {};
        auto ldh = [&](int s, int j) {
            return __builtin_amdgcn_raw_buffer_load_b128(hr, lane * 16 + j * 112 * 1024 + ((s % 7) & 3) * 1024, ((s / 7 + rot) & 3) * 7168 + ((s % 7) >> 2) * 4096, 16);
        };
#pragma unroll
        for (int s = 0; s < WIN; s++) { hf[s][0] = ldh(s, 0); SCHED(); hf[s][1] = ldh(s, 1); SCHED(); }
        TS(26);
#pragma unroll
        for (int s = 0; s < 28; s++) {
            if (s == 12) { TS(27); }
            const u32x4 w0 = wreg[s % 14][0], w1 = wreg[s % 14][1];
            acc[0][0] = MFMA32(w0, hf[s % WIN][0], acc[0][0]); SCHED();
            acc[1][0] = MFMA32(w1, hf[s % WIN][0], acc[1][0]);
            if (s + WIN < 28) hf[s % WIN][0] = ldh(s + WIN, 0);
            SCHED();
            acc[0][1] = MFMA32(w0, hf[s % WIN][1], acc[0][1]);
            if (s >= 12 && s < 26) wreg[s - 12][0] = w2frag(s - 12, 0);
            SCHED();
            acc[1][1] = MFMA32(w1, hf[s % WIN][1], acc[1][1]);
            if (s >= 12 && s < 26) wreg[s - 12][1] = w2frag(s - 12, 1);
            if (s + WIN < 28) hf[s % WIN][1] = ldh(s + WIN, 1);
            if (s + WIN == 28) { LANEVALS(); xe = *(const u32x4*)((const char*)p.x + xo); b2e = *(const u32x4*)((const char*)p.b2 + dt * 128 + (tid & 7) * 16); }
            SCHED();
        }
        TS(29);
        int t2 = threadIdx.x; asm volatile("" : "+v"(t2));
        LAS char* pw = w2a + (t2 & 31) * 272 + ((t2 >> 5) & 1) * 16;
#pragma unroll
        for (int j2 = 0; j2 < 2; j2++)
#pragma unroll
            for (int j = 0; j < 2; j++)
#pragma unroll
                for (int rg = 0; rg < 4; rg++) {
                    f32x4 v = {acc[j2][j][4 * rg], acc[j2][j][4 * rg + 1], acc[j2][j][4 * rg + 2], acc[j2][j][4 * rg + 3]};
                    *(LAS f32x4*)(pw + 32 * j * 272 + (32 * j2 + 8 * rg) * 4) = v;
                }
        TS(32);
    } else {
        LANEVALS();
        const int a = lane & 15, q = lane >> 4;
        const int cl = hw & 1, rh = hw >> 1, chn = ch0 + 8 * cl;
        if (wid >= 6) {
            const int kc = wid & 1;
            PRIO(V_KPRIO);
            u32 junk = 0;
            if (kc == 0) {
                u32 are, aim, bre, bim, cre, cim, ldt, dsk;
                LOAD_PARAMS((u32)((ch0 + 8) * 128 + 64 + lane) * 2u, (u32)(ch0 + 8) * 2u);
                BAR();
                if (V_TLB) {
                    asm volatile("global_load_dword %0, %1, %2" : "+v"(junk) : "v"((u32)((lane & 15) < 10 ? (lane & 15) : 0) * 0x200000u + 0x1fff80u), "s"(p.ws) : "memory");
                    asm volatile("global_load_dword %0, %1, %2" : "+v"(junk) : "v"(xo), "s"(p.out) : "memory");
                    asm volatile("global_load_dword %0, %1, %2" : "+v"(junk) : "v"(0), "s"(p.b2) : "memory");
                    asm volatile("global_load_dword %0, %1, %2" : "+v"(junk) : "v"((u32)(lane & 1) * 0x1bff80u), "s"(p.w2) : "memory");
                }
                if (V_MALL & 1) asm volatile("global_load_dword %0, %1, %2" : "+v"(junk) : "v"((u32)(lane < 56 ? lane : 0) * 128u), "s"(uni((const char*)p.w1 + (size_t)b * 7168)) : "memory");
                if (V_MALL & 2) {
                    __builtin_amdgcn_s_sleep(V_MDELAY);
                    asm volatile("global_load_dword %0, %1, %2" : "+v"(junk) : "v"((u32)(lane < 56 ? lane : 0) * 128u), "s"(uni((const char*)p.w2 + (size_t)b * 7168)) : "memory");
                }
                if (V_TLB) WAIT_PARAMS(5); else WAIT_PARAMS(1);
                TS(41);
                build_table(sm, 3, lane, are, aim, bre, bim, cre, cim, ldt, dsk);
                lds_post(sm + F_P + 8, 1);
                READ_TAG();
                TS(42);
            } else {
                PRIO(3);
                u32x2 tge;
                u32x4 xv[8];
                asm volatile("global_load_dwordx4 %0, %8, %10\n\tglobal_load_dwordx4 %1, %8, %10 offset:1024\n\tglobal_load_dwordx4 %2, %8, %10 offset:2048\n\tglobal_load_dwordx4 %3, %8, %10 offset:3072\n\t"
                             "global_load_dwordx4 %4, %9, %10\n\tglobal_load_dwordx4 %5, %9, %10 offset:1024\n\tglobal_load_dwordx4 %6, %9, %10 offset:2048\n\tglobal_load_dwordx4 %7, %9, %10 offset:3072"
                             : "=&v"(xv[0]), "=&v"(xv[1]), "=&v"(xv[2]), "=&v"(xv[3]), "=&v"(xv[4]), "=&v"(xv[5]), "=&v"(xv[6]), "=&v"(xv[7])
                             : "v"(lane * 16), "v"(lane * 16 + 4096), "s"((const char*)p.x + (size_t)tb8 * 8192) : "memory");
                asm volatile("global_load_dwordx2 %0, %1, %2 sc1" : "=&v"(tge) : "v"(FL_E * 128), "s"(flags) : "memory");
                TS(40);
                BAR();
                asm volatile("s_waitcnt vmcnt(0)" : "+v"(tge), "+v"(xv[0]), "+v"(xv[1]), "+v"(xv[2]), "+v"(xv[3]), "+v"(xv[4]), "+v"(xv[5]), "+v"(xv[6]), "+v"(xv[7]) :: "memory");
                tag1.x = __builtin_amdgcn_readfirstlane(tge.x) + 1u; tag1.y = __builtin_amdgcn_readfirstlane(tge.y);
                TS(41);
                *(volatile LAS u32x2*)(sm + L_TAG + 8) = tag1;
                lds_post(sm + L_TAG + 4, 1);
                float s1[8], s2[8];
#pragma unroll
                for (int i = 0; i < 8; i++) { s1[i] = 0.f; s2[i] = 0.f; }
#pragma unroll
                for (int j = 0; j < 4; j++)
#pragma unroll
                    for (int i = 0; i < 8; i++) { s1[i] = dot2(xv[i][j], 0x3f803f80u, s1[i]); s2[i] = dot2(xv[i][j], xv[i][j], s2[i]); }
                auto red8 = [&](float (&v)[8]) {
                    float a[4], c[2];
#pragma unroll
                    for (int i = 0; i < 4; i++) {
                        const auto t = __builtin_amdgcn_permlane32_swap(__float_as_uint(v[i]), __float_as_uint(v[i + 4]), false, false);
                        a[i] = __uint_as_float(t[0]) + __uint_as_float(t[1]);
                    }
#pragma unroll
                    for (int i = 0; i < 2; i++) {
                        const auto t = __builtin_amdgcn_permlane16_swap(__float_as_uint(a[i]), __float_as_uint(a[i + 2]), false, false);
                        c[i] = dpp_add<0x128, 0xf>(__uint_as_float(t[0]) + __uint_as_float(t[1]));
                    }
                    float e = (lane & 8) ? c[1] : c[0];
                    e = dpp_add<0x141, 0xf>(e); e = dpp_add<0xB1, 0xf>(e); e = dpp_add<0x4E, 0xf>(e);
                    return e;
                };
                const float a1 = red8(s1), a2 = red8(s2);
                const float mean = a1 * (1.f / 512.f);
                const float v1 = fmaxf(a2 * (1.f / 512.f) - mean * mean, 0.f);
                const float q1 = v1 + 1e-5f;
                const float rs = __builtin_amdgcn_rsqf(q1);
                const float q2 = v1 * rs * rs + 1e-5f;
                {
                    u32x4 o = {__float_as_uint(-mean), tag1.x, __float_as_uint(__builtin_amdgcn_sqrtf(q1 * q2)), tag1.x};
                    u64 keep;
                    asm volatile("s_mov_b64 %0, exec\n\ts_mov_b64 exec, %4\n\tglobal_store_dwordx4 %1, %2, %3 sc1\n\ts_mov_b64 exec, %0\n\ts_nop 1"
                                 : "=&s"(keep) : "v"(GR_ST + (u32)(tb8 * 8 + (lane >> 3)) * 16u), "v"(o), "s"(flags), "s"(0x0101010101010101ull) : "memory");
                }
                TS(42);
                f32x2 sc[4], of[4];
                const float nmr = -mean * rs;
#pragma unroll
                for (int i = 0; i < 4; i++) {
                    sc[i] = (f32x2){__builtin_bit_cast(float, __builtin_amdgcn_readlane(__builtin_bit_cast(int, rs), 16 * i)), __builtin_bit_cast(float, __builtin_amdgcn_readlane(__builtin_bit_cast(int, rs), 16 * i + 8))};
                    of[i] = (f32x2){__builtin_bit_cast(float, __builtin_amdgcn_readlane(__builtin_bit_cast(int, nmr), 16 * i)), __builtin_bit_cast(float, __builtin_amdgcn_readlane(__builtin_bit_cast(int, nmr), 16 * i + 8))};
                }
#pragma unroll
                for (int c = 0; c < 8; c++) {
                    u32x4 o;
#pragma unroll
                    for (int i = 0; i < 4; i++) {
                        const u32 a = xv[2 * i][c >> 1], d = xv[2 * i + 1][c >> 1];
                        const f32x2 v = (c & 1) ? (f32x2){bfhi(a), bfhi(d)} : (f32x2){bflo(a), bflo(d)};
                        const f32x2 n = __builtin_elementwise_fma(v, sc[i], of[i]);
                        o[i] = pkh(n.x, n.y);
                    }
                    astore_b128_sc1(p.ws + WS_U + (size_t)tb8 * 8192, (u32)(c * 1024 + lane * 16), o);
                }
                PRIO(V_KPRIO);
                TS(45);
            }
            const int rS = (int)((0x8080840876543210ull >> (4 * a)) & 15), rB = 17 + (int)((0xfa540000u >> (2 * a)) & 3);
            const int rT1 = (a & 3) ? 7 + (a & 3) : 0, rT2 = (a >> 2) ? 10 + (a >> 2) : 0;
            LAS char* tb = sm + L_TAB + kc * TAB_SZ + q * 16;
            LAS char *pS = tb + rS * TAB_ROW, *pB = tb + rB * TAB_ROW, *pT1 = tb + rT1 * TAB_ROW, *pT2 = tb + rT2 * TAB_ROW, *pZ = tb + 14 * TAB_ROW;
            const int hd = tab_off(kc + 2) - tab_off(kc);
            f32x4 G[2][4] = {};
            u32x4 T[2][7];
            auto ldtab = [&](int k) {
                if (k == 4) { pS += hd; pB += hd; pT1 += hd; pT2 += hd; pZ += hd; }
                u32x4* t = T[k & 1];
                const int o = (k & 3) * 64;
                t[0] = *(LAS u32x4*)(pS + o); t[1] = *(LAS u32x4*)(pB + o); t[2] = *(LAS u32x4*)(pT1 + o); t[3] = *(LAS u32x4*)(pT2 + o);
                t[4] = *(LAS u32x4*)(pZ + o); t[5] = *(LAS u32x4*)(pZ + o + TAB_ROW); t[6] = *(LAS u32x4*)(pZ + o + 2 * TAB_ROW);
            };
            int nb = isG ? 0 : 6; bool go = false;
#define SYNCPT() do { if (nb < 6) { if (go) { RBAR(); nb++; } else go = all_ge(*(volatile LAS u32*)(sm + F_L + (lane & 1) * 4), 2); } } while (0)
            lds_wait(sm + F_M + (lane & 3) * 4, 1);
            TS(3);
            ldtab(0);

#pragma unroll
            for (int k = 0; k < 8; k++) {
                u32x4* t = T[k & 1];
                f32x4* Gh = G[k >> 2];
                u32x4 A, B0, B1, B2, B3;
                asm volatile("" :: "v"(t[0]), "v"(t[1]), "v"(t[2]), "v"(t[3]), "v"(t[4]), "v"(t[5]), "v"(t[6]));
                if (k == 3) lds_wait(sm + F_P + 8, 1);
                TF(4 + 2 * k);
                if (k < 7) ldtab(k + 1);
                TF(5 + 2 * k);
                SCHED();
#pragma unroll
                for (int j = 0; j < 4; j++) { B0[j] = cmh2<false>(t[2][j], t[3][j], cmh1<false>(t[2][j], t[3][j])); A[j] = cmh2<true>(t[0][j], t[1][j], cmh1<true>(t[0][j], t[1][j])); }
                SCHED();
#pragma unroll
                for (int j = 0; j < 4; j++) B1[j] = cmh2<false>(B0[j], t[4][j], cmh1<false>(B0[j], t[4][j]));
                SCHED();
                Gh[0] = MFMA16H(A, B0, Gh[0]);
#pragma unroll
                for (int j = 0; j < 4; j++) B2[j] = cmh2<false>(B0[j], t[5][j], cmh1<false>(B0[j], t[5][j]));
                SCHED();
                Gh[1] = MFMA16H(A, B1, Gh[1]);
#pragma unroll
                for (int j = 0; j < 4; j++) B3[j] = cmh2<false>(B0[j], t[6][j], cmh1<false>(B0[j], t[6][j]));
                SCHED();
                Gh[2] = MFMA16H(A, B2, Gh[2]);
                asm volatile("s_nop 1" : "+v"(B3));
                Gh[3] = MFMA16H(A, B3, Gh[3]);
                SCHED();
                if (V_S1) SYNCPT();
                if (k == V_LNK && kc) { WAITV(0); flag_store(flags, (u32)(FL_LN * 128 + tb8 * 8), tag1); if (!isG) lds_post(sm + F_P + 12, 1); TS(47); }
                if (!V_S1) SYNCPT();
            }
            if (V_XP) while (nb < 6) { RBAR(); nb++; }
            TS(20);
            {
                const float M0 = *(LAS float*)(sm + L_MISC + kc * 4), M1 = *(LAS float*)(sm + L_MISC + 8 + kc * 4), M = fmaxf(M0, M1);
                const float s0 = __builtin_amdgcn_exp2f(M0 - M), s1 = __builtin_amdgcn_exp2f(M1 - M);
                const float dx = *(LAS float*)(sm + L_MISC + 16 + kc * 4) * __builtin_amdgcn_exp2f(-M), d0 = lane == 0 ? dx : 0.f;
                LAS char* gx = sm + L_TAB + kc * TAB_SZ;
#pragma unroll
                for (int ct = 0; ct < 4; ct++) {
                    float v[4];
#pragma unroll
                    for (int e = 0; e < 4; e++) v[e] = opq(fmaf(G[1][ct][e], s1, (ct | e) ? opq(G[0][ct][e] * s0) : fmaf(G[0][ct][e], s0, d0)));
                    u32x2 o = {pkh(v[0], v[1]), pkh(v[2], v[3])};
                    *(LAS u32x2*)(gx + (16 * ct + a) * 32 + q * 8) = o;
                }
                SYNCPT();
                WAITL0();
                const u32x4 u = *(LAS u32x4*)(gx + lane * 32), v = *(LAS u32x4*)(gx + lane * 32 + 16);
                LAS char* kp = sm + L_KP + kc * KP_CH + (lane & 1) * KP_PLANE + (16 + (lane >> 1)) * 16;
                const u32 LL = 0x01000100u, HH = 0x03020302u, SW = 0x01000302u;
                const u32 sa = h ? 0x05040100u : 0x05040706u, sb = h ? LL : SW, tie = (lane & 1) ? HH : SW;
                u32x4 w0 = {perm(u.w, v.x, sa), perm(0, u.z, sb), perm(u.y, u.z, sa), perm(0, u.x, sb)};
                u32x4 w1 = {perm(0, v.y, HH), perm(0, v.y, LL), perm(v.x, v.y, 0x07060100u), perm(0, v.x, HH)};
                u32x4 w2 = {perm(0, v.z, HH), perm(0, v.z, tie), perm(0, v.z, LL), perm(0, v.z, LL)};
                u32x4 w3 = {perm(0, v.w, HH), perm(0, v.w, tie), perm(0, v.w, LL), perm(0, v.w, LL)};
                *(LAS u32x4*)kp = w0; *(LAS u32x4*)(kp + 512) = w1; *(LAS u32x4*)(kp + 1024) = w2; *(LAS u32x4*)(kp + 1536) = w3;
                u32x4 z = {0, 0, 0, 0};
                *(LAS u32x4*)(sm + L_KP + kc * KP_CH + (q & 1) * KP_PLANE + a * 16) = z;
            }
            lds_post(sm + F_K + kc * 4, 1);
            TS(21);
            while (nb < 6) { RBAR(); nb++; }
            lds_wait(sm + F_M + (lane & 3) * 4, 9);
            if (!isG) lds_wait(sm + F_L + (lane & 1) * 4, 2);
            w2_dma(IC<V_PACE>(), 2, kc);
            w2_dma(IC<V_PACE>(), 3, kc);
            TS(24);
            asm volatile("s_waitcnt vmcnt(0)" : "+v"(junk) :: "memory");
            lds_post(sm + F_K + kc * 4, 12);
            TS(25);
        } else {
            PRIO(V_LPRIO);
            LOADER_VALS();
            LAS char* fm = sm + F_M + (lane & 3) * 4;
            LAS char* fl = sm + F_L + hw * 4;
            u32 junk = 0;
            TS(40);
            if (__builtin_expect(isG, 1)) { stage(0); RBAR(); }
            const int tb0 = hw * 128 + (lane >> 1);
            u32 ob = (u32)((r >> 2) * 1024 + (8 * g + 2 * (r & 3) + (lane & 1)) * 16), o0 = ob + (u32)tb0 * 8192u;
            u32x4 uv[4], f0, f1; u32x2 pv;
#define LOAD_LNF() asm volatile("global_load_dwordx4 %0, %2, %3 sc1\n\tglobal_load_dwordx4 %1, %2, %3 offset:1024 sc1" : "=&v"(f0), "=&v"(f1) : "v"(lfo), "s"(flags) : "memory")
#define WAIT_LNF() asm volatile("s_waitcnt vmcnt(0)" : "+v"(f0), "+v"(f1), "+v"(junk) :: "memory")
#define LNF_OK() (__builtin_amdgcn_ballot_w64((((f0.x ^ tag1.x) | (f0.z ^ tag1.x) | (f1.x ^ tag1.x) | (f1.z ^ tag1.x)) | ((f0.y ^ tag1.y) | (f0.w ^ tag1.y) | (f1.y ^ tag1.y) | (f1.w ^ tag1.y))) != 0u) == 0)
#define LOAD_U(B, M) do { _Pragma("unroll") for (int i = 0; i < 4; i++) uv[i] = aload_b128_sc1(B, (o0 + i * 32 * 8192) & (M)); pv = aload_b64_sc1(B, (ob + 127u * 8192u + 8u) & (M)); } while (0)
            u32 lfo = (u32)(FL_LN * 128 + lane * 16), vw2 = voffw2;
            asm volatile("" : "+v"(ob), "+v"(o0), "+v"(lfo), "+v"(vw2));
            if (__builtin_expect(isG, 1)) {
                const char* nsrc = chunk_src(1);
#pragma unroll 1
                for (int kc = 1; kc < 8; kc++) {
                    u32 dst = lds0 + (kc & 3) * 32768 + hw * 16384;
                    asm volatile("" : "+s"(nsrc), "+s"(dst));
                    if (kc >= 4) RBAR();
                    TL(kc + 6);
                    glds_run<4, false, V_PACE>(nsrc, lgs, vo0, vo1, vo2, vo3, dst);
                    nsrc = chunk_src(kc + 1);
                    WAITV(16);
                    if (kc < 3) *(volatile LAS u32*)fl = kc;
                    if (kc == 3) READ_TAG();
                }
                RBAR();
                TL(14);
            } else {
                BAR();
                const char* wb = (const char*)p.w1 + (lj < 28 ? lj : 0) * 128 + (size_t)hw * 256 * 3584;
                const u32 v0 = (u32)((lane >> 3) * 3584) + vsw;
                glds_run<8, false, V_PACE>(wb, 32u * 3584u, v0, v0 + 27648u, v0 + 55296u, v0 + 82944u, lds0 + hw * 32768);
                f32x16 c0 = {}, c1 = {};
                const u32x4 ones = {0x3f803f80u, 0x3f803f80u, 0x3f803f80u, 0x3f803f80u};
#pragma unroll 1
                for (int s0 = 0; s0 < 16; s0 += 8) {
                    u32x4 fa[8], fb[8];
                    if (s0 == 0) WAITV(16); else WAITV(0);
#pragma unroll
                    for (int s = 0; s < 8; s++) {
                        LAS char* a = sm + hw * 32768 + (s0 + s) * 2048;
                        fa[s] = trfrag(a + tra0, a + tra0 + 1024); fb[s] = trfrag(a + (tra0 ^ 64), a + (tra0 ^ 64) + 1024);
                    }
                    SCHED();
#pragma unroll
                    for (int s = 0; s < 8; s++) { c0 = MFMA32(ones, fa[s], c0); c1 = MFMA32(ones, fb[s], c1); }
                }
                const float c = h ? c1[0] : c0[0];
                *(LAS float*)(sm + L_MISC + 512 + hw * 256 + lane * 4) = c;
                lds_post(fl, 1);
                READ_TAG();
                lds_wait(sm + F_L + (lane & 1) * 4, 1);
                u32x2 o = {__float_as_uint(*(LAS float*)(sm + L_MISC + 512 + lane * 4) + *(LAS float*)(sm + L_MISC + 768 + lane * 4)), tag1.x};
                if (hw == 0 && lj < 28) astore_b64_sc1(flags, GR_CS + (u32)(lj * 64 + lane) * 8u, o);
                lds_post(fl, 2); lds_wait(fm, 10);
                __builtin_amdgcn_s_sleep(V_SPD);
            }
            LOAD_LNF();
            w2_dma(IC<V_PACE>(), 0, hw);
            WAITV(16);
            if (isG) RBAR();
            TL(15);
            asm volatile("s_waitcnt vmcnt(14)" : "+v"(f0), "+v"(f1) :: "memory");
            const bool uok = LNF_OK();
            { const char* ub = uok ? (const char*)p.ws + WS_U : (const char*)p.b2; LOAD_U(ub, uok ? ~0u : 0u); }
            w2_dma(IC<V_PACE>(), 1, hw);
            TS(24);
            asm volatile("s_waitcnt vmcnt(0)" : "+v"(uv[0]), "+v"(uv[1]), "+v"(uv[2]), "+v"(uv[3]), "+v"(pv) :: "memory");
            lds_post(fl, 12);
            TS(25);
            if (!uok) {
#pragma unroll 1
                for (int spin = 0; spin < (1 << 16); ++spin) {
                    LOAD_LNF(); WAIT_LNF();
                    if (LNF_OK()) break;
                    __builtin_amdgcn_s_sleep(V_PSLEEP);
                }
                TS(22);
                LOAD_U(p.ws + WS_U, ~0u);
                asm volatile("s_waitcnt vmcnt(0)" : "+v"(uv[0]), "+v"(uv[1]), "+v"(uv[2]), "+v"(uv[3]), "+v"(pv) :: "memory");
            }
            if (b == 0 && hw == 0) flag_store(flags, FL_E * 128, tag1);
            PRIO(V_UPRIO);
            lds_wait(fm, 9);
            lds_wait(sm + F_K + (lane & 1) * 4, 1);
            TS(26);
            u32 r2[5], r3[5];
            r2[0] = hw ? pv.x : 0u; r3[0] = hw ? pv.y : 0u;
#pragma unroll
            for (int i = 0; i < 4; i++) {
                r2[i + 1] = __builtin_amdgcn_ds_bpermute(((lane - 2) & 63) * 4, uv[i].z);
                r3[i + 1] = __builtin_amdgcn_ds_bpermute(((lane - 2) & 63) * 4, uv[i].w);
            }
            LAS char* d = sm + L_UC + (lane & 1) * UC_CH + (tb0 + 4) * 16;
#pragma unroll
            for (int i = 0; i < 4; i++) *(LAS u32x4*)(d + i * 512) = uv[i];
#pragma unroll
            for (int i = 0; i < 4; i++) {
                const u32x4 t = uv[i];
                const u32 q2 = lane < 2 ? r2[i] : r2[i + 1], q3 = lane < 2 ? r3[i] : r3[i + 1];
                const u32 a0 = __builtin_amdgcn_alignbit(q3, q2, 16), a1 = __builtin_amdgcn_alignbit(t.x, q3, 16), a2 = __builtin_amdgcn_alignbit(t.y, t.x, 16),
                          a3 = __builtin_amdgcn_alignbit(t.z, t.y, 16), a4 = __builtin_amdgcn_alignbit(t.w, t.z, 16);
                u32x4 c1 = {a1, a2, a3, a4}, c2 = {q3, t.x, t.y, t.z}, c3 = {a0, a1, a2, a3};
                *(LAS u32x4*)(d + i * 512 + UC_COPY) = c1;
                *(LAS u32x4*)(d + i * 512 + 2 * UC_COPY) = c2;
                *(LAS u32x4*)(d + i * 512 + 3 * UC_COPY) = c3;
            }
            u32x4 z = {0, 0, 0, 0};
            *(LAS u32x4*)(sm + L_UC + hw * UC_CH + ((lane >> 2) & 3) * UC_COPY + (lane & 3) * 16) = z;
            lds_post(fl, 13);
            TS(35);
        }
        lds_wait(sm + F_L + (lane & 1) * 4, 13);
        TS(36);
        LAS char* ub = sm + L_UC + cl * UC_CH + (3 - (a & 3)) * UC_COPY + (32 * rh + 2 * q + 5 + (a >> 2)) * 8;
        LAS char* kb = sm + L_KP + cl * KP_CH + ((q + 1) & 1) * KP_PLANE + (16 - 8 * rh - 6 + a - (q >> 1)) * 16;
        f32x4 y[8] = {};
        auto ldA = [&](int dl, int i) {
            const u32x2 lo = *(volatile LAS u32x2*)(ub + dl * 64 + i * 512), hi = *(volatile LAS u32x2*)(ub + dl * 64 + i * 512 + 8);
            return (u32x4){lo.x, lo.y, hi.x, hi.y};
        };
        auto ldB = [&](int dl, int i) { return *(LAS u32x4*)(kb + (3 - dl) * 32 + i * 256); };
        u32x4 A0[8], B0[8], A1[8], B1[8];
#pragma unroll
        for (int i = 0; i < 8; i++) { B0[i] = ldB(0, i); A0[i] = ldA(0, i); }
        lds_wait(sm + F_M + (lane & 3) * 4, 10);
        PRIO(V_CPRIO);
        TS(27);
        auto cstep = [&](u32x4 (&A)[8], u32x4 (&B)[8], u32x4 (&An)[8], u32x4 (&Bn)[8], int dn) {
            asm volatile("" :: "v"(A[0]), "v"(A[1]), "v"(A[2]), "v"(A[3]), "v"(A[4]), "v"(A[5]), "v"(A[6]), "v"(A[7]));
            asm volatile("" :: "v"(B[0]), "v"(B[1]), "v"(B[2]), "v"(B[3]), "v"(B[4]), "v"(B[5]), "v"(B[6]), "v"(B[7]));
            SCHED();
            int n = 0;
#pragma unroll
            for (int gm = 0; gm < 8; gm++)
#pragma unroll
                for (int c = gm; c < 8; c++) {
                    y[c] = MFMA16H(A[gm], B[c - gm], y[c]);
                    if (dn < 4 && n < 24) { if (n % 3 == 0) Bn[n / 3] = ldB(dn, n / 3); else if (n % 3 == 1) An[n / 3] = ldA(dn, n / 3); }
                    n++;
                    SCHED();
                }
        };
        cstep(A0, B0, A1, B1, 1); cstep(A1, B1, A0, B0, 2); cstep(A0, B0, A1, B1, 3); cstep(A1, B1, A0, B0, 4);
        PRIO(3);
        TS(28);
        const float ksc = __builtin_amdgcn_exp2f(fmaxf(*(LAS float*)(sm + L_MISC + cl * 4), *(LAS float*)(sm + L_MISC + 8 + cl * 4)));
        LAS char* fh = sm + F_H + hw * 4;
        LAS char* fp = sm + F_H + (hw ^ 2) * 4;
        lds_post(fh, 1); lds_wait(fp, 1);
        TS(37);
        LAS char* xb = sm + L_UC + cl * UC_CH + lane * 16;
        if (rh) {
#pragma unroll
            for (int c = 0; c < 4; c++) *(LAS f32x4*)(xb + 4096 + c * 1024) = y[c];
            lds_post(fh, 2);
#pragma unroll
            for (int c = 0; c < 4; c++)
#pragma unroll
                for (int e = 0; e < 4; e++) y[c][e] = opq(y[4 + c][e] * ksc);
        } else {
#pragma unroll
            for (int c = 0; c < 4; c++) *(LAS f32x4*)(xb + c * 1024) = y[4 + c];
            lds_post(fh, 2);
#pragma unroll
            for (int c = 0; c < 4; c++)
#pragma unroll
                for (int e = 0; e < 4; e++) y[c][e] = opq(y[c][e] * ksc);
        }
        char* sdst = p.ws + WS_S + (size_t)chn * 4096 + rh * 2048;
        TS(38);
        lds_wait(fp, 2);
        TS(39);
        {
            u32x2 o[4];
#pragma unroll
            for (int c = 0; c < 4; c++) {
                const f32x4 v = *(LAS f32x4*)(xb + (rh ^ 1) * 4096 + c * 1024);
                float f[4];
#pragma unroll
                for (int e = 0; e < 4; e++) f[e] = opq(fmaf(v[e], ksc, y[c][e]));
                o[c] = (u32x2){pkbf(f[0], f[1]), pkbf(f[2], f[3])};
            }
#pragma unroll
            for (int cp = 0; cp < 2; cp++) {
                const auto lo = __builtin_amdgcn_permlane16_swap(o[2 * cp].x, o[2 * cp + 1].x, false, false);
                const auto hi = __builtin_amdgcn_permlane16_swap(o[2 * cp].y, o[2 * cp + 1].y, false, false);
                astore_b128_sc1(sdst, (u32)(cp * 1024 + lane * 16), (u32x4){lo[0], hi[0], lo[1], hi[1]});
            }
        }
        TS(29);
        WAITV(0);
        lds_post(fh, 3);
        lds_wait(sm + F_H + (lane & 3) * 4, 3);
        if (wid == 4) flag_store(flags, (u32)(FL_S + b) * 128u, tag1);
        PRIO(0);
        TS(30);
        xe = *(const u32x4*)((const char*)p.x + xo); b2e = *(const u32x4*)((const char*)p.b2 + dt * 128 + (tid & 7) * 16);
        __builtin_amdgcn_s_sleep(V_SDELAY);
        poll_flags(flags, FL_S + dt + 8 * hw, 32, 8, tag1, lane);
        TS(31);
        u32x4 sv[2];
#pragma unroll
        for (int i = 0; i < 2; i++) {
            const int d = hw * 16 + i * 8 + (lane >> 3);
            sv[i] = aload_b128_sc1(p.ws + WS_S, (u32)((dt * 64 + d) * 4096 + (tt >> 3) * 1024 + ((tt >> 2) & 1) * 256 + ((lane >> 2) & 1) * 512 + (tt & 3) * 64 + (lane & 3) * 16));
        }
        asm volatile("s_waitcnt vmcnt(0)" : "+v"(sv[0]), "+v"(sv[1]) :: "memory");
#pragma unroll
        for (int i = 0; i < 2; i++) {
            const int d = hw * 16 + i * 8 + (lane >> 3);
            *(LAS u32x4*)(sm + L_UC + d * 128 + (((2 * (lane & 3) + ((lane >> 2) & 1)) ^ (d >> 3)) << 4)) = sv[i];
        }
        TS(32);
    }
    {
        LANEVALS();
        const int tok = tid >> 3, dg = tid & 7;
        float m[8], xf[8];
#pragma unroll
        for (int e = 0; e < 4; e++) { m[2 * e] = bflo(b2e[e]); m[2 * e + 1] = bfhi(b2e[e]); xf[2 * e] = bflo(xe[e]); xf[2 * e + 1] = bfhi(xe[e]); }
        u32 pa = (u32)(L_W2 + tok * 272 + dg * 32), pb = pa + 2 * W2_REG, sa = (u32)(L_UC + dg * 1024 + (((tok >> 3) ^ dg) << 4) + (tok & 7) * 2), oo = xo;
        asm volatile("" : "+v"(m[0]), "+v"(m[1]), "+v"(m[2]), "+v"(m[3]), "+v"(m[4]), "+v"(m[5]), "+v"(m[6]), "+v"(m[7]), "+v"(pa), "+v"(pb), "+v"(sa), "+v"(oo));
        asm volatile("" : "+v"(xf[0]), "+v"(xf[1]), "+v"(xf[2]), "+v"(xf[3]), "+v"(xf[4]), "+v"(xf[5]), "+v"(xf[6]), "+v"(xf[7]));
        BAR();
        TS(33);
#pragma unroll
        for (int i = 0; i < 4; i++) {
            LAS char* q = sm + (i < 2 ? pa : pb) + (i & 1) * W2_REG;
            const f32x4 lo = *(LAS f32x4*)q, hi = *(LAS f32x4*)(q + 16);
#pragma unroll
            for (int e = 0; e < 4; e++) { m[e] += lo[e]; m[4 + e] += hi[e]; }
        }
        float o[8];
#pragma unroll
        for (int e = 0; e < 8; e++) {
            const float s = bf2f(*(LAS u16*)(sm + sa + e * 128));
            o[e] = fmaf(s * m[e], __builtin_amdgcn_rcpf(1.f + __builtin_amdgcn_exp2f(-1.4426950408889634f * m[e])), xf[e]);
        }
        u32x4 ov = {pkbf(o[0], o[1]), pkbf(o[2], o[3]), pkbf(o[4], o[5]), pkbf(o[6], o[7])};
        if (V_OST == 0) *(u32x4*)((char*)p.out + oo) = ov;
        else if (V_OST == 1) astore_b128_sc1(p.out, oo, ov);
        else __builtin_nontemporal_store(ov, (u32x4*)((char*)p.out + oo));
    }
    TS(34); TR(63);
    TSOUT();
}

extern "C" void kernel_launch(void* const* d_in, const int* in_sizes, int n_in, void* d_out, int out_size, void* d_ws, size_t ws_size,
                              hipStream_t stream) {
    Params p;
    p.x = (const u16*)d_in[0]; p.Are = (const u16*)d_in[1]; p.Aim = (const u16*)d_in[2]; p.Bre = (const u16*)d_in[3]; p.Bim = (const u16*)d_in[4];
    p.Cre = (const u16*)d_in[5]; p.Cim = (const u16*)d_in[6]; p.logdt = (const u16*)d_in[7]; p.Dskip = (const u16*)d_in[8];
    p.w1 = (const u16*)d_in[10]; p.b1 = (const u16*)d_in[11]; p.w2 = (const u16*)d_in[12]; p.b2 = (const u16*)d_in[13];
    p.out = (u16*)d_out; p.ws = (char*)d_ws;
    fused_kernel<<<256, 512, 0, stream>>>(p);
}
```

```cpp
#include <hip/hip_runtime.h>
#include <stdint.h>

typedef unsigned short u16;
typedef unsigned u32;
typedef unsigned long long u64;
typedef u32 u32x2 __attribute__((ext_vector_type(2)));
typedef u32 u32x4 __attribute__((ext_vector_type(4)));
typedef short s16x4 __attribute__((ext_vector_type(4)));
typedef __bf16 bf16x2 __attribute__((ext_vector_type(2)));
typedef __bf16 bf16x8 __attribute__((ext_vector_type(8)));
typedef _Float16 f16x2 __attribute__((ext_vector_type(2)));
typedef _Float16 f16x8 __attribute__((ext_vector_type(8)));
typedef float f32x2 __attribute__((ext_vector_type(2)));
typedef float f32x4 __attribute__((ext_vector_type(4)));
typedef float f32x16 __attribute__((ext_vector_type(16)));
#define LAS __attribute__((address_space(3)))
#define DEV __device__ __forceinline__

template <int N> struct IC { static constexpr int value = N; };

constexpr u32 CODE_LINES = 240;
constexpr int LTHR = 16, MTHR = 10;
constexpr int WIN = 6;

constexpr int FL_S = 32, FL_E = 288, FL_H = 320, FL_LN = 1344;
constexpr u32 GR_ST = 256u << 10, GR_CS = 320u << 10;
constexpr size_t WS_U = 8u << 20;
constexpr size_t WS_S = 10u << 20;
constexpr size_t WS_H = 12u << 20;

constexpr int L_W2 = 0, W2_REG = 28672;
constexpr int L_UC = 114688, UC_COPY = 4160, UC_CH = 4 * UC_COPY + 64;
constexpr int L_TAB = 131072, TAB_ROW = 288, TAB_SZ = 6144;
constexpr int L_MISC = 149504;
constexpr int L_KP = 150784, KP_PLANE = 2304, KP_CH = 2 * KP_PLANE;
constexpr int F_M = L_MISC + 64, F_L = L_MISC + 80, F_K = L_MISC + 88, F_H = L_MISC + 96, F_P = L_MISC + 160;
constexpr int L_TAG = L_MISC + 112, L_JUNK = L_MISC + 256, L_CS = L_MISC + 512;
DEV int tab_off(int w) { return w == 3 ? L_KP + KP_CH : L_TAB + w * TAB_SZ; }

struct Params {
    const u16 *x, *Are, *Aim, *Bre, *Bim, *Cre, *Cim, *logdt, *Dskip, *w1, *b1, *w2, *b2;
    u16* out;
    char* ws;
};

DEV float bf2f(u32 v) { return __uint_as_float(v << 16); }
DEV float bflo(u32 v) { return __uint_as_float(v << 16); }
DEV float bfhi(u32 v) { return __uint_as_float(v & 0xffff0000u); }
DEV u32 pkbf(float a, float b) { f32x2 f = {a, b}; return __builtin_bit_cast(u32, __builtin_convertvector(f, bf16x2)); }
DEV u32 pkh(float a, float b) { f32x2 f = {a, b}; return __builtin_bit_cast(u32, __builtin_convertvector(f, f16x2)); }
DEV float opq(float a) { asm("" : "+v"(a)); return a; }
DEV float rbf(float a) { return bflo(pkbf(a, a)); }
DEV float dot2(u32 a, u32 b, float c) { return __builtin_amdgcn_fdot2_f32_bf16(__builtin_bit_cast(bf16x2, a), __builtin_bit_cast(bf16x2, b), c, false); }

#define WAITV(n) asm volatile("s_waitcnt vmcnt(" #n ")" ::: "memory")
#define WAITL0() asm volatile("s_waitcnt lgkmcnt(0)" ::: "memory")
#define BAR() do { asm volatile("s_waitcnt lgkmcnt(0)" ::: "memory"); __builtin_amdgcn_s_barrier(); asm volatile("" ::: "memory"); } while (0)
#define RBAR() do { asm volatile("" ::: "memory"); __builtin_amdgcn_s_barrier(); asm volatile("" ::: "memory"); } while (0)
#define SCHED() __builtin_amdgcn_sched_barrier(0)
#define PRIO(n) __builtin_amdgcn_s_setprio(n)
#define MFMA32(a, b, c) __builtin_amdgcn_mfma_f32_32x32x16_bf16(__builtin_bit_cast(bf16x8, a), __builtin_bit_cast(bf16x8, b), c, 0, 0, 0)
#define MFMA16H(a, b, c) __builtin_amdgcn_mfma_f32_16x16x32_f16(__builtin_bit_cast(f16x8, a), __builtin_bit_cast(f16x8, b), c, 0, 0, 0)

DEV const void* uni(const void* q) {
    const u64 a = (u64)q;
    return (const void*)(((u64)(u32)__builtin_amdgcn_readfirstlane((u32)(a >> 32)) << 32) | (u32)__builtin_amdgcn_readfirstlane((u32)a));
}
#define GL4(B) "s_waitcnt vmcnt(%c[p])\n\tglobal_load_lds_dwordx4 %[v0], " B "\n\tglobal_load_lds_dwordx4 %[v1], " B " offset:1024\n\t" \
               "global_load_lds_dwordx4 %[v2], " B " offset:2048\n\tglobal_load_lds_dwordx4 %[v3], " B " offset:3072\n\t"
template <int NG, bool HALF, int THR> DEV void glds_run(const void* base, u32 step, u32 v0, u32 v1, u32 v2, u32 v3, u32 dst) {
    u32 keep;
    dst = __builtin_amdgcn_readfirstlane(dst);
    base = uni(base);
    asm volatile("s_mov_b32 %[k], m0\n\ts_mov_b32 m0, %[d]\n\ts_mov_b64 vcc, %[b]\n\ts_nop 1\n\t" GL4("vcc")
                 ".rept %c[n]\n\ts_add_u32 m0, m0, 0x1000\n\ts_add_u32 vcc_lo, vcc_lo, %[s]\n\ts_addc_u32 vcc_hi, vcc_hi, 0\n\t" GL4("vcc") ".endr\n\t"
                 ".if %c[h]\n\ts_add_u32 m0, m0, 0x1000\n\ts_add_u32 vcc_lo, vcc_lo, %[s]\n\ts_addc_u32 vcc_hi, vcc_hi, 0\n\t"
                 "global_load_lds_dwordx4 %[v0], vcc\n\tglobal_load_lds_dwordx4 %[v1], vcc offset:1024\n\t.endif\n\t"
                 "s_mov_b32 m0, %[k]"
                 : [k] "=&s"(keep) : [v0] "v"(v0), [v1] "v"(v1), [v2] "v"(v2), [v3] "v"(v3), [b] "s"(base), [d] "s"(dst), [n] "i"(NG - 1), [s] "s"(step), [h] "i"(HALF ? 1 : 0), [p] "i"(THR)
                 : "memory", "scc", "vcc");
}
DEV void touch(const void* base, u32 voff, u32 junk) {
    u32 keep;
    base = uni(base);
    asm volatile("s_mov_b32 %0, m0\n\ts_mov_b32 m0, %3\n\ts_nop 2\n\tglobal_load_lds_dword %1, %2\n\ts_mov_b32 m0, %0"
                 : "=&s"(keep) : "v"(voff), "s"(base), "s"(junk) : "memory");
}
DEV u32x2 aload_b64_sc1(const void* base, u32 voff) { u32x2 r; asm volatile("global_load_dwordx2 %0, %1, %2 sc1" : "=v"(r) : "v"(voff), "s"(base) : "memory"); return r; }
DEV u32x4 aload_b128_sc1(const void* base, u32 voff) { u32x4 r; asm volatile("global_load_dwordx4 %0, %1, %2 sc1" : "=v"(r) : "v"(voff), "s"(base) : "memory"); return r; }
DEV void astore_b128_sc1(void* base, u32 voff, u32x4 v) { asm volatile("global_store_dwordx4 %0, %1, %2 sc1\n\ts_nop 1" :: "v"(voff), "v"(v), "s"(base) : "memory"); }
DEV void astore_b64_sc1(void* base, u32 voff, u32x2 v) { asm volatile("global_store_dwordx2 %0, %1, %2 sc1\n\ts_nop 1" :: "v"(voff), "v"(v), "s"(base) : "memory"); }
DEV void flag_store(void* base, u32 off, u32x2 v) {
    u64 keep;
    asm volatile("s_mov_b64 %0, exec\n\ts_mov_b64 exec, 1\n\tglobal_store_dwordx2 %1, %2, %3 sc1\n\ts_mov_b64 exec, %0\n\ts_nop 0"
                 : "=&s"(keep) : "v"(off), "v"(v), "s"(base) : "memory");
}
DEV u32x4 trfrag(LAS char* a, LAS char* b) {
    const u32x2 lo = __builtin_bit_cast(u32x2, __builtin_amdgcn_ds_read_tr16_b64_v4i16((LAS s16x4*)a));
    const u32x2 hi = __builtin_bit_cast(u32x2, __builtin_amdgcn_ds_read_tr16_b64_v4i16((LAS s16x4*)b));
    return (u32x4){lo.x, lo.y, hi.x, hi.y};
}

DEV void poll_flags(const char* flags, int first, int stride, int nfl, u32x2 tag, int lane) {
    const u32 off = (u32)(first + (lane < nfl ? lane : 0) * stride) * 128u;
#pragma unroll 1
    for (int spin = 0; spin < (1 << 16); ++spin) {
        u32x2 v = aload_b64_sc1(flags, off);
        asm volatile("s_waitcnt vmcnt(0)" : "+v"(v) :: "memory");
        if (__builtin_amdgcn_ballot_w64(v.x != tag.x || v.y != tag.y) == 0) break;
        __builtin_amdgcn_s_sleep(6);
    }
}
DEV bool all_ge(u32 v, u32 target) { return __builtin_amdgcn_ballot_w64(v < target) == 0; }
DEV void lds_wait(LAS char* w, u32 target) {
#pragma unroll 1
    for (int spin = 0; spin < (1 << 21); ++spin) {
        if (all_ge(*(volatile LAS u32*)w, target)) break;
        __builtin_amdgcn_s_sleep(1);
    }
    asm volatile("" ::: "memory");
}
DEV void lds_post(LAS char* w, u32 v) { WAITL0(); *(volatile LAS u32*)w = v; }
template <int CTRL, int RM> DEV float dpp_add(float v) {
    return v + __builtin_bit_cast(float, __builtin_amdgcn_update_dpp(0, __builtin_bit_cast(int, v), CTRL, RM, 0xf, false));
}
template <int CTRL, int RM> DEV u32 dpp_umax(u32 v) { const u32 o = __builtin_amdgcn_update_dpp(0u, v, CTRL, RM, 0xf, false); return v > o ? v : o; }
DEV u32 wave_umax(u32 v) {
    v = dpp_umax<0xB1, 0xf>(v); v = dpp_umax<0x4E, 0xf>(v); v = dpp_umax<0x141, 0xf>(v); v = dpp_umax<0x140, 0xf>(v);
    v = dpp_umax<0x142, 0xa>(v); v = dpp_umax<0x143, 0xc>(v);
    return __builtin_amdgcn_readlane(v, 63);
}
DEV f32x2 cexp_f(float e2, float rev) {
    const float m = __builtin_amdgcn_exp2f(e2);
    return (f32x2){m * __builtin_amdgcn_cosf(rev), m * __builtin_amdgcn_sinf(rev)};
}
DEV f32x2 cmul_f(f32x2 a, f32x2 z) {
    f32x2 t, r;
    asm("v_pk_mul_f32 %0, %1, %2 op_sel_hi:[1,0]" : "=v"(t) : "v"(a), "v"(z));
    asm("v_pk_fma_f32 %0, %1, %2, %3 op_sel:[1,1,0] op_sel_hi:[0,1,1] neg_lo:[0,1,0]" : "=v"(r) : "v"(a), "v"(z), "v"(t));
    return r;
}
template <bool CONJ> DEV u32 cmh1(u32 a, u32 z) {
    u32 t;
    if (CONJ) asm("v_pk_mul_f16 %0, %1, %2 op_sel_hi:[1,0] neg_hi:[1,0]" : "=v"(t) : "v"(a), "v"(z));
    else asm("v_pk_mul_f16 %0, %1, %2 op_sel_hi:[1,0]" : "=v"(t) : "v"(a), "v"(z));
    return t;
}
template <bool CONJ> DEV u32 cmh2(u32 a, u32 z, u32 t) {
    u32 r;
    if (CONJ) asm("v_pk_fma_f16 %0, %1, %2, %3 op_sel:[1,1,0] op_sel_hi:[0,1,1] neg_lo:[0,1,0] neg_hi:[0,1,0]" : "=v"(r) : "v"(a), "v"(z), "v"(t));
    else asm("v_pk_fma_f16 %0, %1, %2, %3 op_sel:[1,1,0] op_sel_hi:[0,1,1] neg_lo:[0,1,0]" : "=v"(r) : "v"(a), "v"(z), "v"(t));
    return r;
}
DEV void build_table(LAS char* sm, int w, int lane, u32 are, u32 aim, u32 bre, u32 bim, u32 cre, u32 cim, u32 ldt, u32 dsk) {
    LAS char* t = sm + tab_off(w) + lane * 4;
    *(LAS u32*)(sm + L_MISC + 16 + w * 4) = dsk << 16;
    auto put = [&](int row, f32x2 v) { *(LAS u32*)(t + row * TAB_ROW) = pkh(v.x, v.y); };
    const float dtv = rbf(__builtin_amdgcn_exp2f(bf2f(ldt) * 1.4426950408889634f));
    const float a2 = dtv * bf2f(are) * 1.4426950408889634f;
    float rev = dtv * bf2f(aim) * 0.15915494309189535f;
    rev -= __builtin_rintf(rev);
    f32x2 wv = cmul_f((f32x2){bf2f(cre), bf2f(cim)}, (f32x2){bf2f(bre), bf2f(bim)});
    u32 E = wave_umax(__float_as_uint(fmaxf(dtv * fmaxf(fabsf(wv.x), fabsf(wv.y)) * __builtin_amdgcn_exp2f(fmaxf(a2, 0.f) * 2048.f), fabsf(bf2f(dsk)) * 0.0078125f))) >> 23;
    E = E < 16u ? 16u : (E > 236u ? 236u : E);
    wv *= dtv * __uint_as_float((260u - E) << 23);
    *(LAS float*)(sm + L_MISC + w * 4) = (float)(int)E - 133.f;
    const f32x2 z1 = cexp_f(a2, rev), z64 = cexp_f(a2 * 64.f, rev * 64.f);
    const f32x2 z2 = cmul_f(z1, z1), z3 = cmul_f(z2, z1), z4 = cmul_f(z2, z2), z8 = cmul_f(z4, z4), z16 = cmul_f(z8, z8), z32 = cmul_f(z16, z16);
    const f32x2 z128 = cmul_f(z64, z64), z256 = cmul_f(z128, z128), z512 = cmul_f(z256, z256);
    const f32x2 w1 = cmul_f(wv, z512), w2 = cmul_f(w1, z512);
    *(LAS u32*)t = 0x00003c00u;
    put(1, z1); put(2, z2); put(3, z3); put(4, z4); put(5, cmul_f(z4, z1)); put(6, cmul_f(z3, z3)); put(7, cmul_f(z4, z3)); put(8, z8);
    put(9, z16); put(10, cmul_f(z16, z8)); put(11, z32); put(12, z64); put(13, cmul_f(z64, z32));
    put(14, z128); put(15, z256); put(16, cmul_f(z256, z128));
    put(17, wv); put(18, w1); put(19, w2); put(20, cmul_f(w2, z512));
}
DEV u32 perm(u32 hi, u32 lo, u32 sel) { return __builtin_amdgcn_perm(hi, lo, sel); }

__global__ __launch_bounds__(512, 2) void fused_kernel(Params p) {
    __shared__ __attribute__((aligned(1024))) char shm[163840];
    LAS char* sm = (LAS char*)shm;
    const u32 lds0 = (u32)(uintptr_t)sm;
    const int wid = __builtin_amdgcn_readfirstlane(threadIdx.x >> 6);
    const int b = blockIdx.x, g = b & 7, r = b >> 3, hw = wid & 3;
    char* flags = p.ws;
    const int ch0 = 64 * g + 16 * (r & 3) + (r >> 2);
    const int tt = g * 4 + (r >> 3), dt = r & 7;
    const bool isG = r < 28;
    const int mt = (g >> 1) * 4 + r / 7, ft = (g & 1) * 7 + r % 7;
#define LANEVALS() \
    int tid = threadIdx.x; asm volatile("" : "+v"(tid)); \
    const int lane = tid & 63, h = lane >> 5, l31 = lane & 31; \
    const u32 vsw = (u32)(((lane & 7) ^ (((lane >> 4) & 1) << 2)) << 4);          \
    const u32 voffw2 = (u32)((lane >> 3) * 1024 + dt * 128) + vsw; \
    auto w2_dma = [&](auto thr, int kq, int sg) {        \
        glds_run<3, true, decltype(thr)::value>((const char*)p.w2 + (size_t)(4 * kq + sg) * (112 * 1024), 32768u, voffw2, voffw2 + 7168u, voffw2 + 14336u, voffw2 + 21504u, \
                                 lds0 + L_W2 + kq * W2_REG + (sg & 1) * 14336); \
    }; \
    const int blk = (lane >> 4) & 1, q4 = (lane & 15) >> 2, pp = lane & 3; \
      \
    const u32 tra0 = (u32)((4 * h + q4) * 128 + (((2 * blk + (pp >> 1)) ^ (((q4 >> 1) & 1) << 2)) << 4) + 8 * (pp & 1)); \
    const u32 xo = (u32)((tt * 64 + (tid >> 3)) * 1024 + dt * 128 + (tid & 7) * 16); \
    (void)l31; (void)voffw2; (void)w2_dma; (void)tra0; (void)xo;
    if (wid == 6) { const u32 ln = (u32)(r + 32 * (threadIdx.x & 7)); touch((const char*)(__builtin_amdgcn_s_getpc() & ~127ull), (ln < CODE_LINES ? ln : 0u) * 128u, lds0 + L_JUNK); }
    if (wid == 0) *(LAS u32*)(sm + L_MISC + 64 + (threadIdx.x & 31) * 4) = 0;
    u32x2 tag1;
    u32x4 xe, b2e;
#define WAIT_PARAMS(n) asm volatile("s_waitcnt vmcnt(" #n ")" : "+v"(are), "+v"(aim), "+v"(bre), "+v"(bim), "+v"(cre), "+v"(cim), "+v"(ldt), "+v"(dsk) :: "memory")
#define LOAD_PARAMS(po, c2) \
    asm volatile("global_load_ushort %0, %8, %10\n\tglobal_load_ushort %1, %8, %11\n\tglobal_load_ushort %2, %8, %12\n\tglobal_load_ushort %3, %8, %13\n\t" \
                 "global_load_ushort %4, %8, %14\n\tglobal_load_ushort %5, %8, %15\n\tglobal_load_ushort %6, %9, %16\n\tglobal_load_ushort %7, %9, %17" \
                 : "=&v"(are), "=&v"(aim), "=&v"(bre), "=&v"(bim), "=&v"(cre), "=&v"(cim), "=&v"(ldt), "=&v"(dsk) \
                 : "v"(po), "v"(c2), "s"(p.Are), "s"(p.Aim), "s"(p.Bre), "s"(p.Bim), "s"(p.Cre), "s"(p.Cim), "s"(p.logdt), "s"(p.Dskip) : "memory")
#define LOADER_VALS() \
    const bool lx = wid == 4;                  \
    const u32 lR = lx ? (u32)(lane >> 3) * 1024u : (u32)(lane >> 4) * 3584u; \
    const u32 lS = lx ? (u32)(((lane & 7) ^ (lane >> 4)) << 4) : (u32)(((lane & 15) ^ ((lane >> 4) << 2)) << 4); \
    const u32 lst = lx ? 7168u : 13312u, x1 = lx ? 64u : 16u, x2 = lx ? 0u : 32u; \
    const u32 vo0 = lR + lS, vo1 = lR + (lS ^ x1) + lst, vo2 = lR + (lS ^ x2) + 2 * lst, vo3 = lR + (lS ^ (x1 | x2)) + 3 * lst; \
    const char* lsrc = lx ? (const char*)p.x + (size_t)mt * 128 * 1024 : (const char*)p.w1 + ft * 256; \
    const u32 lgs = lx ? 32768u : 16u * 3584u, lcs = lx ? 128u : 64u * 3584u;
#define READ_TAG() do { u32x4 t_ = *(volatile LAS u32x4*)(sm + L_TAG); \
        if (!all_ge(t_.y, 1)) { lds_wait(sm + L_TAG + 4, 1); t_ = *(volatile LAS u32x4*)(sm + L_TAG); } \
        tag1.x = __builtin_amdgcn_readfirstlane(t_.z); tag1.y = __builtin_amdgcn_readfirstlane(t_.w); } while (0)
    const int lj = g * 4 + (r - 28);

    const int tb8 = g * 32 + r;

    if (wid < 4) {
        LANEVALS();
        const int kq = wid;
        u32x4 wreg[14][2];
        LAS char* w2a = sm + L_W2 + kq * W2_REG;
        auto w2frag = [&](int s, int j2) { LAS char* a = w2a + (j2 ? tra0 ^ 64 : tra0) + s * 2048; return trfrag(a, a + 1024); };
        auto w2load = [&]() {
#pragma unroll
            for (int s = 0; s < 14; s++) { wreg[s][0] = w2frag(s, 0); wreg[s][1] = w2frag(s, 1); }
        };
#define W2PIN() _Pragma("unroll") for (int s = 0; s < 14; s++) asm volatile("" : "+v"(wreg[s][0]), "+v"(wreg[s][1]))
        LAS char* fw = sm + F_L + (wid & 2) * 4 + (lane & 1) * 4;
        bool early = false;
        const int th = wid & 1, fh = wid >> 1;
        u32 are, aim, bre, bim, cre, cim, ldt, dsk, b1a, b1b;
        {
            const int chp = ch0 + 8 * th;
            const u32 po = (u32)(chp * 128 + fh * 64 + lane) * 2u, c2 = (u32)chp * 2u, bo = (u32)(ft * 128 + fh * 64 + l31) * 2u;
            if (wid < 3) LOAD_PARAMS(po, c2);
            asm volatile("global_load_ushort %0, %2, %3\n\tglobal_load_ushort %1, %2, %3 offset:64" : "=&v"(b1a), "=&v"(b1b) : "v"(bo), "s"(p.b1) : "memory");

            BAR();
            SCHED();
        }
        u32 xa[4], wa[2][2];
#pragma unroll
        for (int s = 0; s < 4; s++) xa[s] = (u32)((64 * th + l31) * 128 + (((2 * s + h) ^ ((l31 >> 1) & 7)) << 4));
#pragma unroll
        for (int j2 = 0; j2 < 2; j2++)
#pragma unroll
            for (int t = 0; t < 2; t++)
                wa[j2][t] = (u32)(16384 + 256 * (8 * h + 4 * t + q4) + (((4 * (2 * fh + j2) + 2 * blk + (pp >> 1)) ^ ((q4 << 2) | (2 * h + t))) << 4) + 8 * (pp & 1));
        u32 so = GR_ST + (u32)(mt * 128 + th * 64 + l31) * 16u, co = GR_CS + (u32)(ft * 128 + fh * 64 + l31) * 8u;
        asm volatile("" : "+v"(so), "+v"(co), "+v"(xa[0]), "+v"(xa[1]), "+v"(xa[2]), "+v"(xa[3]), "+v"(wa[0][0]), "+v"(wa[0][1]), "+v"(wa[1][0]), "+v"(wa[1][1]));
        WAIT_PARAMS(2);

        if (wid < 3) build_table(sm, wid, lane, are, aim, bre, bim, cre, cim, ldt, dsk);
        LAS char* fm = sm + F_M + wid * 4;
        LAS char* fl = sm + F_L + (lane & 1) * 4;
        lds_post(fm, 1);

        if (isG) {
            f32x16 acc[2][2] = {};
            lds_wait(fl, 1);

            u32x4 X[4][2], W[4][2];
            u32 wp[2][2], xp;
#define RX(s, j) X[s][j] = *(LAS u32x4*)(sm + xp + (j) * 4096)
#define RW(s, j) W[s][j] = trfrag(sm + wp[j][0] + (s) * 4096, sm + wp[j][1] + (s) * 4096)
#define OPQ(v) asm volatile("" : "+v"(v))
#define MM(s, j2, j) acc[j2][j] = MFMA32(W[s][j2], X[s][j], acc[j2][j])
            u32x4 st0, st1; u32x2 cs0, cs1;
#define LOAD_GRAN() asm volatile("global_load_dwordx4 %0, %4, %6 sc1\n\tglobal_load_dwordx4 %1, %4, %6 offset:512 sc1\n\tglobal_load_dwordx2 %2, %5, %6 sc1\n\tglobal_load_dwordx2 %3, %5, %6 offset:256 sc1" \
                                 : "=&v"(st0), "=&v"(st1), "=&v"(cs0), "=&v"(cs1) : "v"(so), "v"(co), "s"(flags) : "memory")
            wp[0][0] = wa[0][0]; wp[0][1] = wa[0][1]; wp[1][0] = wa[1][0]; wp[1][1] = wa[1][1];
            xp = xa[0]; RX(0, 0); SCHED(); RW(0, 0); SCHED(); RW(0, 1); SCHED(); RX(0, 1); SCHED();
            xp = xa[1]; RX(1, 0); SCHED(); RW(1, 0); SCHED(); RW(1, 1); SCHED(); RX(1, 1); SCHED();
            xp = xa[2];
#pragma unroll
            for (int kc = 0; kc < 7; kc++) {
                const u32 sl = (u32)(kc & 3) * 32768u, sn = (u32)((kc + 1) & 3) * 32768u;
                if (kc == 5) LOAD_GRAN();
                MM(0, 0, 0); RX(2, 0); SCHED(); MM(0, 1, 0); RW(2, 0); SCHED(); MM(0, 0, 1); RW(2, 1); SCHED();
                MM(0, 1, 1); RX(2, 1); xp = xa[3] + sl; OPQ(xp); SCHED();
                MM(1, 0, 0); RX(3, 0);
                SCHED();
                MM(1, 1, 0); RW(3, 0); SCHED(); MM(1, 0, 1); RW(3, 1); SCHED();
                MM(1, 1, 1); RX(3, 1); xp = xa[0] + sn; wp[0][0] = wa[0][0] + sn; wp[0][1] = wa[0][1] + sn; OPQ(xp); OPQ(wp[0][0]); OPQ(wp[0][1]); SCHED();
                MM(2, 0, 0); SCHED();
                if (kc == 0) lds_wait(fl, 2); else RBAR();
                SCHED();
                RX(0, 0); wp[1][0] = wa[1][0] + sn; wp[1][1] = wa[1][1] + sn; OPQ(wp[1][0]); OPQ(wp[1][1]); SCHED();
                MM(2, 1, 0); RW(0, 0); SCHED(); MM(2, 0, 1); RW(0, 1); SCHED();
                MM(2, 1, 1); RX(0, 1); xp = xa[1] + sn; OPQ(xp); SCHED();
                MM(3, 0, 0); RX(1, 0); SCHED(); MM(3, 1, 0); RW(1, 0); SCHED(); MM(3, 0, 1); RW(1, 1); SCHED();
                MM(3, 1, 1); RX(1, 1); xp = xa[2] + sn; OPQ(xp);
                SCHED();

            }
            MM(0, 0, 0); RX(2, 0); RW(2, 0); SCHED(); MM(0, 1, 0); RW(2, 1); RX(2, 1); xp = xa[3] + 3 * 32768; SCHED();
            MM(0, 0, 1); RX(3, 0); RW(3, 0); SCHED(); MM(0, 1, 1); RW(3, 1); RX(3, 1); SCHED();
            MM(1, 0, 0); MM(1, 1, 0); SCHED();
            READ_TAG();
            lds_post(fm, 9);
            asm volatile("s_waitcnt vmcnt(0)" : "+v"(st0), "+v"(st1), "+v"(cs0), "+v"(cs1), "+v"(b1a), "+v"(b1b) :: "memory");
#pragma unroll 1
            for (int spin = 0; spin < (1 << 16); ++spin) {
                if (__builtin_amdgcn_ballot_w64((((st0.y ^ tag1.x) | (st0.w ^ tag1.x) | (st1.y ^ tag1.x)) | ((st1.w ^ tag1.x) | (cs0.y ^ tag1.x) | (cs1.y ^ tag1.x))) != 0u) == 0) break;
                __builtin_amdgcn_s_sleep(2);
                LOAD_GRAN();
                asm volatile("s_waitcnt vmcnt(0)" : "+v"(st0), "+v"(st1), "+v"(cs0), "+v"(cs1) :: "memory");
            }

            float rinv[2] = {__uint_as_float(st0.z), __uint_as_float(st1.z)};
            float k1[2], k2[2];
            {
                u32x4 Ax[2], Bx[2];
                auto prepB = [&](int j) {
                    const float nm = __uint_as_float(j ? st1.x : st0.x), m1 = rbf(nm), mr = opq(nm - m1), m2 = rbf(mr), ih = rbf(rinv[j]);
                    const u32 mm = pkbf(m1, m2);
                    const u32 hm = (u32)h - 1u;
                    Bx[j] = (u32x4){mm & hm, mm & hm, pkbf(opq(mr - m2), m1) & hm, pkbf(ih, opq(rinv[j] - ih)) & hm};
                };
                auto prepA = [&](int j) {
                    const float c = __uint_as_float(j ? cs1.x : cs0.x), c1 = rbf(c), cr = opq(c - c1), c2 = rbf(cr);
                    const u32 bb = j ? b1b : b1a;
                    Ax[j] = (u32x4){pkbf(c1, c1), pkbf(c2, c2), pkbf(c1, opq(cr - c2)), bb | (bb << 16)};
                };
                auto prepK = [&](int j) {
                    const float rr = __builtin_amdgcn_rcpf(rinv[j]);
                    k1[j] = opq(-2.302208198144325f * rr); k2[j] = opq(-0.10294323958002349f * rr * rr * rr);
                };
                MM(1, 0, 1); MM(1, 1, 1); prepB(0); SCHED();
                MM(2, 0, 0); prepA(0); SCHED(); MM(2, 1, 0); prepA(1); SCHED(); MM(2, 0, 1); MM(2, 1, 1); prepB(1); SCHED();
                MM(3, 0, 0); prepK(0); SCHED(); acc[0][0] = MFMA32(Ax[0], Bx[0], acc[0][0]); prepK(1); SCHED();
                MM(3, 1, 0); acc[1][0] = MFMA32(Ax[1], Bx[0], acc[1][0]);
                MM(3, 0, 1); acc[0][1] = MFMA32(Ax[0], Bx[1], acc[0][1]);
                MM(3, 1, 1); acc[1][1] = MFMA32(Ax[1], Bx[1], acc[1][1]);
                SCHED();

            }
            PRIO(2);

            char* hdst = p.ws + WS_H + (size_t)((mt * 4 + th * 2) * 112 + ft * 8 + fh * 4) * 1024;
#pragma unroll
            for (int j = 0; j < 2; j++) {
                const f32x2 K1 = {k1[j], k1[j]}, K2 = {k2[j], k2[j]}, RI = {rinv[j], rinv[j]};
#pragma unroll
                for (int j2 = 0; j2 < 2; j2++) {
#pragma unroll
                    for (int s = 0; s < 2; s++) {
                        u32x4 o;
#pragma unroll
                        for (int i = 0; i < 4; i++) {
                            const f32x2 u = {acc[j2][j][8 * s + 2 * i], acc[j2][j][8 * s + 2 * i + 1]};
                            const f32x2 z = __builtin_elementwise_fma(u * u, K2, K1) * u;
                            const f32x2 e = {__builtin_amdgcn_exp2f(z.x), __builtin_amdgcn_exp2f(z.y)};
                            const f32x2 dn = __builtin_elementwise_fma(e, RI, RI);
                            const f32x2 gl = u * (f32x2){__builtin_amdgcn_rcpf(dn.x), __builtin_amdgcn_rcpf(dn.y)};
                            o[i] = pkbf(gl.x, gl.y);
                        }
                        asm volatile("global_store_dwordx4 %0, %1, %2 offset:%3 sc1\n\ts_nop 1" :: "v"(lane * 16 + j * 112 * 1024), "v"(o), "s"(hdst), "i"((j2 * 2 + s) * 1024) : "memory");
                        SCHED();
                        if (j == 1 && j2 == 1 && s == 0) { early = all_ge(*(volatile LAS u32*)fw, 12); asm volatile("" ::: "memory"); if (early) w2load(); SCHED(); }
                    }
                }
            }
            if (early) { W2PIN(); }
            lds_post(fm, 10);

        } else {
            asm volatile("s_waitcnt vmcnt(0)" : "+v"(b1a), "+v"(b1b) :: "memory");
            lds_wait(sm + F_P + 12, 1);
            {
                const u32 v0 = (u32)lane * 16u;
                glds_run<3, true, MTHR>((const char*)p.w2 + (size_t)(lj * 4 + wid) * 14336, 4096u, v0, v0, v0, v0, lds0 + 65536);
            }
            WAITV(0); lds_post(fm, 10); READ_TAG();
        }
        PRIO(1);
        if (isG) { WAITV(0); flag_store(flags, (u32)(FL_H + (mt * 2 + th) * 32 + ft * 2 + fh) * 128u, tag1); }
        if (!early) {
            lds_wait(fw, 12);
            w2load();
            W2PIN();
            if (!isG) lds_wait(fl, 13);
            WAITL0();
        }
        w2_dma(IC<MTHR>(), kq, 2);
        w2_dma(IC<MTHR>(), kq, 3);
        poll_flags(flags, FL_H + tt * 32 + 7 * kq, 1, 7, tag1, lane);

        const __amdgpu_buffer_rsrc_t hr = __builtin_amdgcn_make_buffer_rsrc(p.ws + WS_H + (size_t)(tt * 2 * 112 + kq * 28) * 1024, 0, 2 * 112 * 1024, 0x00020000);
        u32x4 hf[WIN][2];
        f32x16 acc[2][2] = {};
        auto ldh = [&](int s, int j) {
            return __builtin_amdgcn_raw_buffer_load_b128(hr, lane * 16 + j * 112 * 1024 + (s & 3) * 1024, (s >> 2) * 4096, 16);
        };
#pragma unroll
        for (int s = 0; s < WIN; s++) { hf[s][0] = ldh(s, 0); SCHED(); hf[s][1] = ldh(s, 1); SCHED(); }

#pragma unroll
        for (int s = 0; s < 28; s++) {
            const u32x4 w0 = wreg[s % 14][0], w1 = wreg[s % 14][1];
            acc[0][0] = MFMA32(w0, hf[s % WIN][0], acc[0][0]); SCHED();
            acc[1][0] = MFMA32(w1, hf[s % WIN][0], acc[1][0]);
            if (s + WIN < 28) hf[s % WIN][0] = ldh(s + WIN, 0);
            SCHED();
            acc[0][1] = MFMA32(w0, hf[s % WIN][1], acc[0][1]);
            if (s >= 12 && s < 26) wreg[s - 12][0] = w2frag(s - 12, 0);
            SCHED();
            acc[1][1] = MFMA32(w1, hf[s % WIN][1], acc[1][1]);
            if (s >= 12 && s < 26) wreg[s - 12][1] = w2frag(s - 12, 1);
            if (s + WIN < 28) hf[s % WIN][1] = ldh(s + WIN, 1);
            if (s + WIN == 28) { LANEVALS(); xe = *(const u32x4*)((const char*)p.x + xo); b2e = *(const u32x4*)((const char*)p.b2 + dt * 128 + (tid & 7) * 16); }
            SCHED();
        }

        int t2 = threadIdx.x; asm volatile("" : "+v"(t2));
        LAS char* pw = w2a + (t2 & 31) * 272 + ((t2 >> 5) & 1) * 16;
#pragma unroll
        for (int j2 = 0; j2 < 2; j2++)
#pragma unroll
            for (int j = 0; j < 2; j++)
#pragma unroll
                for (int rg = 0; rg < 4; rg++) {
                    f32x4 v = {acc[j2][j][4 * rg], acc[j2][j][4 * rg + 1], acc[j2][j][4 * rg + 2], acc[j2][j][4 * rg + 3]};
                    *(LAS f32x4*)(pw + 32 * j * 272 + (32 * j2 + 8 * rg) * 4) = v;
                }

    } else {
        LANEVALS();
        const int a = lane & 15, q = lane >> 4;
        const int cl = hw & 1, rh = hw >> 1, chn = ch0 + 8 * cl;
        if (wid >= 6) {
            const int kc = wid & 1;
            PRIO(1);
            u32 junk = 0;
            if (kc == 0) {
                u32 are, aim, bre, bim, cre, cim, ldt, dsk;
                LOAD_PARAMS((u32)((ch0 + 8) * 128 + 64 + lane) * 2u, (u32)(ch0 + 8) * 2u);
                BAR();
                asm volatile("s_nop 4\n\tglobal_load_dword %0, %1, %2" : "+v"(junk) : "v"((u32)(lane < 56 ? lane : 0) * 128u), "s"(uni((const char*)p.w1 + (size_t)b * 7168)) : "memory");
                WAIT_PARAMS(1);
                build_table(sm, 3, lane, are, aim, bre, bim, cre, cim, ldt, dsk);
                lds_post(sm + F_P + 8, 1);
                READ_TAG();

            } else {
                PRIO(3);
                u32x2 tge;
                u32x4 xv[8];
                asm volatile("global_load_dwordx4 %0, %8, %10\n\tglobal_load_dwordx4 %1, %8, %10 offset:1024\n\tglobal_load_dwordx4 %2, %8, %10 offset:2048\n\tglobal_load_dwordx4 %3, %8, %10 offset:3072\n\t"
                             "global_load_dwordx4 %4, %9, %10\n\tglobal_load_dwordx4 %5, %9, %10 offset:1024\n\tglobal_load_dwordx4 %6, %9, %10 offset:2048\n\tglobal_load_dwordx4 %7, %9, %10 offset:3072"
                             : "=&v"(xv[0]), "=&v"(xv[1]), "=&v"(xv[2]), "=&v"(xv[3]), "=&v"(xv[4]), "=&v"(xv[5]), "=&v"(xv[6]), "=&v"(xv[7])
                             : "v"(lane * 16), "v"(lane * 16 + 4096), "s"((const char*)p.x + (size_t)tb8 * 8192) : "memory");
                asm volatile("global_load_dwordx2 %0, %1, %2 sc1" : "=&v"(tge) : "v"(FL_E * 128), "s"(flags) : "memory");

                BAR();
                asm volatile("s_waitcnt vmcnt(0)" : "+v"(tge), "+v"(xv[0]), "+v"(xv[1]), "+v"(xv[2]), "+v"(xv[3]), "+v"(xv[4]), "+v"(xv[5]), "+v"(xv[6]), "+v"(xv[7]) :: "memory");
                tag1.x = __builtin_amdgcn_readfirstlane(tge.x) + 1u; tag1.y = __builtin_amdgcn_readfirstlane(tge.y);

                *(volatile LAS u32x2*)(sm + L_TAG + 8) = tag1;
                lds_post(sm + L_TAG + 4, 1);
                float s1[8], s2[8];
#pragma unroll
                for (int i = 0; i < 8; i++) { s1[i] = 0.f; s2[i] = 0.f; }
#pragma unroll
                for (int j = 0; j < 4; j++)
#pragma unroll
                    for (int i = 0; i < 8; i++) { s1[i] = dot2(xv[i][j], 0x3f803f80u, s1[i]); s2[i] = dot2(xv[i][j], xv[i][j], s2[i]); }
                auto red8 = [&](float (&v)[8]) {
                    float a[4], c[2];
#pragma unroll
                    for (int i = 0; i < 4; i++) {
                        const auto t = __builtin_amdgcn_permlane32_swap(__float_as_uint(v[i]), __float_as_uint(v[i + 4]), false, false);
                        a[i] = __uint_as_float(t[0]) + __uint_as_float(t[1]);
                    }
#pragma unroll
                    for (int i = 0; i < 2; i++) {
                        const auto t = __builtin_amdgcn_permlane16_swap(__float_as_uint(a[i]), __float_as_uint(a[i + 2]), false, false);
                        c[i] = dpp_add<0x128, 0xf>(__uint_as_float(t[0]) + __uint_as_float(t[1]));
                    }
                    float e = (lane & 8) ? c[1] : c[0];
                    e = dpp_add<0x141, 0xf>(e); e = dpp_add<0xB1, 0xf>(e); e = dpp_add<0x4E, 0xf>(e);
                    return e;
                };
                const float a1 = red8(s1), a2 = red8(s2);
                const float mean = a1 * (1.f / 512.f);
                const float v1 = fmaxf(a2 * (1.f / 512.f) - mean * mean, 0.f);
                const float q1 = v1 + 1e-5f;
                const float rs = __builtin_amdgcn_rsqf(q1);
                const float q2 = v1 * rs * rs + 1e-5f;
                {
                    u32x4 o = {__float_as_uint(-mean), tag1.x, __float_as_uint(__builtin_amdgcn_sqrtf(q1 * q2)), tag1.x};
                    u64 keep;
                    asm volatile("s_mov_b64 %0, exec\n\ts_mov_b64 exec, %4\n\tglobal_store_dwordx4 %1, %2, %3 sc1\n\ts_mov_b64 exec, %0\n\ts_nop 1"
                                 : "=&s"(keep) : "v"(GR_ST + (u32)(tb8 * 8 + (lane >> 3)) * 16u), "v"(o), "s"(flags), "s"(0x0101010101010101ull) : "memory");
                }

                f32x2 sc[4], of[4];
                const float nmr = -mean * rs;
#pragma unroll
                for (int i = 0; i < 4; i++) {
                    sc[i] = (f32x2){__builtin_bit_cast(float, __builtin_amdgcn_readlane(__builtin_bit_cast(int, rs), 16 * i)), __builtin_bit_cast(float, __builtin_amdgcn_readlane(__builtin_bit_cast(int, rs), 16 * i + 8))};
                    of[i] = (f32x2){__builtin_bit_cast(float, __builtin_amdgcn_readlane(__builtin_bit_cast(int, nmr), 16 * i)), __builtin_bit_cast(float, __builtin_amdgcn_readlane(__builtin_bit_cast(int, nmr), 16 * i + 8))};
                }
#pragma unroll
                for (int c = 0; c < 8; c++) {
                    u32x4 o;
#pragma unroll
                    for (int i = 0; i < 4; i++) {
                        const u32 a = xv[2 * i][c >> 1], d = xv[2 * i + 1][c >> 1];
                        const f32x2 v = (c & 1) ? (f32x2){bfhi(a), bfhi(d)} : (f32x2){bflo(a), bflo(d)};
                        const f32x2 n = __builtin_elementwise_fma(v, sc[i], of[i]);
                        o[i] = pkh(n.x, n.y);
                    }
                    astore_b128_sc1(p.ws + WS_U + (size_t)tb8 * 8192, (u32)(c * 1024 + lane * 16), o);
                }
                PRIO(1);

            }
            const int rS = (int)((0x8080840876543210ull >> (4 * a)) & 15), rB = 17 + (int)((0xfa540000u >> (2 * a)) & 3);
            const int rT1 = (a & 3) ? 7 + (a & 3) : 0, rT2 = (a >> 2) ? 10 + (a >> 2) : 0;
            LAS char* tb = sm + L_TAB + kc * TAB_SZ + q * 16;
            LAS char *pS = tb + rS * TAB_ROW, *pB = tb + rB * TAB_ROW, *pT1 = tb + rT1 * TAB_ROW, *pT2 = tb + rT2 * TAB_ROW, *pZ = tb + 14 * TAB_ROW;
            const int hd = tab_off(kc + 2) - tab_off(kc);
            f32x4 G[2][4] = {};
            u32x4 T[2][7];
            auto ldtab = [&](int k) {
                if (k == 4) { pS += hd; pB += hd; pT1 += hd; pT2 += hd; pZ += hd; }
                u32x4* t = T[k & 1];
                const int o = (k & 3) * 64;
                t[0] = *(LAS u32x4*)(pS + o); t[1] = *(LAS u32x4*)(pB + o); t[2] = *(LAS u32x4*)(pT1 + o); t[3] = *(LAS u32x4*)(pT2 + o);
                t[4] = *(LAS u32x4*)(pZ + o); t[5] = *(LAS u32x4*)(pZ + o + TAB_ROW); t[6] = *(LAS u32x4*)(pZ + o + 2 * TAB_ROW);
            };
            int nb = isG ? 0 : 6; bool go = false;
#define SYNCPT() do { if (nb < 6) { if (go) { RBAR(); nb++; } else go = all_ge(*(volatile LAS u32*)(sm + F_L + (lane & 1) * 4), 2); } } while (0)
            lds_wait(sm + F_M + (lane & 3) * 4, 1);

            ldtab(0);

#pragma unroll
            for (int k = 0; k < 8; k++) {
                u32x4* t = T[k & 1];
                f32x4* Gh = G[k >> 2];
                u32x4 A, B0, B1, B2, B3;
                asm volatile("" :: "v"(t[0]), "v"(t[1]), "v"(t[2]), "v"(t[3]), "v"(t[4]), "v"(t[5]), "v"(t[6]));
                if (k == 3) lds_wait(sm + F_P + 8, 1);

                if (k < 7) ldtab(k + 1);

                SCHED();
#pragma unroll
                for (int j = 0; j < 4; j++) { B0[j] = cmh2<false>(t[2][j], t[3][j], cmh1<false>(t[2][j], t[3][j])); A[j] = cmh2<true>(t[0][j], t[1][j], cmh1<true>(t[0][j], t[1][j])); }
                SCHED();
#pragma unroll
                for (int j = 0; j < 4; j++) B1[j] = cmh2<false>(B0[j], t[4][j], cmh1<false>(B0[j], t[4][j]));
                SCHED();
                Gh[0] = MFMA16H(A, B0, Gh[0]);
#pragma unroll
                for (int j = 0; j < 4; j++) B2[j] = cmh2<false>(B0[j], t[5][j], cmh1<false>(B0[j], t[5][j]));
                SCHED();
                Gh[1] = MFMA16H(A, B1, Gh[1]);
#pragma unroll
                for (int j = 0; j < 4; j++) B3[j] = cmh2<false>(B0[j], t[6][j], cmh1<false>(B0[j], t[6][j]));
                SCHED();
                Gh[2] = MFMA16H(A, B2, Gh[2]);
                asm volatile("s_nop 1" : "+v"(B3));
                Gh[3] = MFMA16H(A, B3, Gh[3]);
                SCHED();
                if (k == 1 && kc) { WAITV(0); flag_store(flags, (u32)(FL_LN * 128 + tb8 * 8), tag1); if (!isG) lds_post(sm + F_P + 12, 1); }
                SYNCPT();
            }

            {
                const float M0 = *(LAS float*)(sm + L_MISC + kc * 4), M1 = *(LAS float*)(sm + L_MISC + 8 + kc * 4), M = fmaxf(M0, M1);
                const float s0 = __builtin_amdgcn_exp2f(M0 - M), s1 = __builtin_amdgcn_exp2f(M1 - M);
                const float dx = *(LAS float*)(sm + L_MISC + 16 + kc * 4) * __builtin_amdgcn_exp2f(-M), d0 = lane == 0 ? dx : 0.f;
                LAS char* gx = sm + L_TAB + kc * TAB_SZ;
#pragma unroll
                for (int ct = 0; ct < 4; ct++) {
                    float v[4];
#pragma unroll
                    for (int e = 0; e < 4; e++) v[e] = opq(fmaf(G[1][ct][e], s1, (ct | e) ? opq(G[0][ct][e] * s0) : fmaf(G[0][ct][e], s0, d0)));
                    u32x2 o = {pkh(v[0], v[1]), pkh(v[2], v[3])};
                    *(LAS u32x2*)(gx + (16 * ct + a) * 32 + q * 8) = o;
                }
                SYNCPT();
                WAITL0();
                const u32x4 u = *(LAS u32x4*)(gx + lane * 32), v = *(LAS u32x4*)(gx + lane * 32 + 16);
                LAS char* kp = sm + L_KP + kc * KP_CH + (lane & 1) * KP_PLANE + (16 + (lane >> 1)) * 16;
                const u32 LL = 0x01000100u, HH = 0x03020302u, SW = 0x01000302u;
                const u32 sa = h ? 0x05040100u : 0x05040706u, sb = h ? LL : SW, tie = (lane & 1) ? HH : SW;
                u32x4 w0 = {perm(u.w, v.x, sa), perm(0, u.z, sb), perm(u.y, u.z, sa), perm(0, u.x, sb)};
                u32x4 w1 = {perm(0, v.y, HH), perm(0, v.y, LL), perm(v.x, v.y, 0x07060100u), perm(0, v.x, HH)};
                u32x4 w2 = {perm(0, v.z, HH), perm(0, v.z, tie), perm(0, v.z, LL), perm(0, v.z, LL)};
                u32x4 w3 = {perm(0, v.w, HH), perm(0, v.w, tie), perm(0, v.w, LL), perm(0, v.w, LL)};
                *(LAS u32x4*)kp = w0; *(LAS u32x4*)(kp + 512) = w1; *(LAS u32x4*)(kp + 1024) = w2; *(LAS u32x4*)(kp + 1536) = w3;
                u32x4 z = {0, 0, 0, 0};
                *(LAS u32x4*)(sm + L_KP + kc * KP_CH + (q & 1) * KP_PLANE + a * 16) = z;
            }
            lds_post(sm + F_K + kc * 4, 1);

            while (nb < 6) { RBAR(); nb++; }
            lds_wait(sm + F_M + (lane & 3) * 4, 9);
            if (!isG) lds_wait(sm + F_L + (lane & 1) * 4, 2);
            w2_dma(IC<LTHR>(), 2, kc);
            w2_dma(IC<LTHR>(), 3, kc);

            asm volatile("s_waitcnt vmcnt(0)" : "+v"(junk) :: "memory");
            lds_post(sm + F_K + kc * 4, 12);

        } else {
            LOADER_VALS();
            LAS char* fm = sm + F_M + (lane & 3) * 4;
            LAS char* fl = sm + F_L + hw * 4;

            if (__builtin_expect(isG, 1)) { glds_run<4, false, LTHR>(lsrc, lgs, vo0, vo1, vo2, vo3, lds0 + hw * 16384); RBAR(); }
            const int tb0 = hw * 128 + (lane >> 1);
            u32 ob = (u32)((r >> 2) * 1024 + (8 * g + 2 * (r & 3) + (lane & 1)) * 16), o0 = ob + (u32)tb0 * 8192u;
            u32x4 uv[4], f0, f1; u32x2 pv;
#define LOAD_LNF() asm volatile("global_load_dwordx4 %0, %2, %3 sc1\n\tglobal_load_dwordx4 %1, %2, %3 offset:1024 sc1" : "=&v"(f0), "=&v"(f1) : "v"(lfo), "s"(flags) : "memory")
#define WAIT_LNF() asm volatile("s_waitcnt vmcnt(0)" : "+v"(f0), "+v"(f1) :: "memory")
#define LNF_OK() (__builtin_amdgcn_ballot_w64((((f0.x ^ tag1.x) | (f0.z ^ tag1.x) | (f1.x ^ tag1.x) | (f1.z ^ tag1.x)) | ((f0.y ^ tag1.y) | (f0.w ^ tag1.y) | (f1.y ^ tag1.y) | (f1.w ^ tag1.y))) != 0u) == 0)
#define LOAD_U(B, M) do { _Pragma("unroll") for (int i = 0; i < 4; i++) uv[i] = aload_b128_sc1(B, (o0 + i * 32 * 8192) & (M)); pv = aload_b64_sc1(B, (ob + 127u * 8192u + 8u) & (M)); } while (0)
            u32 lfo = (u32)(FL_LN * 128 + lane * 16);
            asm volatile("" : "+v"(ob), "+v"(o0), "+v"(lfo));
            if (__builtin_expect(isG, 1)) {
                const char* nsrc = lsrc + lcs;
#pragma unroll 1
                for (int kc = 1; kc < 8; kc++) {
                    u32 dst = lds0 + (kc & 3) * 32768 + hw * 16384;
                    asm volatile("" : "+s"(nsrc), "+s"(dst));
                    if (kc >= 4) RBAR();

                    glds_run<4, false, LTHR>(nsrc, lgs, vo0, vo1, vo2, vo3, dst);
                    nsrc += lcs;
                    WAITV(16);
                    if (kc < 3) *(volatile LAS u32*)fl = kc;
                    if (kc == 3) READ_TAG();
                }
                RBAR();

            } else {
                BAR();
                const char* wb = (const char*)p.w1 + (lj < 28 ? lj : 0) * 128 + (size_t)hw * 256 * 3584;
                const u32 v0 = (u32)((lane >> 3) * 3584) + vsw;
                glds_run<8, false, LTHR>(wb, 32u * 3584u, v0, v0 + 27648u, v0 + 55296u, v0 + 82944u, lds0 + hw * 32768);
                f32x16 c0 = {}, c1 = {};
                const u32x4 ones = {0x3f803f80u, 0x3f803f80u, 0x3f803f80u, 0x3f803f80u};
#pragma unroll 1
                for (int s0 = 0; s0 < 16; s0 += 8) {
                    u32x4 fa[8], fb[8];
                    if (s0 == 0) WAITV(16); else WAITV(0);
#pragma unroll
                    for (int s = 0; s < 8; s++) {
                        LAS char* a = sm + hw * 32768 + (s0 + s) * 2048;
                        fa[s] = trfrag(a + tra0, a + tra0 + 1024); fb[s] = trfrag(a + (tra0 ^ 64), a + (tra0 ^ 64) + 1024);
                    }
                    SCHED();
#pragma unroll
                    for (int s = 0; s < 8; s++) { c0 = MFMA32(ones, fa[s], c0); c1 = MFMA32(ones, fb[s], c1); }
                }
                const float c = h ? c1[0] : c0[0];
                *(LAS float*)(sm + L_CS + hw * 256 + lane * 4) = c;
                lds_post(fl, 1);
                READ_TAG();
                lds_wait(sm + F_L + (lane & 1) * 4, 1);
                u32x2 o = {__float_as_uint(*(LAS float*)(sm + L_CS + lane * 4) + *(LAS float*)(sm + L_CS + 256 + lane * 4)), tag1.x};
                if (hw == 0 && lj < 28) astore_b64_sc1(flags, GR_CS + (u32)(lj * 64 + lane) * 8u, o);
                lds_post(fl, 2); lds_wait(fm, 10);
                __builtin_amdgcn_s_sleep(16);
            }
            LOAD_LNF();
            w2_dma(IC<LTHR>(), 0, hw);
            WAITV(16);
            if (isG) RBAR();

            asm volatile("s_waitcnt vmcnt(14)" : "+v"(f0), "+v"(f1) :: "memory");
            const bool uok = LNF_OK();
            { const char* ub = uok ? (const char*)p.ws + WS_U : (const char*)p.b2; LOAD_U(ub, uok ? ~0u : 0u); }
            w2_dma(IC<LTHR>(), 1, hw);

            asm volatile("s_waitcnt vmcnt(0)" : "+v"(uv[0]), "+v"(uv[1]), "+v"(uv[2]), "+v"(uv[3]), "+v"(pv) :: "memory");
            lds_post(fl, 12);

            if (!uok) {
#pragma unroll 1
                for (int spin = 0; spin < (1 << 16); ++spin) {
                    LOAD_LNF(); WAIT_LNF();
                    if (LNF_OK()) break;
                    __builtin_amdgcn_s_sleep(6);
                }

                LOAD_U(p.ws + WS_U, ~0u);
                asm volatile("s_waitcnt vmcnt(0)" : "+v"(uv[0]), "+v"(uv[1]), "+v"(uv[2]), "+v"(uv[3]), "+v"(pv) :: "memory");
            }
            if (b == 0 && hw == 0) flag_store(flags, FL_E * 128, tag1);
            PRIO(3);
            lds_wait(fm, 9);
            lds_wait(sm + F_K + (lane & 1) * 4, 1);

            u32 r2[5], r3[5];
            r2[0] = hw ? pv.x : 0u; r3[0] = hw ? pv.y : 0u;
#pragma unroll
            for (int i = 0; i < 4; i++) {
                r2[i + 1] = __builtin_amdgcn_ds_bpermute(((lane - 2) & 63) * 4, uv[i].z);
                r3[i + 1] = __builtin_amdgcn_ds_bpermute(((lane - 2) & 63) * 4, uv[i].w);
            }
            LAS char* d = sm + L_UC + (lane & 1) * UC_CH + (tb0 + 4) * 16;
#pragma unroll
            for (int i = 0; i < 4; i++) *(LAS u32x4*)(d + i * 512) = uv[i];
#pragma unroll
            for (int i = 0; i < 4; i++) {
                const u32x4 t = uv[i];
                const u32 q2 = lane < 2 ? r2[i] : r2[i + 1], q3 = lane < 2 ? r3[i] : r3[i + 1];
                const u32 a0 = __builtin_amdgcn_alignbit(q3, q2, 16), a1 = __builtin_amdgcn_alignbit(t.x, q3, 16), a2 = __builtin_amdgcn_alignbit(t.y, t.x, 16),
                          a3 = __builtin_amdgcn_alignbit(t.z, t.y, 16), a4 = __builtin_amdgcn_alignbit(t.w, t.z, 16);
                u32x4 c1 = {a1, a2, a3, a4}, c2 = {q3, t.x, t.y, t.z}, c3 = {a0, a1, a2, a3};
                *(LAS u32x4*)(d + i * 512 + UC_COPY) = c1;
                *(LAS u32x4*)(d + i * 512 + 2 * UC_COPY) = c2;
                *(LAS u32x4*)(d + i * 512 + 3 * UC_COPY) = c3;
            }
            u32x4 z = {0, 0, 0, 0};
            *(LAS u32x4*)(sm + L_UC + hw * UC_CH + ((lane >> 2) & 3) * UC_COPY + (lane & 3) * 16) = z;
            lds_post(fl, 13);

        }
        lds_wait(sm + F_L + (lane & 1) * 4, 13);

        LAS char* ub = sm + L_UC + cl * UC_CH + (3 - (a & 3)) * UC_COPY + (32 * rh + 2 * q + 5 + (a >> 2)) * 8;
        LAS char* kb = sm + L_KP + cl * KP_CH + ((q + 1) & 1) * KP_PLANE + (16 - 8 * rh - 6 + a - (q >> 1)) * 16;
        f32x4 y[8] = {};
        auto ldA = [&](int dl, int i) {
            const u32x2 lo = *(volatile LAS u32x2*)(ub + dl * 64 + i * 512), hi = *(volatile LAS u32x2*)(ub + dl * 64 + i * 512 + 8);
            return (u32x4){lo.x, lo.y, hi.x, hi.y};
        };
        auto ldB = [&](int dl, int i) { return *(LAS u32x4*)(kb + (3 - dl) * 32 + i * 256); };
        u32x4 A0[8], B0[8], A1[8], B1[8];
#pragma unroll
        for (int i = 0; i < 8; i++) { B0[i] = ldB(0, i); A0[i] = ldA(0, i); }
        lds_wait(sm + F_M + (lane & 3) * 4, 10);
        PRIO(2);

        auto cstep = [&](u32x4 (&A)[8], u32x4 (&B)[8], u32x4 (&An)[8], u32x4 (&Bn)[8], int dn) {
            asm volatile("" :: "v"(A[0]), "v"(A[1]), "v"(A[2]), "v"(A[3]), "v"(A[4]), "v"(A[5]), "v"(A[6]), "v"(A[7]));
            asm volatile("" :: "v"(B[0]), "v"(B[1]), "v"(B[2]), "v"(B[3]), "v"(B[4]), "v"(B[5]), "v"(B[6]), "v"(B[7]));
            SCHED();
            int n = 0;
#pragma unroll
            for (int gm = 0; gm < 8; gm++)
#pragma unroll
                for (int c = gm; c < 8; c++) {
                    y[c] = MFMA16H(A[gm], B[c - gm], y[c]);
                    if (dn < 4 && n < 24) { if (n % 3 == 0) Bn[n / 3] = ldB(dn, n / 3); else if (n % 3 == 1) An[n / 3] = ldA(dn, n / 3); }
                    n++;
                    SCHED();
                }
        };
        cstep(A0, B0, A1, B1, 1); cstep(A1, B1, A0, B0, 2); cstep(A0, B0, A1, B1, 3); cstep(A1, B1, A0, B0, 4);
        PRIO(3);

        const float ksc = __builtin_amdgcn_exp2f(fmaxf(*(LAS float*)(sm + L_MISC + cl * 4), *(LAS float*)(sm + L_MISC + 8 + cl * 4)));
        LAS char* fh = sm + F_H + hw * 4;
        LAS char* fp = sm + F_H + (hw ^ 2) * 4;
        lds_post(fh, 1); lds_wait(fp, 1);

        LAS char* xb = sm + L_UC + cl * UC_CH + lane * 16;
        if (rh) {
#pragma unroll
            for (int c = 0; c < 4; c++) *(LAS f32x4*)(xb + 4096 + c * 1024) = y[c];
            lds_post(fh, 2);
#pragma unroll
            for (int c = 0; c < 4; c++)
#pragma unroll
                for (int e = 0; e < 4; e++) y[c][e] = opq(y[4 + c][e] * ksc);
        } else {
#pragma unroll
            for (int c = 0; c < 4; c++) *(LAS f32x4*)(xb + c * 1024) = y[4 + c];
            lds_post(fh, 2);
#pragma unroll
            for (int c = 0; c < 4; c++)
#pragma unroll
                for (int e = 0; e < 4; e++) y[c][e] = opq(y[c][e] * ksc);
        }
        char* sdst = p.ws + WS_S + (size_t)chn * 4096 + rh * 2048;

        lds_wait(fp, 2);

        {
            u32x2 o[4];
#pragma unroll
            for (int c = 0; c < 4; c++) {
                const f32x4 v = *(LAS f32x4*)(xb + (rh ^ 1) * 4096 + c * 1024);
                float f[4];
#pragma unroll
                for (int e = 0; e < 4; e++) f[e] = opq(fmaf(v[e], ksc, y[c][e]));
                o[c] = (u32x2){pkbf(f[0], f[1]), pkbf(f[2], f[3])};
            }
#pragma unroll
            for (int cp = 0; cp < 2; cp++) {
                const auto lo = __builtin_amdgcn_permlane16_swap(o[2 * cp].x, o[2 * cp + 1].x, false, false);
                const auto hi = __builtin_amdgcn_permlane16_swap(o[2 * cp].y, o[2 * cp + 1].y, false, false);
                astore_b128_sc1(sdst, (u32)(cp * 1024 + lane * 16), (u32x4){lo[0], hi[0], lo[1], hi[1]});
            }
        }

        WAITV(0);
        lds_post(fh, 3);
        lds_wait(sm + F_H + (lane & 3) * 4, 3);
        if (wid == 4) flag_store(flags, (u32)(FL_S + b) * 128u, tag1);
        PRIO(0);

        xe = *(const u32x4*)((const char*)p.x + xo); b2e = *(const u32x4*)((const char*)p.b2 + dt * 128 + (tid & 7) * 16);
        __builtin_amdgcn_s_sleep(16);
        poll_flags(flags, FL_S + dt + 8 * hw, 32, 8, tag1, lane);

        u32x4 sv[2];
#pragma unroll
        for (int i = 0; i < 2; i++) {
            const int d = hw * 16 + i * 8 + (lane >> 3);
            sv[i] = aload_b128_sc1(p.ws + WS_S, (u32)((dt * 64 + d) * 4096 + (tt >> 3) * 1024 + ((tt >> 2) & 1) * 256 + ((lane >> 2) & 1) * 512 + (tt & 3) * 64 + (lane & 3) * 16));
        }
        asm volatile("s_waitcnt vmcnt(0)" : "+v"(sv[0]), "+v"(sv[1]) :: "memory");
#pragma unroll
        for (int i = 0; i < 2; i++) {
            const int d = hw * 16 + i * 8 + (lane >> 3);
            *(LAS u32x4*)(sm + L_UC + d * 128 + (((2 * (lane & 3) + ((lane >> 2) & 1)) ^ (d >> 3)) << 4)) = sv[i];
        }

    }
    {
        LANEVALS();
        const int tok = tid >> 3, dg = tid & 7;
        float m[8], xf[8];
#pragma unroll
        for (int e = 0; e < 4; e++) { m[2 * e] = bflo(b2e[e]); m[2 * e + 1] = bfhi(b2e[e]); xf[2 * e] = bflo(xe[e]); xf[2 * e + 1] = bfhi(xe[e]); }
        u32 pa = (u32)(L_W2 + tok * 272 + dg * 32), pb = pa + 2 * W2_REG, sa = (u32)(L_UC + dg * 1024 + (((tok >> 3) ^ dg) << 4) + (tok & 7) * 2), oo = xo;
        asm volatile("" : "+v"(m[0]), "+v"(m[1]), "+v"(m[2]), "+v"(m[3]), "+v"(m[4]), "+v"(m[5]), "+v"(m[6]), "+v"(m[7]), "+v"(pa), "+v"(pb), "+v"(sa), "+v"(oo));
        asm volatile("" : "+v"(xf[0]), "+v"(xf[1]), "+v"(xf[2]), "+v"(xf[3]), "+v"(xf[4]), "+v"(xf[5]), "+v"(xf[6]), "+v"(xf[7]));
        BAR();

#pragma unroll
        for (int i = 0; i < 4; i++) {
            LAS char* q = sm + (i < 2 ? pa : pb) + (i & 1) * W2_REG;
            const f32x4 lo = *(LAS f32x4*)q, hi = *(LAS f32x4*)(q + 16);
#pragma unroll
            for (int e = 0; e < 4; e++) { m[e] += lo[e]; m[4 + e] += hi[e]; }
        }
        float o[8];
#pragma unroll
        for (int e = 0; e < 8; e++) {
            const float s = bf2f(*(LAS u16*)(sm + sa + e * 128));
            o[e] = fmaf(s * m[e], __builtin_amdgcn_rcpf(1.f + __builtin_amdgcn_exp2f(-1.4426950408889634f * m[e])), xf[e]);
        }
        u32x4 ov = {pkbf(o[0], o[1]), pkbf(o[2], o[3]), pkbf(o[4], o[5]), pkbf(o[6], o[7])};
        astore_b128_sc1(p.out, oo, ov);
    }

}

extern "C" void kernel_launch(void* const* d_in, const int* in_sizes, int n_in, void* d_out, int out_size, void* d_ws, size_t ws_size,
                              hipStream_t stream) {
    Params p;
    p.x = (const u16*)d_in[0]; p.Are = (const u16*)d_in[1]; p.Aim = (const u16*)d_in[2]; p.Bre = (const u16*)d_in[3]; p.Bim = (const u16*)d_in[4];
    p.Cre = (const u16*)d_in[5]; p.Cim = (const u16*)d_in[6]; p.logdt = (const u16*)d_in[7]; p.Dskip = (const u16*)d_in[8];
    p.w1 = (const u16*)d_in[10]; p.b1 = (const u16*)d_in[11]; p.w2 = (const u16*)d_in[12]; p.b2 = (const u16*)d_in[13];
    p.out = (u16*)d_out; p.ws = (char*)d_ws;
    fused_kernel<<<256, 512, 0, stream>>>(p);
}
```

```cpp
#include <hip/hip_runtime.h>
#include <stdint.h>

typedef unsigned short u16;
typedef unsigned u32;
typedef unsigned long long u64;
typedef u32 u32x2 __attribute__((ext_vector_type(2)));
typedef u32 u32x4 __attribute__((ext_vector_type(4)));
typedef short s16x4 __attribute__((ext_vector_type(4)));
typedef __bf16 bf16x2 __attribute__((ext_vector_type(2)));
typedef __bf16 bf16x8 __attribute__((ext_vector_type(8)));
typedef _Float16 f16x2 __attribute__((ext_vector_type(2)));
typedef _Float16 f16x8 __attribute__((ext_vector_type(8)));
typedef float f32x2 __attribute__((ext_vector_type(2)));
typedef float f32x4 __attribute__((ext_vector_type(4)));
typedef float f32x16 __attribute__((ext_vector_type(16)));
#define LAS __attribute__((address_space(3)))
#define DEV __device__ __forceinline__

template <int N> struct IC { static constexpr int value = N; };

constexpr u32 CODE_LINES = 240;
constexpr int LTHR = 16, MTHR = 10;
constexpr int WIN = 6;

constexpr int FL_S = 32, FL_E = 288, FL_H = 320, FL_LN = 1344;
constexpr u32 GR_ST = 256u << 10, GR_CS = 320u << 10;
constexpr size_t WS_U = 8u << 20;
constexpr size_t WS_S = 10u << 20;
constexpr size_t WS_H = 12u << 20;

constexpr int L_W2 = 0, W2_REG = 28672;
constexpr int L_UC = 114688, UC_COPY = 4160, UC_CH = 4 * UC_COPY + 64;
constexpr int L_TAB = 131072, TAB_ROW = 288, TAB_SZ = 6144;
constexpr int L_MISC = 149504;
constexpr int L_KP = 150784, KP_PLANE = 2304, KP_CH = 2 * KP_PLANE;
constexpr int F_M = L_MISC + 64, F_L = L_MISC + 80, F_K = L_MISC + 88, F_H = L_MISC + 96, F_P = L_MISC + 160;
constexpr int L_TAG = L_MISC + 112, L_JUNK = L_MISC + 256, L_CS = L_MISC + 512;
DEV int tab_off(int w) { return w == 3 ? L_KP + KP_CH : L_TAB + w * TAB_SZ; }

struct Params {
    const u16 *x, *Are, *Aim, *Bre, *Bim, *Cre, *Cim, *logdt, *Dskip, *w1, *b1, *w2, *b2;
    u16* out;
    char* ws;
};

DEV float bf2f(u32 v) { return __uint_as_float(v << 16); }
DEV float bflo(u32 v) { return __uint_as_float(v << 16); }
DEV float bfhi(u32 v) { return __uint_as_float(v & 0xffff0000u); }
DEV u32 pkbf(float a, float b) { f32x2 f = {a, b}; return __builtin_bit_cast(u32, __builtin_convertvector(f, bf16x2)); }
DEV u32 pkh(float a, float b) { f32x2 f = {a, b}; return __builtin_bit_cast(u32, __builtin_convertvector(f, f16x2)); }
DEV float opq(float a) { asm("" : "+v"(a)); return a; }
DEV float rbf(float a) { return bflo(pkbf(a, a)); }
DEV float dot2(u32 a, u32 b, float c) { return __builtin_amdgcn_fdot2_f32_bf16(__builtin_bit_cast(bf16x2, a), __builtin_bit_cast(bf16x2, b), c, false); }

#define WAITV(n) asm volatile("s_waitcnt vmcnt(" #n ")" ::: "memory")
#define WAITL0() asm volatile("s_waitcnt lgkmcnt(0)" ::: "memory")
#define BAR() do { asm volatile("s_waitcnt lgkmcnt(0)" ::: "memory"); __builtin_amdgcn_s_barrier(); asm volatile("" ::: "memory"); } while (0)
#define RBAR() do { asm volatile("" ::: "memory"); __builtin_amdgcn_s_barrier(); asm volatile("" ::: "memory"); } while (0)
#define SCHED() __builtin_amdgcn_sched_barrier(0)
#define PRIO(n) __builtin_amdgcn_s_setprio(n)
#define MFMA32(a, b, c) __builtin_amdgcn_mfma_f32_32x32x16_bf16(__builtin_bit_cast(bf16x8, a), __builtin_bit_cast(bf16x8, b), c, 0, 0, 0)
#define MFMA16H(a, b, c) __builtin_amdgcn_mfma_f32_16x16x32_f16(__builtin_bit_cast(f16x8, a), __builtin_bit_cast(f16x8, b), c, 0, 0, 0)

DEV const void* uni(const void* q) {
    const u64 a = (u64)q;
    return (const void*)(((u64)(u32)__builtin_amdgcn_readfirstlane((u32)(a >> 32)) << 32) | (u32)__builtin_amdgcn_readfirstlane((u32)a));
}
#define GL4(B) "s_waitcnt vmcnt(%c[p])\n\tglobal_load_lds_dwordx4 %[v0], " B "\n\tglobal_load_lds_dwordx4 %[v1], " B " offset:1024\n\t" \
               "global_load_lds_dwordx4 %[v2], " B " offset:2048\n\tglobal_load_lds_dwordx4 %[v3], " B " offset:3072\n\t"
template <int NG, bool HALF, int THR> DEV void glds_run(const void* base, u32 step, u32 v0, u32 v1, u32 v2, u32 v3, u32 dst) {
    u32 keep;
    dst = __builtin_amdgcn_readfirstlane(dst);
    base = uni(base);
    asm volatile("s_mov_b32 %[k], m0\n\ts_mov_b32 m0, %[d]\n\ts_mov_b64 vcc, %[b]\n\ts_nop 1\n\t" GL4("vcc")
                 ".rept %c[n]\n\ts_add_u32 m0, m0, 0x1000\n\ts_add_u32 vcc_lo, vcc_lo, %[s]\n\ts_addc_u32 vcc_hi, vcc_hi, 0\n\t" GL4("vcc") ".endr\n\t"
                 ".if %c[h]\n\ts_add_u32 m0, m0, 0x1000\n\ts_add_u32 vcc_lo, vcc_lo, %[s]\n\ts_addc_u32 vcc_hi, vcc_hi, 0\n\t"
                 "global_load_lds_dwordx4 %[v0], vcc\n\tglobal_load_lds_dwordx4 %[v1], vcc offset:1024\n\t.endif\n\t"
                 "s_mov_b32 m0, %[k]"
                 : [k] "=&s"(keep) : [v0] "v"(v0), [v1] "v"(v1), [v2] "v"(v2), [v3] "v"(v3), [b] "s"(base), [d] "s"(dst), [n] "i"(NG - 1), [s] "s"(step), [h] "i"(HALF ? 1 : 0), [p] "i"(THR)
                 : "memory", "scc", "vcc");
}
DEV void touch(const void* base, u32 voff, u32 junk) {
    u32 keep;
    base = uni(base);
    asm volatile("s_mov_b32 %0, m0\n\ts_mov_b32 m0, %3\n\ts_nop 2\n\tglobal_load_lds_dword %1, %2\n\ts_mov_b32 m0, %0"
                 : "=&s"(keep) : "v"(voff), "s"(base), "s"(junk) : "memory");
}
DEV u32x2 aload_b64_sc1(const void* base, u32 voff) { u32x2 r; asm volatile("global_load_dwordx2 %0, %1, %2 sc1" : "=v"(r) : "v"(voff), "s"(base) : "memory"); return r; }
DEV u32x4 aload_b128_sc1(const void* base, u32 voff) { u32x4 r; asm volatile("global_load_dwordx4 %0, %1, %2 sc1" : "=v"(r) : "v"(voff), "s"(base) : "memory"); return r; }
DEV void astore_b128_sc1(void* base, u32 voff, u32x4 v) { asm volatile("global_store_dwordx4 %0, %1, %2 sc1\n\ts_nop 1" :: "v"(voff), "v"(v), "s"(base) : "memory"); }
DEV void astore_b64_sc1(void* base, u32 voff, u32x2 v) { asm volatile("global_store_dwordx2 %0, %1, %2 sc1\n\ts_nop 1" :: "v"(voff), "v"(v), "s"(base) : "memory"); }
DEV void flag_store(void* base, u32 off, u32x2 v) {
    u64 keep;
    asm volatile("s_mov_b64 %0, exec\n\ts_mov_b64 exec, 1\n\tglobal_store_dwordx2 %1, %2, %3 sc1\n\ts_mov_b64 exec, %0\n\ts_nop 0"
                 : "=&s"(keep) : "v"(off), "v"(v), "s"(base) : "memory");
}
DEV u32x4 trfrag(LAS char* a, LAS char* b) {
    const u32x2 lo = __builtin_bit_cast(u32x2, __builtin_amdgcn_ds_read_tr16_b64_v4i16((LAS s16x4*)a));
    const u32x2 hi = __builtin_bit_cast(u32x2, __builtin_amdgcn_ds_read_tr16_b64_v4i16((LAS s16x4*)b));
    return (u32x4){lo.x, lo.y, hi.x, hi.y};
}

DEV void poll_flags(const char* flags, int first, int stride, int nfl, u32x2 tag, int lane) {
    const u32 off = (u32)(first + (lane < nfl ? lane : 0) * stride) * 128u;
#pragma unroll 1
    for (int spin = 0; spin < (1 << 16); ++spin) {
        u32x2 v = aload_b64_sc1(flags, off);
        asm volatile("s_waitcnt vmcnt(0)" : "+v"(v) :: "memory");
        if (__builtin_amdgcn_ballot_w64(v.x != tag.x || v.y != tag.y) == 0) break;
        __builtin_amdgcn_s_sleep(6);
    }
}
DEV bool all_ge(u32 v, u32 target) { return __builtin_amdgcn_ballot_w64(v < target) == 0; }
DEV void lds_wait(LAS char* w, u32 target) {
#pragma unroll 1
    for (int spin = 0; spin < (1 << 21); ++spin) {
        if (all_ge(*(volatile LAS u32*)w, target)) break;
        __builtin_amdgcn_s_sleep(1);
    }
    asm volatile("" ::: "memory");
}
DEV void lds_post(LAS char* w, u32 v) { WAITL0(); *(volatile LAS u32*)w = v; }
template <int CTRL, int RM> DEV float dpp_add(float v) {
    return v + __builtin_bit_cast(float, __builtin_amdgcn_update_dpp(0, __builtin_bit_cast(int, v), CTRL, RM, 0xf, false));
}
template <int CTRL, int RM> DEV u32 dpp_umax(u32 v) { const u32 o = __builtin_amdgcn_update_dpp(0u, v, CTRL, RM, 0xf, false); return v > o ? v : o; }
DEV u32 wave_umax(u32 v) {
    v = dpp_umax<0xB1, 0xf>(v); v = dpp_umax<0x4E, 0xf>(v); v = dpp_umax<0x141, 0xf>(v); v = dpp_umax<0x140, 0xf>(v);
    v = dpp_umax<0x142, 0xa>(v); v = dpp_umax<0x143, 0xc>(v);
    return __builtin_amdgcn_readlane(v, 63);
}
DEV f32x2 cexp_f(float e2, float rev) {
    const float m = __builtin_amdgcn_exp2f(e2);
    return (f32x2){m * __builtin_amdgcn_cosf(rev), m * __builtin_amdgcn_sinf(rev)};
}
DEV f32x2 cmul_f(f32x2 a, f32x2 z) {
    f32x2 t, r;
    asm("v_pk_mul_f32 %0, %1, %2 op_sel_hi:[1,0]" : "=v"(t) : "v"(a), "v"(z));
    asm("v_pk_fma_f32 %0, %1, %2, %3 op_sel:[1,1,0] op_sel_hi:[0,1,1] neg_lo:[0,1,0]" : "=v"(r) : "v"(a), "v"(z), "v"(t));
    return r;
}
template <bool CONJ> DEV u32 cmh1(u32 a, u32 z) {
    u32 t;
    if (CONJ) asm("v_pk_mul_f16 %0, %1, %2 op_sel_hi:[1,0] neg_hi:[1,0]" : "=v"(t) : "v"(a), "v"(z));
    else asm("v_pk_mul_f16 %0, %1, %2 op_sel_hi:[1,0]" : "=v"(t) : "v"(a), "v"(z));
    return t;
}
template <bool CONJ> DEV u32 cmh2(u32 a, u32 z, u32 t) {
    u32 r;
    if (CONJ) asm("v_pk_fma_f16 %0, %1, %2, %3 op_sel:[1,1,0] op_sel_hi:[0,1,1] neg_lo:[0,1,0] neg_hi:[0,1,0]" : "=v"(r) : "v"(a), "v"(z), "v"(t));
    else asm("v_pk_fma_f16 %0, %1, %2, %3 op_sel:[1,1,0] op_sel_hi:[0,1,1] neg_lo:[0,1,0]" : "=v"(r) : "v"(a), "v"(z), "v"(t));
    return r;
}
DEV void build_table(LAS char* sm, int w, int lane, u32 are, u32 aim, u32 bre, u32 bim, u32 cre, u32 cim, u32 ldt, u32 dsk) {
    LAS char* t = sm + tab_off(w) + lane * 4;
    *(LAS u32*)(sm + L_MISC + 16 + w * 4) = dsk << 16;
    auto put = [&](int row, f32x2 v) { *(LAS u32*)(t + row * TAB_ROW) = pkh(v.x, v.y); };
    const float dtv = rbf(__builtin_amdgcn_exp2f(bf2f(ldt) * 1.4426950408889634f));
    const float a2 = dtv * bf2f(are) * 1.4426950408889634f;
    float rev = dtv * bf2f(aim) * 0.15915494309189535f;
    rev -= __builtin_rintf(rev);
    f32x2 wv = cmul_f((f32x2){bf2f(cre), bf2f(cim)}, (f32x2){bf2f(bre), bf2f(bim)});
    u32 E = wave_umax(__float_as_uint(fmaxf(dtv * fmaxf(fabsf(wv.x), fabsf(wv.y)) * __builtin_amdgcn_exp2f(fmaxf(a2, 0.f) * 2048.f), fabsf(bf2f(dsk)) * 0.0078125f))) >> 23;
    E = E < 16u ? 16u : (E > 236u ? 236u : E);
    wv *= dtv * __uint_as_float((260u - E) << 23);
    *(LAS float*)(sm + L_MISC + w * 4) = (float)(int)E - 133.f;
    const f32x2 z1 = cexp_f(a2, rev), z64 = cexp_f(a2 * 64.f, rev * 64.f);
    const f32x2 z2 = cmul_f(z1, z1), z3 = cmul_f(z2, z1), z4 = cmul_f(z2, z2), z8 = cmul_f(z4, z4), z16 = cmul_f(z8, z8), z32 = cmul_f(z16, z16);
    const f32x2 z128 = cmul_f(z64, z64), z256 = cmul_f(z128, z128), z512 = cmul_f(z256, z256);
    const f32x2 w1 = cmul_f(wv, z512), w2 = cmul_f(w1, z512);
    *(LAS u32*)t = 0x00003c00u;
    put(1, z1); put(2, z2); put(3, z3); put(4, z4); put(5, cmul_f(z4, z1)); put(6, cmul_f(z3, z3)); put(7, cmul_f(z4, z3)); put(8, z8);
    put(9, z16); put(10, cmul_f(z16, z8)); put(11, z32); put(12, z64); put(13, cmul_f(z64, z32));
    put(14, z128); put(15, z256); put(16, cmul_f(z256, z128));
    put(17, wv); put(18, w1); put(19, w2); put(20, cmul_f(w2, z512));
}
DEV u32 perm(u32 hi, u32 lo, u32 sel) { return __builtin_amdgcn_perm(hi, lo, sel); }

__global__ __launch_bounds__(512, 2) void fused_kernel(Params p) {
    __shared__ __attribute__((aligned(1024))) char shm[163840];
    LAS char* sm = (LAS char*)shm;
    const u32 lds0 = (u32)(uintptr_t)sm;
    const int wid = __builtin_amdgcn_readfirstlane(threadIdx.x >> 6);
    const int b = blockIdx.x, g = b & 7, r = b >> 3, hw = wid & 3;
    char* flags = p.ws;
    const int ch0 = 64 * g + 16 * (r & 3) + (r >> 2);
    const int tt = g * 4 + (r >> 3), dt = r & 7;
    const bool isG = r < 28;
    const int mt = (g >> 1) * 4 + r / 7, ft = (g & 1) * 7 + r % 7;
#define LANEVALS() \
    int tid = threadIdx.x; asm volatile("" : "+v"(tid)); \
    const int lane = tid & 63, h = lane >> 5, l31 = lane & 31; \
    const u32 vsw = (u32)(((lane & 7) ^ (((lane >> 4) & 1) << 2)) << 4);          \
    const u32 voffw2 = (u32)((lane >> 3) * 1024 + dt * 128) + vsw; \
    auto w2_dma = [&](auto thr, int kq, int sg) {        \
        glds_run<3, true, decltype(thr)::value>((const char*)p.w2 + (size_t)(4 * kq + sg) * (112 * 1024), 32768u, voffw2, voffw2 + 7168u, voffw2 + 14336u, voffw2 + 21504u, \
                                 lds0 + L_W2 + kq * W2_REG + (sg & 1) * 14336); \
    }; \
    const int blk = (lane >> 4) & 1, q4 = (lane & 15) >> 2, pp = lane & 3; \
      \
    const u32 tra0 = (u32)((4 * h + q4) * 128 + (((2 * blk + (pp >> 1)) ^ (((q4 >> 1) & 1) << 2)) << 4) + 8 * (pp & 1)); \
    const u32 xo = (u32)((tt * 64 + (tid >> 3)) * 1024 + dt * 128 + (tid & 7) * 16); \
    (void)l31; (void)voffw2; (void)w2_dma; (void)tra0; (void)xo;
    if (wid == 3) { const u32 ln = (u32)(r + 32 * (threadIdx.x & 7)); touch((const char*)(__builtin_amdgcn_s_getpc() & ~127ull), (ln < CODE_LINES ? ln : 0u) * 128u, lds0 + L_JUNK); }
    if (wid == 0) *(LAS u32*)(sm + L_MISC + 64 + (threadIdx.x & 31) * 4) = 0;
    u32x2 tag1;
    u32x4 xe, b2e;
#define WAIT_PARAMS(n) do { SCHED(); WAITV(n); SCHED(); asm volatile("" : "+v"(are), "+v"(aim), "+v"(bre), "+v"(bim), "+v"(cre), "+v"(cim), "+v"(ldt), "+v"(dsk)); } while (0)
#define LOAD_PARAMS(po, c2) \
    asm volatile("global_load_ushort %0, %8, %10\n\tglobal_load_ushort %1, %8, %11\n\tglobal_load_ushort %2, %8, %12\n\tglobal_load_ushort %3, %8, %13\n\t" \
                 "global_load_ushort %4, %8, %14\n\tglobal_load_ushort %5, %8, %15\n\tglobal_load_ushort %6, %9, %16\n\tglobal_load_ushort %7, %9, %17" \
                 : "=&v"(are), "=&v"(aim), "=&v"(bre), "=&v"(bim), "=&v"(cre), "=&v"(cim), "=&v"(ldt), "=&v"(dsk) \
                 : "v"(po), "v"(c2), "s"(p.Are), "s"(p.Aim), "s"(p.Bre), "s"(p.Bim), "s"(p.Cre), "s"(p.Cim), "s"(p.logdt), "s"(p.Dskip) : "memory")
#define LOADER_VALS() \
    const bool lx = wid == 4;                  \
    const u32 lR = (u32)(lane >> (3 + hw)) * (1024u + 2560u * hw); \
    const u32 lS = (u32)(((lane & (7 + 8 * hw)) ^ ((lane >> 4) << (2 * hw))) << 4); \
    const u32 lst = 7168u + 6144u * hw, x1 = 64u - 48u * hw, x2 = 32u * hw; \
    const u32 vo0 = lR + lS, vo1 = lR + (lS ^ x1) + lst, vo2 = lR + (lS ^ x2) + 2 * lst, vo3 = lR + (lS ^ (x1 | x2)) + 3 * lst; \
    const char* lsrc = (lx ? (const char*)p.x : (const char*)p.w1) + (u32)(lx ? mt * 131072 : ft * 256); \
    const u32 lgs = 32768u + 24576u * hw, lcs = 128u + (64u * 3584u - 128u) * hw;
#define READ_TAG() do { u32x4 t_ = *(volatile LAS u32x4*)(sm + L_TAG); \
        if (!all_ge(t_.y, 1)) { lds_wait(sm + L_TAG + 4, 1); t_ = *(volatile LAS u32x4*)(sm + L_TAG); } \
        tag1.x = __builtin_amdgcn_readfirstlane(t_.z); tag1.y = __builtin_amdgcn_readfirstlane(t_.w); } while (0)
    const int lj = g * 4 + (r - 28);

    const int tb8 = g * 32 + r;
    u32 are, aim, bre, bim, cre, cim, ldt, dsk, b1a, b1b;
    u32x2 tge;
    u32x4 xv[8];
    {
        const int lane = threadIdx.x & 63;
        if (wid == 7) {
                asm volatile("global_load_dwordx4 %0, %8, %10\n\tglobal_load_dwordx4 %1, %8, %10 offset:1024\n\tglobal_load_dwordx4 %2, %8, %10 offset:2048\n\tglobal_load_dwordx4 %3, %8, %10 offset:3072\n\t"
                             "global_load_dwordx4 %4, %9, %10\n\tglobal_load_dwordx4 %5, %9, %10 offset:1024\n\tglobal_load_dwordx4 %6, %9, %10 offset:2048\n\tglobal_load_dwordx4 %7, %9, %10 offset:3072"
                             : "=&v"(xv[0]), "=&v"(xv[1]), "=&v"(xv[2]), "=&v"(xv[3]), "=&v"(xv[4]), "=&v"(xv[5]), "=&v"(xv[6]), "=&v"(xv[7])
                             : "v"(lane * 16), "v"(lane * 16 + 4096), "s"((const char*)p.x + (size_t)tb8 * 8192) : "memory");
                asm volatile("global_load_dwordx2 %0, %1, %2 sc1" : "=&v"(tge) : "v"(FL_E * 128), "s"(flags) : "memory");
        }
        int w2_ = wid; asm volatile("" : "+s"(w2_));
        const int tw = w2_ == 6 ? 3 : w2_, chp = ch0 + 8 * (tw & 1);
        if ((0x47 >> w2_) & 1) LOAD_PARAMS((u32)(chp * 128 + (tw >> 1) * 64 + lane) * 2u, (u32)chp * 2u);
        if (w2_ < 4)
            asm volatile("global_load_ushort %0, %2, %3\n\tglobal_load_ushort %1, %2, %3 offset:64" : "=&v"(b1a), "=&v"(b1b) : "v"((u32)(ft * 128 + (tw >> 1) * 64 + (lane & 31)) * 2u), "s"(p.b1) : "memory");

        if ((w2_ & 6) != 4) BAR();
    }

    if (wid < 4) {
        LANEVALS();
        const int kq = wid;
        u32x4 wreg[14][2];
        LAS char* w2a = sm + L_W2 + kq * W2_REG;
        auto w2frag = [&](int s, int j2) { LAS char* a = w2a + (j2 ? tra0 ^ 64 : tra0) + s * 2048; return trfrag(a, a + 1024); };
        auto w2load = [&]() {
#pragma unroll
            for (int s = 0; s < 14; s++) { wreg[s][0] = w2frag(s, 0); wreg[s][1] = w2frag(s, 1); }
        };
#define W2PIN() _Pragma("unroll") for (int s = 0; s < 14; s++) asm volatile("" : "+v"(wreg[s][0]), "+v"(wreg[s][1]))
        LAS char* fw = sm + F_L + (wid & 2) * 4 + (lane & 1) * 4;
        bool early = false;
        const int th = wid & 1, fh = wid >> 1;
        u32 xa[4], wa[2][2];
#pragma unroll
        for (int s = 0; s < 4; s++) xa[s] = (u32)((64 * th + l31) * 128 + (((2 * s + h) ^ ((l31 >> 1) & 7)) << 4));
#pragma unroll
        for (int j2 = 0; j2 < 2; j2++)
#pragma unroll
            for (int t = 0; t < 2; t++)
                wa[j2][t] = (u32)(16384 + 256 * (8 * h + 4 * t + q4) + (((4 * (2 * fh + j2) + 2 * blk + (pp >> 1)) ^ ((q4 << 2) | (2 * h + t))) << 4) + 8 * (pp & 1));
        u32 so = GR_ST + (u32)(mt * 128 + th * 64 + l31) * 16u, co = GR_CS + (u32)(ft * 128 + fh * 64 + l31) * 8u;
        asm volatile("" : "+v"(so), "+v"(co), "+v"(xa[0]), "+v"(xa[1]), "+v"(xa[2]), "+v"(xa[3]), "+v"(wa[0][0]), "+v"(wa[0][1]), "+v"(wa[1][0]), "+v"(wa[1][1]));
        WAIT_PARAMS(2);

        if (wid < 3) build_table(sm, wid, lane, are, aim, bre, bim, cre, cim, ldt, dsk);
        LAS char* fm = sm + F_M + wid * 4;
        LAS char* fl = sm + F_L + (lane & 1) * 4;
        lds_post(fm, 1);
        SCHED(); WAITV(0); SCHED();
        asm volatile("" : "+v"(b1a), "+v"(b1b));

        if (isG) {
            f32x16 acc[2][2] = {};
            lds_wait(fl, 1);

            u32x4 X[4][2], W[4][2];
            u32 wp[2][2], xp;
#define RX(s, j) X[s][j] = *(LAS u32x4*)(sm + xp + (j) * 4096)
#define RW(s, j) W[s][j] = trfrag(sm + wp[j][0] + (s) * 4096, sm + wp[j][1] + (s) * 4096)
#define OPQ(v) asm volatile("" : "+v"(v))
#define MM(s, j2, j) acc[j2][j] = MFMA32(W[s][j2], X[s][j], acc[j2][j])
            u32x4 st0, st1; u32x2 cs0, cs1;
#define LOAD_GRAN() asm volatile("global_load_dwordx4 %0, %4, %6 sc1\n\tglobal_load_dwordx4 %1, %4, %6 offset:512 sc1\n\tglobal_load_dwordx2 %2, %5, %6 sc1\n\tglobal_load_dwordx2 %3, %5, %6 offset:256 sc1" \
                                 : "=&v"(st0), "=&v"(st1), "=&v"(cs0), "=&v"(cs1) : "v"(so), "v"(co), "s"(flags) : "memory")
            wp[0][0] = wa[0][0]; wp[0][1] = wa[0][1]; wp[1][0] = wa[1][0]; wp[1][1] = wa[1][1];
            xp = xa[0]; RX(0, 0); SCHED(); RW(0, 0); SCHED(); RW(0, 1); SCHED(); RX(0, 1); SCHED();
            xp = xa[1]; RX(1, 0); SCHED(); RW(1, 0); SCHED(); RW(1, 1); SCHED(); RX(1, 1); SCHED();
            xp = xa[2];
#pragma unroll
            for (int kc = 0; kc < 7; kc++) {
                const u32 sl = (u32)(kc & 3) * 32768u, sn = (u32)((kc + 1) & 3) * 32768u;
                if (kc == 6) LOAD_GRAN();
                MM(0, 0, 0); RX(2, 0); SCHED(); MM(0, 1, 0); RW(2, 0); SCHED(); MM(0, 0, 1); RW(2, 1); SCHED();
                MM(0, 1, 1); RX(2, 1); xp = xa[3] + sl; OPQ(xp); SCHED();
                MM(1, 0, 0); RX(3, 0);
                SCHED();
                MM(1, 1, 0); RW(3, 0); SCHED(); MM(1, 0, 1); RW(3, 1); SCHED();
                MM(1, 1, 1); RX(3, 1); xp = xa[0] + sn; wp[0][0] = wa[0][0] + sn; wp[0][1] = wa[0][1] + sn; OPQ(xp); OPQ(wp[0][0]); OPQ(wp[0][1]); SCHED();
                MM(2, 0, 0); SCHED();
                if (kc == 0) lds_wait(fl, 2); else RBAR();
                SCHED();
                RX(0, 0); wp[1][0] = wa[1][0] + sn; wp[1][1] = wa[1][1] + sn; OPQ(wp[1][0]); OPQ(wp[1][1]); SCHED();
                MM(2, 1, 0); RW(0, 0); SCHED(); MM(2, 0, 1); RW(0, 1); SCHED();
                MM(2, 1, 1); RX(0, 1); xp = xa[1] + sn; OPQ(xp); SCHED();
                MM(3, 0, 0); RX(1, 0); SCHED(); MM(3, 1, 0); RW(1, 0); SCHED(); MM(3, 0, 1); RW(1, 1); SCHED();
                MM(3, 1, 1); RX(1, 1); xp = xa[2] + sn; OPQ(xp);
                SCHED();

            }
            MM(0, 0, 0); RX(2, 0); RW(2, 0); SCHED(); MM(0, 1, 0); RW(2, 1); RX(2, 1); xp = xa[3] + 3 * 32768; SCHED();
            MM(0, 0, 1); RX(3, 0); RW(3, 0); SCHED(); MM(0, 1, 1); RW(3, 1); RX(3, 1); SCHED();
            MM(1, 0, 0); MM(1, 1, 0); SCHED();
            READ_TAG();
            lds_post(fm, 9);
            asm volatile("s_waitcnt vmcnt(0)" : "+v"(st0), "+v"(st1), "+v"(cs0), "+v"(cs1) :: "memory");
#pragma unroll 1
            for (int spin = 0; spin < (1 << 16); ++spin) {
                if (__builtin_amdgcn_ballot_w64((((st0.y ^ tag1.x) | (st0.w ^ tag1.x) | (st1.y ^ tag1.x)) | ((st1.w ^ tag1.x) | (cs0.y ^ tag1.x) | (cs1.y ^ tag1.x))) != 0u) == 0) break;
                __builtin_amdgcn_s_sleep(2);
                LOAD_GRAN();
                asm volatile("s_waitcnt vmcnt(0)" : "+v"(st0), "+v"(st1), "+v"(cs0), "+v"(cs1) :: "memory");
            }

            float rinv[2] = {__uint_as_float(st0.z), __uint_as_float(st1.z)};
            float k1[2], k2[2];
            {
                u32x4 Ax[2], Bx[2];
                auto prepB = [&](int j) {
                    const float nm = __uint_as_float(j ? st1.x : st0.x), m1 = rbf(nm), mr = opq(nm - m1), m2 = rbf(mr), ih = rbf(rinv[j]);
                    const u32 mm = pkbf(m1, m2);
                    const u32 hm = (u32)h - 1u;
                    Bx[j] = (u32x4){mm & hm, mm & hm, pkbf(opq(mr - m2), m1) & hm, pkbf(ih, opq(rinv[j] - ih)) & hm};
                };
                auto prepA = [&](int j) {
                    const float c = __uint_as_float(j ? cs1.x : cs0.x), c1 = rbf(c), cr = opq(c - c1), c2 = rbf(cr);
                    const u32 bb = j ? b1b : b1a;
                    Ax[j] = (u32x4){pkbf(c1, c1), pkbf(c2, c2), pkbf(c1, opq(cr - c2)), bb | (bb << 16)};
                };
                auto prepK = [&](int j) {
                    const float rr = __builtin_amdgcn_rcpf(rinv[j]);
                    k1[j] = opq(-2.302208198144325f * rr); k2[j] = opq(-0.10294323958002349f * rr * rr * rr);
                };
                MM(1, 0, 1); MM(1, 1, 1); prepB(0); SCHED();
                MM(2, 0, 0); prepA(0); SCHED(); MM(2, 1, 0); prepA(1); SCHED(); MM(2, 0, 1); MM(2, 1, 1); prepB(1); SCHED();
                MM(3, 0, 0); prepK(0); SCHED(); acc[0][0] = MFMA32(Ax[0], Bx[0], acc[0][0]); prepK(1); SCHED();
                MM(3, 1, 0); acc[1][0] = MFMA32(Ax[1], Bx[0], acc[1][0]);
                MM(3, 0, 1); acc[0][1] = MFMA32(Ax[0], Bx[1], acc[0][1]);
                MM(3, 1, 1); acc[1][1] = MFMA32(Ax[1], Bx[1], acc[1][1]);
                SCHED();

            }
            PRIO(2);

            char* hdst = p.ws + WS_H + (size_t)((mt * 4 + th * 2) * 112 + ft * 8 + fh * 4) * 1024;
#pragma unroll
            for (int j = 0; j < 2; j++) {
                const f32x2 K1 = {k1[j], k1[j]}, K2 = {k2[j], k2[j]}, RI = {rinv[j], rinv[j]};
#pragma unroll
                for (int j2 = 0; j2 < 2; j2++) {
#pragma unroll
                    for (int s = 0; s < 2; s++) {
                        u32x4 o;
#pragma unroll
                        for (int i = 0; i < 4; i++) {
                            const f32x2 u = {acc[j2][j][8 * s + 2 * i], acc[j2][j][8 * s + 2 * i + 1]};
                            const f32x2 z = __builtin_elementwise_fma(u * u, K2, K1) * u;
                            const f32x2 e = {__builtin_amdgcn_exp2f(z.x), __builtin_amdgcn_exp2f(z.y)};
                            const f32x2 dn = __builtin_elementwise_fma(e, RI, RI);
                            const f32x2 gl = u * (f32x2){__builtin_amdgcn_rcpf(dn.x), __builtin_amdgcn_rcpf(dn.y)};
                            o[i] = pkbf(gl.x, gl.y);
                        }
                        asm volatile("global_store_dwordx4 %0, %1, %2 offset:%3 sc1\n\ts_nop 1" :: "v"(lane * 16 + j * 112 * 1024), "v"(o), "s"(hdst), "i"((j2 * 2 + s) * 1024) : "memory");
                        SCHED();
                        if (j == 1 && j2 == 1 && s == 0) { early = all_ge(*(volatile LAS u32*)fw, 12); asm volatile("" ::: "memory"); if (early) w2load(); SCHED(); }
                    }
                }
            }
            if (early) { W2PIN(); }
            lds_post(fm, 10);
        } else {
            lds_wait(sm + F_P + 12, 1);
            {
                const u32 v0 = (u32)lane * 16u;
                glds_run<3, true, MTHR>((const char*)p.w2 + (size_t)(lj * 4 + wid) * 14336, 4096u, v0, v0, v0, v0, lds0 + 65536);
            }
            WAITV(0); lds_post(fm, 10); READ_TAG();
        }
        PRIO(1);
        if (isG) { WAITV(0); flag_store(flags, (u32)(FL_H + (mt * 2 + th) * 32 + ft * 2 + fh) * 128u, tag1); }
        if (!early) {
            lds_wait(fw, 12);
            w2load();
            W2PIN();
            if (!isG) lds_wait(fl, 13);
            WAITL0();
        }
        w2_dma(IC<MTHR>(), kq, 2);
        w2_dma(IC<MTHR>(), kq, 3);
        poll_flags(flags, FL_H + tt * 32 + 7 * kq, 1, 7, tag1, lane);

        const __amdgpu_buffer_rsrc_t hr = __builtin_amdgcn_make_buffer_rsrc(p.ws + WS_H + (size_t)(tt * 2 * 112 + kq * 28) * 1024, 0, 2 * 112 * 1024, 0x00020000);
        u32x4 hf[WIN][2];
        f32x16 acc[2][2] = {};
        auto ldh = [&](int s, int j) {
            return __builtin_amdgcn_raw_buffer_load_b128(hr, lane * 16 + j * 112 * 1024 + (s & 3) * 1024, (s >> 2) * 4096, 16);
        };
#pragma unroll
        for (int s = 0; s < WIN; s++) { hf[s][0] = ldh(s, 0); SCHED(); hf[s][1] = ldh(s, 1); SCHED(); }

#pragma unroll
        for (int s = 0; s < 28; s++) {
            const u32x4 w0 = wreg[s % 14][0], w1 = wreg[s % 14][1];
            acc[0][0] = MFMA32(w0, hf[s % WIN][0], acc[0][0]); SCHED();
            acc[1][0] = MFMA32(w1, hf[s % WIN][0], acc[1][0]);
            if (s + WIN < 28) hf[s % WIN][0] = ldh(s + WIN, 0);
            SCHED();
            acc[0][1] = MFMA32(w0, hf[s % WIN][1], acc[0][1]);
            if (s >= 12 && s < 26) wreg[s - 12][0] = w2frag(s - 12, 0);
            SCHED();
            acc[1][1] = MFMA32(w1, hf[s % WIN][1], acc[1][1]);
            if (s >= 12 && s < 26) wreg[s - 12][1] = w2frag(s - 12, 1);
            if (s + WIN < 28) hf[s % WIN][1] = ldh(s + WIN, 1);
            if (s + WIN == 28) { LANEVALS(); xe = *(const u32x4*)((const char*)p.x + xo); b2e = *(const u32x4*)((const char*)p.b2 + dt * 128 + (tid & 7) * 16); }
            SCHED();
        }

        int t2 = threadIdx.x; asm volatile("" : "+v"(t2));
        LAS char* pw = w2a + (t2 & 31) * 272 + ((t2 >> 5) & 1) * 16;
#pragma unroll
        for (int j2 = 0; j2 < 2; j2++)
#pragma unroll
            for (int j = 0; j < 2; j++)
#pragma unroll
                for (int rg = 0; rg < 4; rg++) {
                    f32x4 v = {acc[j2][j][4 * rg], acc[j2][j][4 * rg + 1], acc[j2][j][4 * rg + 2], acc[j2][j][4 * rg + 3]};
                    *(LAS f32x4*)(pw + 32 * j * 272 + (32 * j2 + 8 * rg) * 4) = v;
                }

    } else {
        LANEVALS();
        const int a = lane & 15, q = lane >> 4;
        const int cl = hw & 1, rh = hw >> 1, chn = ch0 + 8 * cl;
        if (wid >= 6) {
            const int kc = wid & 1;
            PRIO(1);
            u32 junk = 0;
            if (kc == 0) {

                asm volatile("s_nop 4\n\tglobal_load_dword %0, %1, %2" : "+v"(junk) : "v"((u32)(lane < 56 ? lane : 0) * 128u), "s"(uni((const char*)p.w1 + (size_t)b * 7168)) : "memory");
                WAIT_PARAMS(1);

                build_table(sm, 3, lane, are, aim, bre, bim, cre, cim, ldt, dsk);
                lds_post(sm + F_P + 8, 1);
                asm volatile("s_nop 4\n\tglobal_load_dword %0, %1, %2" : "+v"(junk) : "v"((u32)(lane < 56 ? lane : 0) * 128u), "s"(uni((const char*)p.w2 + (size_t)b * 7168)) : "memory");
                READ_TAG();

            } else {
                PRIO(3);

                asm volatile("s_waitcnt vmcnt(0)" : "+v"(tge), "+v"(xv[0]), "+v"(xv[1]), "+v"(xv[2]), "+v"(xv[3]), "+v"(xv[4]), "+v"(xv[5]), "+v"(xv[6]), "+v"(xv[7]) :: "memory");
                tag1.x = __builtin_amdgcn_readfirstlane(tge.x) + 1u; tag1.y = __builtin_amdgcn_readfirstlane(tge.y);

                *(volatile LAS u32x2*)(sm + L_TAG + 8) = tag1;
                lds_post(sm + L_TAG + 4, 1);
                float s1[8], s2[8];
#pragma unroll
                for (int i = 0; i < 8; i++) { s1[i] = 0.f; s2[i] = 0.f; }
#pragma unroll
                for (int j = 0; j < 4; j++)
#pragma unroll
                    for (int i = 0; i < 8; i++) { s1[i] = dot2(xv[i][j], 0x3f803f80u, s1[i]); s2[i] = dot2(xv[i][j], xv[i][j], s2[i]); }
                auto red8 = [&](float (&v)[8]) {
                    float a[4], c[2];
#pragma unroll
                    for (int i = 0; i < 4; i++) {
                        const auto t = __builtin_amdgcn_permlane32_swap(__float_as_uint(v[i]), __float_as_uint(v[i + 4]), false, false);
                        a[i] = __uint_as_float(t[0]) + __uint_as_float(t[1]);
                    }
#pragma unroll
                    for (int i = 0; i < 2; i++) {
                        const auto t = __builtin_amdgcn_permlane16_swap(__float_as_uint(a[i]), __float_as_uint(a[i + 2]), false, false);
                        c[i] = dpp_add<0x128, 0xf>(__uint_as_float(t[0]) + __uint_as_float(t[1]));
                    }
                    float e = (lane & 8) ? c[1] : c[0];
                    e = dpp_add<0x141, 0xf>(e); e = dpp_add<0xB1, 0xf>(e); e = dpp_add<0x4E, 0xf>(e);
                    return e;
                };
                const float a1 = red8(s1), a2 = red8(s2);
                const float mean = a1 * (1.f / 512.f);
                const float v1 = fmaxf(a2 * (1.f / 512.f) - mean * mean, 0.f);
                const float q1 = v1 + 1e-5f;
                const float rs = __builtin_amdgcn_rsqf(q1);
                const float q2 = v1 * rs * rs + 1e-5f;
                f32x2 sc[4], of[4];
                const float nmr = -mean * rs;
#pragma unroll
                for (int i = 0; i < 4; i++) {
                    sc[i] = (f32x2){__builtin_bit_cast(float, __builtin_amdgcn_readlane(__builtin_bit_cast(int, rs), 16 * i)), __builtin_bit_cast(float, __builtin_amdgcn_readlane(__builtin_bit_cast(int, rs), 16 * i + 8))};
                    of[i] = (f32x2){__builtin_bit_cast(float, __builtin_amdgcn_readlane(__builtin_bit_cast(int, nmr), 16 * i)), __builtin_bit_cast(float, __builtin_amdgcn_readlane(__builtin_bit_cast(int, nmr), 16 * i + 8))};
                }
                u32x4 ob[8];
#pragma unroll
                for (int c = 0; c < 8; c++) {
                    u32x4 o;
#pragma unroll
                    for (int i = 0; i < 4; i++) {
                        const u32 a = xv[2 * i][c >> 1], d = xv[2 * i + 1][c >> 1];
                        const f32x2 v = (c & 1) ? (f32x2){bfhi(a), bfhi(d)} : (f32x2){bflo(a), bflo(d)};
                        const f32x2 n = __builtin_elementwise_fma(v, sc[i], of[i]);
                        o[i] = pkh(n.x, n.y);
                    }
                    ob[c] = o;
                }
                {
                    asm volatile("" : "+v"(ob[0]), "+v"(ob[1]), "+v"(ob[2]), "+v"(ob[3]), "+v"(ob[4]), "+v"(ob[5]), "+v"(ob[6]), "+v"(ob[7]));

                {
                    u32x4 o = {__float_as_uint(-mean), tag1.x, __float_as_uint(__builtin_amdgcn_sqrtf(q1 * q2)), tag1.x};
                    u64 keep;
                    asm volatile("s_mov_b64 %0, exec\n\ts_mov_b64 exec, %4\n\tglobal_store_dwordx4 %1, %2, %3 sc1\n\ts_mov_b64 exec, %0\n\ts_nop 1"
                                 : "=&s"(keep) : "v"(GR_ST + (u32)(tb8 * 8 + (lane >> 3)) * 16u), "v"(o), "s"(flags), "s"(0x0101010101010101ull) : "memory");
                }
#pragma unroll
                    for (int c = 0; c < 8; c++) astore_b128_sc1(p.ws + WS_U + (size_t)tb8 * 8192, (u32)(c * 1024 + lane * 16), ob[c]);
                }
                PRIO(1);

            }
            const int rS = (int)((0x8080840876543210ull >> (4 * a)) & 15), rB = 17 + (int)((0xfa540000u >> (2 * a)) & 3);
            const int rT1 = (a & 3) ? 7 + (a & 3) : 0, rT2 = (a >> 2) ? 10 + (a >> 2) : 0;
            LAS char* tb = sm + L_TAB + kc * TAB_SZ + q * 16;
            LAS char *pS = tb + rS * TAB_ROW, *pB = tb + rB * TAB_ROW, *pT1 = tb + rT1 * TAB_ROW, *pT2 = tb + rT2 * TAB_ROW, *pZ = tb + 14 * TAB_ROW;
            const int hd = tab_off(kc + 2) - tab_off(kc);
            f32x4 G[2][4] = {};
            u32x4 T[2][7];
            auto ldtab = [&](int k) {
                if (k == 4) { pS += hd; pB += hd; pT1 += hd; pT2 += hd; pZ += hd; }
                u32x4* t = T[k & 1];
                const int o = (k & 3) * 64;
                t[0] = *(LAS u32x4*)(pS + o); t[1] = *(LAS u32x4*)(pB + o); t[2] = *(LAS u32x4*)(pT1 + o); t[3] = *(LAS u32x4*)(pT2 + o);
                t[4] = *(LAS u32x4*)(pZ + o); t[5] = *(LAS u32x4*)(pZ + o + TAB_ROW); t[6] = *(LAS u32x4*)(pZ + o + 2 * TAB_ROW);
            };
            int nb = isG ? 0 : 6; bool go = false;
#define SYNCPT() do { if (nb < 6) { if (go) { RBAR(); nb++; } else go = all_ge(*(volatile LAS u32*)(sm + F_L + (lane & 1) * 4), 2); } } while (0)
            lds_wait(sm + F_M + (lane & 3) * 4, 1);

            ldtab(0);

#pragma unroll
            for (int k = 0; k < 8; k++) {
                u32x4* t = T[k & 1];
                f32x4* Gh = G[k >> 2];
                u32x4 A, B0, B1, B2, B3;
                asm volatile("" :: "v"(t[0]), "v"(t[1]), "v"(t[2]), "v"(t[3]), "v"(t[4]), "v"(t[5]), "v"(t[6]));
                if (k == 3) lds_wait(sm + F_P + 8, 1);

                if (k < 7) ldtab(k + 1);

                SCHED();
#pragma unroll
                for (int j = 0; j < 4; j++) { B0[j] = cmh2<false>(t[2][j], t[3][j], cmh1<false>(t[2][j], t[3][j])); A[j] = cmh2<true>(t[0][j], t[1][j], cmh1<true>(t[0][j], t[1][j])); }
                SCHED();
#pragma unroll
                for (int j = 0; j < 4; j++) B1[j] = cmh2<false>(B0[j], t[4][j], cmh1<false>(B0[j], t[4][j]));
                SCHED();
                Gh[0] = MFMA16H(A, B0, Gh[0]);
#pragma unroll
                for (int j = 0; j < 4; j++) B2[j] = cmh2<false>(B0[j], t[5][j], cmh1<false>(B0[j], t[5][j]));
                SCHED();
                Gh[1] = MFMA16H(A, B1, Gh[1]);
#pragma unroll
                for (int j = 0; j < 4; j++) B3[j] = cmh2<false>(B0[j], t[6][j], cmh1<false>(B0[j], t[6][j]));
                SCHED();
                Gh[2] = MFMA16H(A, B2, Gh[2]);
                asm volatile("s_nop 1" : "+v"(B3));
                Gh[3] = MFMA16H(A, B3, Gh[3]);
                SCHED();
                if (k == 1 && kc) { WAITV(0); flag_store(flags, (u32)(FL_LN * 128 + tb8 * 8), tag1); if (!isG) lds_post(sm + F_P + 12, 1); }
                SYNCPT();
            }

            {
                const float M0 = *(LAS float*)(sm + L_MISC + kc * 4), M1 = *(LAS float*)(sm + L_MISC + 8 + kc * 4), M = fmaxf(M0, M1);
                const float s0 = __builtin_amdgcn_exp2f(M0 - M), s1 = __builtin_amdgcn_exp2f(M1 - M);
                const float dx = *(LAS float*)(sm + L_MISC + 16 + kc * 4) * __builtin_amdgcn_exp2f(-M), d0 = lane == 0 ? dx : 0.f;
                LAS char* gx = sm + L_TAB + kc * TAB_SZ;
#pragma unroll
                for (int ct = 0; ct < 4; ct++) {
                    float v[4];
#pragma unroll
                    for (int e = 0; e < 4; e++) v[e] = opq(fmaf(G[1][ct][e], s1, (ct | e) ? opq(G[0][ct][e] * s0) : fmaf(G[0][ct][e], s0, d0)));
                    u32x2 o = {pkh(v[0], v[1]), pkh(v[2], v[3])};
                    *(LAS u32x2*)(gx + (16 * ct + a) * 32 + q * 8) = o;
                }
                SYNCPT();
                WAITL0();
                const u32x4 u = *(LAS u32x4*)(gx + lane * 32), v = *(LAS u32x4*)(gx + lane * 32 + 16);
                LAS char* kp = sm + L_KP + kc * KP_CH + (lane & 1) * KP_PLANE + (16 + (lane >> 1)) * 16;
                const u32 LL = 0x01000100u, HH = 0x03020302u, SW = 0x01000302u;
                const u32 sa = h ? 0x05040100u : 0x05040706u, sb = h ? LL : SW, tie = (lane & 1) ? HH : SW;
                u32x4 w0 = {perm(u.w, v.x, sa), perm(0, u.z, sb), perm(u.y, u.z, sa), perm(0, u.x, sb)};
                u32x4 w1 = {perm(0, v.y, HH), perm(0, v.y, LL), perm(v.x, v.y, 0x07060100u), perm(0, v.x, HH)};
                u32x4 w2 = {perm(0, v.z, HH), perm(0, v.z, tie), perm(0, v.z, LL), perm(0, v.z, LL)};
                u32x4 w3 = {perm(0, v.w, HH), perm(0, v.w, tie), perm(0, v.w, LL), perm(0, v.w, LL)};
                *(LAS u32x4*)kp = w0; *(LAS u32x4*)(kp + 512) = w1; *(LAS u32x4*)(kp + 1024) = w2; *(LAS u32x4*)(kp + 1536) = w3;
                u32x4 z = {0, 0, 0, 0};
                *(LAS u32x4*)(sm + L_KP + kc * KP_CH + (q & 1) * KP_PLANE + a * 16) = z;
            }
            lds_post(sm + F_K + kc * 4, 1);

            while (nb < 6) { RBAR(); nb++; }
            lds_wait(sm + F_M + (lane & 3) * 4, 9);
            if (!isG) lds_wait(sm + F_L + (lane & 1) * 4, 2);
            w2_dma(IC<LTHR>(), 2, kc);
            w2_dma(IC<LTHR>(), 3, kc);

            asm volatile("s_waitcnt vmcnt(0)" : "+v"(junk) :: "memory");
            lds_post(sm + F_K + kc * 4, 12);

        } else {
            LOADER_VALS();
            LAS char* fm = sm + F_M + (lane & 3) * 4;
            LAS char* fl = sm + F_L + hw * 4;
            asm volatile("" :: "v"(vo0), "v"(vo1), "v"(vo2), "v"(vo3), "s"(lsrc), "s"(lgs));
            RBAR();

            if (__builtin_expect(isG, 1)) glds_run<4, false, LTHR>(lsrc, lgs, vo0, vo1, vo2, vo3, lds0 + hw * 16384);
            const int tb0 = hw * 128 + (lane >> 1);
            u32 ob = (u32)((r >> 2) * 1024 + (8 * g + 2 * (r & 3) + (lane & 1)) * 16), o0 = ob + (u32)tb0 * 8192u;
            u32x4 uv[4], f0, f1; u32x2 pv;
#define LOAD_LNF() asm volatile("global_load_dwordx4 %0, %2, %3 sc1\n\tglobal_load_dwordx4 %1, %2, %3 offset:1024 sc1" : "=&v"(f0), "=&v"(f1) : "v"(lfo), "s"(flags) : "memory")
#define WAIT_LNF() asm volatile("s_waitcnt vmcnt(0)" : "+v"(f0), "+v"(f1) :: "memory")
#define LNF_OK() (__builtin_amdgcn_ballot_w64((((f0.x ^ tag1.x) | (f0.z ^ tag1.x) | (f1.x ^ tag1.x) | (f1.z ^ tag1.x)) | ((f0.y ^ tag1.y) | (f0.w ^ tag1.y) | (f1.y ^ tag1.y) | (f1.w ^ tag1.y))) != 0u) == 0)
#define LOAD_U(B, M) do { _Pragma("unroll") for (int i = 0; i < 4; i++) uv[i] = aload_b128_sc1(B, (o0 + i * 32 * 8192) & (M)); pv = aload_b64_sc1(B, (ob + 127u * 8192u + 8u) & (M)); } while (0)
            u32 lfo = (u32)(FL_LN * 128 + lane * 16);
            asm volatile("" : "+v"(ob), "+v"(o0), "+v"(lfo));
            if (__builtin_expect(isG, 1)) {
                const char* nsrc = lsrc + lcs;
#pragma unroll 1
                for (int kc = 1; kc < 8; kc++) {
                    u32 dst = lds0 + (kc & 3) * 32768 + hw * 16384;
                    asm volatile("" : "+s"(nsrc), "+s"(dst));
                    if (kc >= 4) RBAR();

                    glds_run<4, false, LTHR>(nsrc, lgs, vo0, vo1, vo2, vo3, dst);
                    nsrc += lcs;
                    WAITV(16);
                    if (kc < 3) *(volatile LAS u32*)fl = kc;
                    if (kc == 3) READ_TAG();
                }
                RBAR();

            } else {
                const char* wb = (const char*)p.w1 + (lj < 28 ? lj : 0) * 128 + (size_t)hw * 256 * 3584;
                const u32 v0 = (u32)((lane >> 3) * 3584) + vsw;
                glds_run<8, false, LTHR>(wb, 32u * 3584u, v0, v0 + 27648u, v0 + 55296u, v0 + 82944u, lds0 + hw * 32768);
                f32x16 c0 = {}, c1 = {};
                const u32x4 ones = {0x3f803f80u, 0x3f803f80u, 0x3f803f80u, 0x3f803f80u};
#pragma unroll 1
                for (int s0 = 0; s0 < 16; s0 += 8) {
                    u32x4 fa[8], fb[8];
                    if (s0 == 0) { WAITV(16); } else { WAITV(0); }
#pragma unroll
                    for (int s = 0; s < 8; s++) {
                        LAS char* a = sm + hw * 32768 + (s0 + s) * 2048;
                        fa[s] = trfrag(a + tra0, a + tra0 + 1024); fb[s] = trfrag(a + (tra0 ^ 64), a + (tra0 ^ 64) + 1024);
                    }
                    SCHED();
#pragma unroll
                    for (int s = 0; s < 8; s++) { c0 = MFMA32(ones, fa[s], c0); c1 = MFMA32(ones, fb[s], c1); }
                }
                const float c = h ? c1[0] : c0[0];
                *(LAS float*)(sm + L_CS + hw * 256 + lane * 4) = c;
                lds_post(fl, 1);
                READ_TAG();
                lds_wait(sm + F_L + (lane & 1) * 4, 1);
                u32x2 o = {__float_as_uint(*(LAS float*)(sm + L_CS + lane * 4) + *(LAS float*)(sm + L_CS + 256 + lane * 4)), tag1.x};
                if (hw == 0 && lj < 28) astore_b64_sc1(flags, GR_CS + (u32)(lj * 64 + lane) * 8u, o);

                lds_post(fl, 2); lds_wait(fm, 10);
                __builtin_amdgcn_s_sleep(16);
            }
            LOAD_LNF();
            w2_dma(IC<LTHR>(), 0, hw);
            WAITV(16);
            if (isG) RBAR();

            asm volatile("s_waitcnt vmcnt(14)" : "+v"(f0), "+v"(f1) :: "memory");
            const bool uok = LNF_OK();
            { const char* ub = uok ? (const char*)p.ws + WS_U : (const char*)p.b2; LOAD_U(ub, uok ? ~0u : 0u); }
            w2_dma(IC<LTHR>(), 1, hw);

            asm volatile("s_waitcnt vmcnt(0)" : "+v"(uv[0]), "+v"(uv[1]), "+v"(uv[2]), "+v"(uv[3]), "+v"(pv) :: "memory");
            lds_post(fl, 12);

            if (!uok) {
#pragma unroll 1
                for (int spin = 0; spin < (1 << 16); ++spin) {
                    LOAD_LNF(); WAIT_LNF();
                    if (LNF_OK()) break;
                    __builtin_amdgcn_s_sleep(6);
                }

                LOAD_U(p.ws + WS_U, ~0u);
                asm volatile("s_waitcnt vmcnt(0)" : "+v"(uv[0]), "+v"(uv[1]), "+v"(uv[2]), "+v"(uv[3]), "+v"(pv) :: "memory");
            }
            if (b == 0 && hw == 0) flag_store(flags, FL_E * 128, tag1);
            PRIO(3);
            lds_wait(fm, 9);
            lds_wait(sm + F_K + (lane & 1) * 4, 1);

            u32 r2[5], r3[5];
            r2[0] = hw ? pv.x : 0u; r3[0] = hw ? pv.y : 0u;
#pragma unroll
            for (int i = 0; i < 4; i++) {
                r2[i + 1] = __builtin_amdgcn_ds_bpermute(((lane - 2) & 63) * 4, uv[i].z);
                r3[i + 1] = __builtin_amdgcn_ds_bpermute(((lane - 2) & 63) * 4, uv[i].w);
            }
            LAS char* d = sm + L_UC + (lane & 1) * UC_CH + (tb0 + 4) * 16;
#pragma unroll
            for (int i = 0; i < 4; i++) *(LAS u32x4*)(d + i * 512) = uv[i];
#pragma unroll
            for (int i = 0; i < 4; i++) {
                const u32x4 t = uv[i];
                const u32 q2 = lane < 2 ? r2[i] : r2[i + 1], q3 = lane < 2 ? r3[i] : r3[i + 1];
                const u32 a0 = __builtin_amdgcn_alignbit(q3, q2, 16), a1 = __builtin_amdgcn_alignbit(t.x, q3, 16), a2 = __builtin_amdgcn_alignbit(t.y, t.x, 16),
                          a3 = __builtin_amdgcn_alignbit(t.z, t.y, 16), a4 = __builtin_amdgcn_alignbit(t.w, t.z, 16);
                u32x4 c1 = {a1, a2, a3, a4}, c2 = {q3, t.x, t.y, t.z}, c3 = {a0, a1, a2, a3};
                *(LAS u32x4*)(d + i * 512 + UC_COPY) = c1;
                *(LAS u32x4*)(d + i * 512 + 2 * UC_COPY) = c2;
                *(LAS u32x4*)(d + i * 512 + 3 * UC_COPY) = c3;
            }
            u32x4 z = {0, 0, 0, 0};
            *(LAS u32x4*)(sm + L_UC + hw * UC_CH + ((lane >> 2) & 3) * UC_COPY + (lane & 3) * 16) = z;
            lds_post(fl, 13);

        }
        lds_wait(sm + F_L + (lane & 1) * 4, 13);

        LAS char* ub = sm + L_UC + cl * UC_CH + (3 - (a & 3)) * UC_COPY + (32 * rh + 2 * q + 5 + (a >> 2)) * 8;
        LAS char* kb = sm + L_KP + cl * KP_CH + ((q + 1) & 1) * KP_PLANE + (16 - 8 * rh - 6 + a - (q >> 1)) * 16;
        f32x4 y[8] = {};
        auto ldA = [&](int dl, int i) {
            const u32x2 lo = *(volatile LAS u32x2*)(ub + dl * 64 + i * 512), hi = *(volatile LAS u32x2*)(ub + dl * 64 + i * 512 + 8);
            return (u32x4){lo.x, lo.y, hi.x, hi.y};
        };
        auto ldB = [&](int dl, int i) { return *(LAS u32x4*)(kb + (3 - dl) * 32 + i * 256); };
        u32x4 A0[8], B0[8], A1[8], B1[8];
#pragma unroll
        for (int i = 0; i < 8; i++) { B0[i] = ldB(0, i); A0[i] = ldA(0, i); }
        lds_wait(sm + F_M + (lane & 3) * 4, 10);
        PRIO(2);

        auto cstep = [&](u32x4 (&A)[8], u32x4 (&B)[8], u32x4 (&An)[8], u32x4 (&Bn)[8], int dn) {
            asm volatile("" :: "v"(A[0]), "v"(A[1]), "v"(A[2]), "v"(A[3]), "v"(A[4]), "v"(A[5]), "v"(A[6]), "v"(A[7]));
            asm volatile("" :: "v"(B[0]), "v"(B[1]), "v"(B[2]), "v"(B[3]), "v"(B[4]), "v"(B[5]), "v"(B[6]), "v"(B[7]));
            SCHED();
            int n = 0;
#pragma unroll
            for (int gm = 0; gm < 8; gm++)
#pragma unroll
                for (int c = gm; c < 8; c++) {
                    y[c] = MFMA16H(A[gm], B[c - gm], y[c]);
                    if (dn < 4 && n < 24) { if (n % 3 == 0) Bn[n / 3] = ldB(dn, n / 3); else if (n % 3 == 1) An[n / 3] = ldA(dn, n / 3); }
                    n++;
                    SCHED();
                }
        };
        cstep(A0, B0, A1, B1, 1); cstep(A1, B1, A0, B0, 2); cstep(A0, B0, A1, B1, 3); cstep(A1, B1, A0, B0, 4);
        PRIO(3);

        const float ksc = __builtin_amdgcn_exp2f(fmaxf(*(LAS float*)(sm + L_MISC + cl * 4), *(LAS float*)(sm + L_MISC + 8 + cl * 4)));
        LAS char* fh = sm + F_H + hw * 4;
        LAS char* fp = sm + F_H + (hw ^ 2) * 4;
        lds_post(fh, 1); lds_wait(fp, 1);

        LAS char* xb = sm + L_UC + cl * UC_CH + lane * 16;
        if (rh) {
#pragma unroll
            for (int c = 0; c < 4; c++) *(LAS f32x4*)(xb + 4096 + c * 1024) = y[c];
            lds_post(fh, 2);
#pragma unroll
            for (int c = 0; c < 4; c++)
#pragma unroll
                for (int e = 0; e < 4; e++) y[c][e] = opq(y[4 + c][e] * ksc);
        } else {
#pragma unroll
            for (int c = 0; c < 4; c++) *(LAS f32x4*)(xb + c * 1024) = y[4 + c];
            lds_post(fh, 2);
#pragma unroll
            for (int c = 0; c < 4; c++)
#pragma unroll
                for (int e = 0; e < 4; e++) y[c][e] = opq(y[c][e] * ksc);
        }
        char* sdst = p.ws + WS_S + (size_t)chn * 4096 + rh * 2048;

        lds_wait(fp, 2);

        {
            u32x2 o[4];
#pragma unroll
            for (int c = 0; c < 4; c++) {
                const f32x4 v = *(LAS f32x4*)(xb + (rh ^ 1) * 4096 + c * 1024);
                float f[4];
#pragma unroll
                for (int e = 0; e < 4; e++) f[e] = opq(fmaf(v[e], ksc, y[c][e]));
                o[c] = (u32x2){pkbf(f[0], f[1]), pkbf(f[2], f[3])};
            }
#pragma unroll
            for (int cp = 0; cp < 2; cp++) {
                const auto lo = __builtin_amdgcn_permlane16_swap(o[2 * cp].x, o[2 * cp + 1].x, false, false);
                const auto hi = __builtin_amdgcn_permlane16_swap(o[2 * cp].y, o[2 * cp + 1].y, false, false);
                astore_b128_sc1(sdst, (u32)(cp * 1024 + lane * 16), (u32x4){lo[0], hi[0], lo[1], hi[1]});
            }
        }

        WAITV(0);
        lds_post(fh, 3);
        lds_wait(sm + F_H + (lane & 3) * 4, 3);
        if (wid == 4) flag_store(flags, (u32)(FL_S + b) * 128u, tag1);
        PRIO(0);

        xe = *(const u32x4*)((const char*)p.x + xo); b2e = *(const u32x4*)((const char*)p.b2 + dt * 128 + (tid & 7) * 16);
        __builtin_amdgcn_s_sleep(16);
        poll_flags(flags, FL_S + dt + 8 * hw, 32, 8, tag1, lane);

        u32x4 sv[2];
#pragma unroll
        for (int i = 0; i < 2; i++) {
            const int d = hw * 16 + i * 8 + (lane >> 3);
            sv[i] = aload_b128_sc1(p.ws + WS_S, (u32)((dt * 64 + d) * 4096 + (tt >> 3) * 1024 + ((tt >> 2) & 1) * 256 + ((lane >> 2) & 1) * 512 + (tt & 3) * 64 + (lane & 3) * 16));
        }
        asm volatile("s_waitcnt vmcnt(0)" : "+v"(sv[0]), "+v"(sv[1]) :: "memory");
#pragma unroll
        for (int i = 0; i < 2; i++) {
            const int d = hw * 16 + i * 8 + (lane >> 3);
            *(LAS u32x4*)(sm + L_UC + d * 128 + (((2 * (lane & 3) + ((lane >> 2) & 1)) ^ (d >> 3)) << 4)) = sv[i];
        }

    }
    {
        LANEVALS();
        const int tok = tid >> 3, dg = tid & 7;
        float m[8], xf[8];
#pragma unroll
        for (int e = 0; e < 4; e++) { m[2 * e] = bflo(b2e[e]); m[2 * e + 1] = bfhi(b2e[e]); xf[2 * e] = bflo(xe[e]); xf[2 * e + 1] = bfhi(xe[e]); }
        u32 pa = (u32)(L_W2 + tok * 272 + dg * 32), pb = pa + 2 * W2_REG, sa = (u32)(L_UC + dg * 1024 + (((tok >> 3) ^ dg) << 4) + (tok & 7) * 2), oo = xo;
        asm volatile("" : "+v"(m[0]), "+v"(m[1]), "+v"(m[2]), "+v"(m[3]), "+v"(m[4]), "+v"(m[5]), "+v"(m[6]), "+v"(m[7]), "+v"(pa), "+v"(pb), "+v"(sa), "+v"(oo));
        asm volatile("" : "+v"(xf[0]), "+v"(xf[1]), "+v"(xf[2]), "+v"(xf[3]), "+v"(xf[4]), "+v"(xf[5]), "+v"(xf[6]), "+v"(xf[7]));
        BAR();

#pragma unroll
        for (int i = 0; i < 4; i++) {
            LAS char* q = sm + (i < 2 ? pa : pb) + (i & 1) * W2_REG;
            const f32x4 lo = *(LAS f32x4*)q, hi = *(LAS f32x4*)(q + 16);
#pragma unroll
            for (int e = 0; e < 4; e++) { m[e] += lo[e]; m[4 + e] += hi[e]; }
        }
        float o[8];
#pragma unroll
        for (int e = 0; e < 8; e++) {
            const float s = bf2f(*(LAS u16*)(sm + sa + e * 128));
            o[e] = fmaf(s * m[e], __builtin_amdgcn_rcpf(1.f + __builtin_amdgcn_exp2f(-1.4426950408889634f * m[e])), xf[e]);
        }
        u32x4 ov = {pkbf(o[0], o[1]), pkbf(o[2], o[3]), pkbf(o[4], o[5]), pkbf(o[6], o[7])};
        astore_b128_sc1(p.out, oo, ov);
    }

}

extern "C" void kernel_launch(void* const* d_in, const int* in_sizes, int n_in, void* d_out, int out_size, void* d_ws, size_t ws_size,
                              hipStream_t stream) {
    Params p;
    p.x = (const u16*)d_in[0]; p.Are = (const u16*)d_in[1]; p.Aim = (const u16*)d_in[2]; p.Bre = (const u16*)d_in[3]; p.Bim = (const u16*)d_in[4];
    p.Cre = (const u16*)d_in[5]; p.Cim = (const u16*)d_in[6]; p.logdt = (const u16*)d_in[7]; p.Dskip = (const u16*)d_in[8];
    p.w1 = (const u16*)d_in[10]; p.b1 = (const u16*)d_in[11]; p.w2 = (const u16*)d_in[12]; p.b2 = (const u16*)d_in[13];
    p.out = (u16*)d_out; p.ws = (char*)d_ws;
    fused_kernel<<<256, 512, 0, stream>>>(p);
}
```
